```python
import torch

def _softmax(x, axis=-1):
    return torch.softmax(x, dim=axis)

def _gelu(x):
    return torch.nn.functional.gelu(x, approximate='tanh')

def _silu(x):
    return torch.nn.functional.silu(x)

def _sigmoid(x):
    return torch.sigmoid(x)

def _layernorm(x, axis=-1, eps=1e-05):
    m = x.mean(dim=axis, keepdim=True)
    v = x.var(dim=axis, keepdim=True, unbiased=False)
    return (x - m) / torch.sqrt(v + eps)

def _scatter_add(tgt, idx, src, axis=0):
    return tgt.clone().index_add_(axis, idx, src)

def _segment_sum(src, seg, n):
    out = torch.zeros((n,) + src.shape[1:], dtype=src.dtype)
    return out.index_add_(0, seg, src)

def _pad(x, pw):
    flat = tuple((v for pair in reversed(pw) for v in pair))
    return torch.nn.functional.pad(x, flat)
kernel_name = 'adaLN_cond__compressed_kv_cla__geglu_ffn__prompt'

def setup_inputs():

    _w = lambda a: (lambda t: t.to(torch.bfloat16) if t.is_floating_point() else t)(torch.as_tensor(a))

    def _A_setup():
        B, H, Sq, Sk, D, M = (1, 32, 2048, 2048, 128, 512)
        Dc = 4 * D
        return {'x': _w((lambda *s: torch.randn(*s, dtype=torch.bfloat16) * 0.05)(B, Sq, M)), 'c_kv': _w((lambda *s: torch.randn(*s, dtype=torch.bfloat16) * 1.6)(B, Sk, Dc)), 'w_q': _w((lambda *s: torch.randn(*s, dtype=torch.bfloat16) * 0.8)(M, H, D) * M ** (-0.5)), 'w_uk': _w((lambda *s: torch.randn(*s, dtype=torch.bfloat16) * 0.8)(Dc, H, D) * Dc ** (-0.5)), 'w_uv': _w((lambda *s: torch.randn(*s, dtype=torch.bfloat16) * 1.6)(Dc, H, D) * Dc ** (-0.5)), 'w_o': _w((lambda *s: torch.randn(*s, dtype=torch.bfloat16) * 1.6)(H, D, M) * (H * D) ** (-0.5))}
    _A_in = _A_setup()

    def _B_setup():
        B, S, D, F = (1, 2048, 512, 1792)
        return {'x': _w((lambda *s: torch.randn(*s, dtype=torch.bfloat16) * 0.05)(B, S, D)), 'w_gate': _w((lambda *s: torch.randn(*s, dtype=torch.bfloat16) * 0.05)(D, F)), 'w_up': _w((lambda *s: torch.randn(*s, dtype=torch.bfloat16) * 0.05)(D, F)), 'w_down': _w((lambda *s: torch.randn(*s, dtype=torch.bfloat16) * 0.05)(F, D))}
    _B_in = _B_setup()
    return {'A_x': _A_in['x'], 'A_c_kv': _A_in['c_kv'], 'A_w_q': _A_in['w_q'], 'A_w_uk': _A_in['w_uk'], 'A_w_uv': _A_in['w_uv'], 'A_w_o': _A_in['w_o'], 'B_x': _B_in['x'], 'B_w_gate': _B_in['w_gate'], 'B_w_up': _B_in['w_up'], 'B_w_down': _B_in['w_down'], 'c': _w((lambda *s: torch.randn(*s, dtype=torch.bfloat16) * 0.1)(1, 512) * 8.0), 'w_mod': _w((lambda *s: torch.randn(*s, dtype=torch.bfloat16) * 0.1)(512, 6 * 512) * 512 ** (-0.5) * 8.0)}

def reference(A_x, A_c_kv, A_w_q, A_w_uk, A_w_uv, A_w_o, B_x, B_w_gate, B_w_up, B_w_down, c, w_mod):

    def _A_ref(x, c_kv, w_q, w_uk, w_uv, w_o):
        B, Sq, M = (x.shape[0], x.shape[1], x.shape[2])
        Sk = c_kv.shape[1]
        H, D = (w_q.shape[1], w_q.shape[2])
        q = torch.einsum('bqm,mhd->bhqd', x, w_q)
        k = torch.einsum('bkc,chd->bhkd', c_kv, w_uk)
        v = torch.einsum('bkc,chd->bhkd', c_kv, w_uv)
        scores = torch.einsum('bhqd,bhkd->bhqk', q, k) * D ** (-0.5)
        off = Sk - Sq
        qi = torch.arange(Sq)[:, None]
        kj = torch.arange(Sk)[None, :]
        cmask = kj <= qi + off
        scores = torch.where(cmask[None, None], scores, -1000000000.0)
        p = _softmax(scores, axis=-1)
        ctx = torch.einsum('bhqk,bhkd->bhqd', p, v)
        return torch.einsum('bhqd,hdm->bqm', ctx, w_o)

    def _B_ref(x, w_gate, w_up, w_down):
        return _gelu(x @ w_gate) * (x @ w_up) @ w_down
    x = A_x
    mod = (_silu(c) @ w_mod).reshape(c.shape[0], 6, 1, -1)
    sa, ca, ga, sb, cb, gb = (mod[:, 0].clone(), mod[:, 1].clone(), mod[:, 2].clone(), mod[:, 3].clone(), mod[:, 4].clone(), mod[:, 5].clone())
    h = _layernorm(x, axis=-1) * (1 + ca) + sa
    x = x + ga * _A_ref(h, A_c_kv, A_w_q, A_w_uk, A_w_uv, A_w_o)
    h = _layernorm(x, axis=-1) * (1 + cb) + sb
    x = x + gb * _B_ref(h, B_w_gate, B_w_up, B_w_down)
    return x
```

```cpp
#include <hip/hip_runtime.h>
#include <hip/hip_bf16.h>
#include <stdint.h>
typedef unsigned short u16;
__device__ __forceinline__ float bf2f(u16 v) { return __uint_as_float(((unsigned)v) << 16); }
__device__ __forceinline__ u16 f2bf(float f) { __bf16 b = (__bf16)f; return __builtin_bit_cast(u16, b); }
__device__ __forceinline__ float rbf(float f) { return bf2f(f2bf(f)); }

constexpr int S = 2048, M = 512, H = 32, D = 128, HD = 4096, F = 1792;

__global__ void n_mod(const u16* c, const u16* wmod, float* mod) {
  int n = blockIdx.x * 256 + threadIdx.x;
  float acc = 0;
  for (int k = 0; k < 512; ++k) { float x = bf2f(c[k]); float s = rbf(x / (1.f + __expf(-x))); acc += s * bf2f(wmod[k * 3072 + n]); }
  mod[n] = rbf(acc);
}
__global__ void n_ln(const float* x, const float* sh, const float* sc, u16* h) {
  int r = blockIdx.x; __shared__ float red[256];
  float v0 = x[r * 512 + threadIdx.x], v1 = x[r * 512 + 256 + threadIdx.x];
  red[threadIdx.x] = v0 + v1; __syncthreads();
  for (int s = 128; s > 0; s >>= 1) { if (threadIdx.x < s) red[threadIdx.x] += red[threadIdx.x + s]; __syncthreads(); }
  float mean = red[0] / 512.f; __syncthreads();
  float d0 = v0 - mean, d1 = v1 - mean;
  red[threadIdx.x] = d0 * d0 + d1 * d1; __syncthreads();
  for (int s = 128; s > 0; s >>= 1) { if (threadIdx.x < s) red[threadIdx.x] += red[threadIdx.x + s]; __syncthreads(); }
  float rstd = rsqrtf(red[0] / 512.f + 1e-5f);
  int c0 = threadIdx.x, c1 = threadIdx.x + 256;
  h[r * 512 + c0] = f2bf(rbf(d0 * rstd) * rbf(1.f + sc[c0]) + sh[c0]);
  h[r * 512 + c1] = f2bf(rbf(d1 * rstd) * rbf(1.f + sc[c1]) + sh[c1]);
}
__global__ void n_cvt(const u16* x, float* y, int n) { int i = blockIdx.x * 256 + threadIdx.x; if (i < n) y[i] = bf2f(x[i]); }
__global__ void n_gemm(const u16* A, const u16* B, u16* C, int Mm, int N, int K) {
  __shared__ float As[16][17], Bs[16][17];
  int tx = threadIdx.x & 15, ty = threadIdx.x >> 4;
  int row = blockIdx.y * 16 + ty, col = blockIdx.x * 16 + tx;
  float acc = 0;
  for (int k0 = 0; k0 < K; k0 += 16) {
    As[ty][tx] = bf2f(A[(long)row * K + k0 + tx]); Bs[ty][tx] = bf2f(B[(long)(k0 + ty) * N + col]);
    __syncthreads();
    for (int k = 0; k < 16; ++k) acc += As[ty][k] * Bs[k][tx];
    __syncthreads();
  }
  C[(long)row * N + col] = f2bf(acc);
}
__global__ void n_attn(const u16* q, const u16* k, const u16* v, u16* ctx) {
  int r = blockIdx.x, h = blockIdx.y, t = threadIdx.x;
  __shared__ float qs[128]; __shared__ float p[2048]; __shared__ float red[128];
  qs[t] = bf2f(q[(long)r * HD + h * D + t]); __syncthreads();
  float mx = -1e30f;
  for (int j = t; j <= r; j += 128) { float s = 0; const u16* kr = k + (long)j * HD + h * D; for (int d = 0; d < 128; ++d) s += qs[d] * bf2f(kr[d]); s *= 0.08838834764831845f; p[j] = s; mx = fmaxf(mx, s); }
  red[t] = mx; __syncthreads();
  for (int s = 64; s > 0; s >>= 1) { if (t < s) red[t] = fmaxf(red[t], red[t + s]); __syncthreads(); }
  mx = red[0]; __syncthreads();
  float sum = 0;
  for (int j = t; j <= r; j += 128) { float e = __expf(p[j] - mx); p[j] = e; sum += e; }
  red[t] = sum; __syncthreads();
  for (int s = 64; s > 0; s >>= 1) { if (t < s) red[t] += red[t + s]; __syncthreads(); }
  sum = red[0];
  float acc = 0;
  for (int j = 0; j <= r; ++j) acc += p[j] * bf2f(v[(long)j * HD + h * D + t]);
  ctx[(long)r * HD + h * D + t] = f2bf(acc / sum);
}
__global__ void n_res(const float* x, const float* g, const u16* y, float* o, u16* ob) {
  int i = blockIdx.x * 256 + threadIdx.x; float r = rbf(x[i] + rbf(g[i & 511] * bf2f(y[i]))); o[i] = r; if (ob) ob[i] = f2bf(r);
}
__global__ void n_geglu(const u16* g, const u16* u, u16* a, int n) {
  int i = blockIdx.x * 256 + threadIdx.x; if (i >= n) return;
  float x = bf2f(g[i]); float t = tanhf(0.7978845608028654f * (x + 0.044715f * x * x * x)); float ge = rbf(0.5f * x * (1.f + t));
  a[i] = f2bf(ge * bf2f(u[i]));
}

extern "C" void kernel_launch(void* const* d_in, const int* in_sizes, int n_in, void* d_out, int out_size, void* d_ws, size_t ws_size, hipStream_t stream) {
  const u16* x = (const u16*)d_in[0]; const u16* ckv = (const u16*)d_in[1]; const u16* wq = (const u16*)d_in[2]; const u16* wuk = (const u16*)d_in[3];
  const u16* wuv = (const u16*)d_in[4]; const u16* wo = (const u16*)d_in[5]; const u16* wg = (const u16*)d_in[7]; const u16* wu = (const u16*)d_in[8];
  const u16* wd = (const u16*)d_in[9]; const u16* c = (const u16*)d_in[10]; const u16* wmod = (const u16*)d_in[11];
  char* ws = (char*)d_ws; size_t o = 0;
  auto alloc = [&](size_t b) { void* p = ws + o; o += (b + 255) / 256 * 256; return p; };
  float* mod = (float*)alloc(3072 * 4); float* xf = (float*)alloc(S * M * 4); float* x1 = (float*)alloc(S * M * 4); float* x2 = (float*)alloc(S * M * 4);
  u16* h = (u16*)alloc(S * M * 2); u16* q = (u16*)alloc((size_t)S * HD * 2); u16* k = (u16*)alloc((size_t)S * HD * 2); u16* v = (u16*)alloc((size_t)S * HD * 2);
  u16* ctx = (u16*)alloc((size_t)S * HD * 2); u16* ao = (u16*)alloc(S * M * 2); u16* g = (u16*)alloc(S * F * 2); u16* u = (u16*)alloc(S * F * 2); u16* a = (u16*)alloc(S * F * 2);
  n_mod<<<12, 256, 0, stream>>>(c, wmod, mod);
  n_cvt<<<S * M / 256, 256, 0, stream>>>(x, xf, S * M);
  n_ln<<<S, 256, 0, stream>>>(xf, mod, mod + 512, h);
  n_gemm<<<dim3(HD / 16, S / 16), 256, 0, stream>>>(h, wq, q, S, HD, M);
  n_gemm<<<dim3(HD / 16, S / 16), 256, 0, stream>>>(ckv, wuk, k, S, HD, M);
  n_gemm<<<dim3(HD / 16, S / 16), 256, 0, stream>>>(ckv, wuv, v, S, HD, M);
  n_attn<<<dim3(S, H), 128, 0, stream>>>(q, k, v, ctx);
  n_gemm<<<dim3(M / 16, S / 16), 256, 0, stream>>>(ctx, wo, ao, S, M, HD);
  n_res<<<S * M / 256, 256, 0, stream>>>(xf, mod + 1024, ao, x1, nullptr);
  n_ln<<<S, 256, 0, stream>>>(x1, mod + 1536, mod + 2048, h);
  n_gemm<<<dim3(F / 16, S / 16), 256, 0, stream>>>(h, wg, g, S, F, M);
  n_gemm<<<dim3(F / 16, S / 16), 256, 0, stream>>>(h, wu, u, S, F, M);
  n_geglu<<<S * F / 256, 256, 0, stream>>>(g, u, a, S * F);
  n_gemm<<<dim3(M / 16, S / 16), 256, 0, stream>>>(a, wd, ao, S, M, F);
  n_res<<<S * M / 256, 256, 0, stream>>>(x1, mod + 2560, ao, x2, (u16*)d_out);
}
```

```cpp
#include <hip/hip_runtime.h>
#include <hip/hip_bf16.h>
#include <stdint.h>

typedef unsigned short u16;
typedef __bf16 bf16x8 __attribute__((ext_vector_type(8)));
typedef __bf16 bf16x2 __attribute__((ext_vector_type(2)));
typedef short s16x4 __attribute__((ext_vector_type(4)));
typedef float f32x4 __attribute__((ext_vector_type(4)));
typedef float f32x2 __attribute__((ext_vector_type(2)));
typedef unsigned u32x4 __attribute__((ext_vector_type(4)));
typedef unsigned u32x2 __attribute__((ext_vector_type(2)));
#define DI __device__ __forceinline__
#define LDSP(T) __attribute__((address_space(3))) T*

constexpr int SEQ = 2048, DM = 512, NH = 32, HDIM = 128, HD = 4096, FF = 1792;

constexpr size_t WS_MOD = 0;
constexpr size_t WS_SIL = 12288;
constexpr size_t WS_FLG = 16384;
constexpr size_t WS_XN  = 65536;
constexpr size_t WS_KF  = WS_XN + 2097152;
constexpr size_t WS_VF  = WS_KF + 16777216;
constexpr size_t WS_CT  = WS_VF + 16777216;
constexpr size_t WS_PART = WS_CT + 16777216;
constexpr size_t WS_X1  = WS_PART + 16777216;
constexpr size_t WS_H2  = WS_X1 + 4194304;
constexpr size_t WS_ACT = WS_H2 + 2097152;
constexpr size_t WS_CF  = WS_ACT + 7340032;
constexpr size_t WS_WDF = WS_CF + 2097152;
constexpr size_t WS_WGU = WS_WDF + 1835008;
constexpr size_t WS_PT2 = WS_WGU + 3670016;
constexpr size_t WS_END = WS_PT2 + 14680064;

DI float bf2f(u16 v) { return __uint_as_float(((unsigned)v) << 16); }
DI float bflo(unsigned w) { return __uint_as_float(w << 16); }
DI float bfhi(unsigned w) { return __uint_as_float(w & 0xffff0000u); }
DI unsigned cvtpk(float lo, float hi) { f32x2 v = {lo, hi}; bf16x2 b = __builtin_convertvector(v, bf16x2); return __builtin_bit_cast(unsigned, b); }
DI float rbf(float f) { return bflo(cvtpk(f, f)); }
DI unsigned rfl(unsigned x) { return __builtin_amdgcn_readfirstlane(x); }
DI __amdgpu_buffer_rsrc_t mk_rsrc(const void* p, unsigned bytes) { return __builtin_amdgcn_make_buffer_rsrc((void*)p, (short)0, (int)bytes, 0x00020000); }
DI void dma16(__amdgpu_buffer_rsrc_t srd, unsigned voff, unsigned soff, unsigned ldsaddr) {
  asm volatile("s_mov_b32 m0, %0\n\ts_nop 0\n\tbuffer_load_dwordx4 %1, %2, %3 offen lds" :: "s"(ldsaddr), "v"(voff), "s"(srd), "s"(soff) : "memory");
}
#define STM ""
DI void bstore16(u32x4 v, __amdgpu_buffer_rsrc_t srd, unsigned voff, unsigned soff) {
  asm volatile("buffer_store_dwordx4 %0, %1, %2, %3 offen " STM "\n\ts_nop 1" :: "v"(v), "v"(voff), "s"(srd), "s"(soff) : "memory");
}
DI void gstore16(void* p, u32x4 v) { asm volatile("global_store_dwordx4 %0, %1, off " STM "\n\ts_nop 1" :: "v"(p), "v"(v) : "memory"); }
#define WAIT_V(n) asm volatile("s_waitcnt vmcnt(" #n ")" ::: "memory")
#define WAIT_L(n) asm volatile("s_waitcnt lgkmcnt(" #n ")" ::: "memory")
#define BAR() __builtin_amdgcn_s_barrier()
#define SBAR() __builtin_amdgcn_sched_barrier(0)
DI bf16x8 lds_b128(unsigned addr) { return *(const LDSP(bf16x8))(uintptr_t)addr; }
DI bf16x8 lds_tr2(unsigned a0, unsigned a1) {
  s16x4 lo = __builtin_amdgcn_ds_read_tr16_b64_v4i16((LDSP(s16x4))(uintptr_t)a0);
  s16x4 hi = __builtin_amdgcn_ds_read_tr16_b64_v4i16((LDSP(s16x4))(uintptr_t)a1);
  return __builtin_bit_cast(bf16x8, __builtin_shufflevector(lo, hi, 0, 1, 2, 3, 4, 5, 6, 7));
}
#define MFMA16(a, b, c) __builtin_amdgcn_mfma_f32_16x16x32_bf16((a), (b), (c), 0, 0, 0)
DI u32x4 pack2(const f32x4& a, const f32x4& b) { u32x4 w = {cvtpk(a[0], a[1]), cvtpk(a[2], a[3]), cvtpk(b[0], b[1]), cvtpk(b[2], b[3])}; return w; }
#define ST(i)
#define RT(k, i)
#define RTW(k, i)
DI float wave_sum(float v) {
#pragma unroll
  for (int o = 32; o > 0; o >>= 1) v += __shfl_xor(v, o);
  return v;
}

#undef ST
#define ST(i)
#define TM_DECL
#define TM(i)
#define TM_OUT
constexpr int K2_LDS = 163840;
constexpr unsigned L_K = 0, L_V = 65536, L_Q = 131072;
#define ACLOB "a0", "a1", "a2", "a3", "a4", "a5", "a6", "a7", "a8", "a9", "a10", "a11", "a12", "a13", "a14", "a15", "a16", "a17", "a18", "a19", "a20", "a21", "a22", "a23", "a24", "a25", "a26", "a27", "a28", "a29", "a30", "a31", "a32", "a33", "a34", "a35", "a36", "a37", "a38", "a39", "a40", "a41", "a42", "a43", "a44", "a45", "a46", "a47", "a48", "a49", "a50", "a51", "a52", "a53", "a54", "a55", "a56", "a57", "a58", "a59", "a60", "a61", "a62", "a63", "a64", "a65", "a66", "a67", "a68", "a69", "a70", "a71", "a72", "a73", "a74", "a75", "a76", "a77", "a78", "a79", "a80", "a81", "a82", "a83", "a84", "a85", "a86", "a87", "a88", "a89", "a90", "a91", "a92", "a93", "a94", "a95", "a96", "a97", "a98", "a99", "a100", "a101", "a102", "a103"
#define ACLOBQ "a0", "a1", "a2", "a3", "a4", "a5", "a6", "a7", "a8", "a9", "a10", "a11", "a12", "a13", "a14", "a15", "a16", "a17", "a18", "a19", "a20", "a21", "a22", "a23", "a24", "a25", "a26", "a27", "a28", "a29", "a30", "a31", "a32", "a33", "a34", "a35", "a36", "a37", "a38", "a39", "a40", "a41", "a42", "a43", "a44", "a45", "a46", "a47", "a48", "a49", "a50", "a51", "a52", "a53", "a54", "a55", "a56", "a57", "a58", "a59", "a60", "a61", "a62", "a63", "a64", "a65", "a66", "a67", "a68", "a69", "a70", "a71", "a72", "a73", "a74", "a75", "a76", "a77", "a78", "a79", "a80", "a81", "a82", "a83", "a84", "a85", "a86", "a87", "a88", "a89", "a90", "a91", "a92", "a93", "a94", "a95", "a96", "a97", "a98", "a99", "a100", "a101", "a102", "a103", "a104", "a105", "a106", "a107", "a108", "a109", "a110", "a111", "a112", "a113", "a114", "a115", "a116", "a117", "a118", "a119", "a120", "a121", "a122", "a123", "a124", "a125", "a126", "a127"
#define UNLIKELY(x) __builtin_expect(!!(x), 0)
#define LIKELY(x) __builtin_expect(!!(x), 1)
DI u32x4 gload16(const void* p) { u32x4 v; asm volatile("global_load_dwordx4 %0, %1, off" : "=v"(v) : "v"(p) : "memory"); return v; }
#define WAIT_V4(n, a, b, c, d) asm volatile("s_waitcnt vmcnt(" #n ")" : "+v"(a), "+v"(b), "+v"(c), "+v"(d) :: "memory")
DI void lds_w128(unsigned addr, u32x4 v) { *(LDSP(u32x4))(uintptr_t)addr = v; }
DI f32x4 lds_f4(unsigned addr) { return *(const LDSP(f32x4))(uintptr_t)addr; }
DI bf16x8 exp_pack(const f32x4& a, const f32x4& b) {
  f32x4 e0, e1;
#pragma unroll
  for (int e = 0; e < 4; ++e) { e0[e] = __builtin_amdgcn_exp2f(a[e]); e1[e] = __builtin_amdgcn_exp2f(b[e]); }
  return __builtin_bit_cast(bf16x8, pack2(e0, e1));
}
template <int OFF> DI void dma2(__amdgpu_buffer_rsrc_t srd, unsigned voff, unsigned soff, unsigned ldsaddr) {
  asm volatile("s_mov_b32 m0, %0\n\ts_nop 0\n\tbuffer_load_dwordx4 %1, %2, %3 offen offset:%4 sc1 lds\n\tbuffer_load_dwordx4 %1, %2, %3 offen offset:%5 sc1 lds"
               :: "s"(ldsaddr), "v"(voff), "s"(srd), "s"(soff), "n"(OFF), "n"(OFF + 1024) : "memory");
}

constexpr unsigned F_S3 = 48, F_S4 = 64, F_S6 = 96;
constexpr unsigned F_K = 264;
constexpr unsigned F_WT = 256;
constexpr unsigned F_CB = 128, F_CA = 192;
typedef unsigned u32x32 __attribute__((ext_vector_type(32)));
typedef unsigned u32x16 __attribute__((ext_vector_type(16)));
DI void sstore16(u32x4 v, __amdgpu_buffer_rsrc_t srd, unsigned voff, unsigned soff) {
  asm volatile("buffer_store_dwordx4 %0, %1, %2, %3 offen sc1\n\ts_nop 1" :: "v"(v), "v"(voff), "s"(srd), "s"(soff) : "memory");
}
DI void count_up(__amdgpu_buffer_rsrc_t srd, unsigned f, bool w) {
  if (w) asm volatile("s_mov_b64 exec, 1\n\tbuffer_atomic_add %0, off, %1, %2\n\ts_mov_b64 exec, -1" :: "v"(1u), "s"(srd), "s"((unsigned)WS_FLG + f * 128u) : "memory");
}
DI void await(__amdgpu_buffer_rsrc_t srd, unsigned f, unsigned n, unsigned wid) {
  if (wid == 0) {
    unsigned pv;
    do {
      asm volatile("buffer_load_dword %0, off, %1, %2 sc1\n\ts_waitcnt vmcnt(0)" : "=v"(pv) : "s"(srd), "s"((unsigned)WS_FLG + f * 128u) : "memory");
    } while (rfl(pv) < n);
  }
  BAR();
}
DI float red_hi(float v) {
  v += __builtin_bit_cast(float, __builtin_amdgcn_update_dpp(0, __builtin_bit_cast(int, v), 0x128, 0xf, 0xf, false));
  auto a = __builtin_amdgcn_permlane16_swap(__float_as_uint(v), __float_as_uint(v), false, false);
  v = __uint_as_float(a[0]) + __uint_as_float(a[1]);
  auto c = __builtin_amdgcn_permlane32_swap(__float_as_uint(v), __float_as_uint(v), false, false);
  return __uint_as_float(c[0]) + __uint_as_float(c[1]);
}
template <int C> DI float dpp_add(float v) { return v + __builtin_bit_cast(float, __builtin_amdgcn_update_dpp(0, __builtin_bit_cast(int, v), C, 0xf, 0xf, false)); }
DI float red_row(float v) { return dpp_add<0x121>(dpp_add<0x122>(dpp_add<0x124>(dpp_add<0x128>(v)))); }
DI float red_all(float v) { return red_hi(dpp_add<0x121>(dpp_add<0x122>(dpp_add<0x124>(v)))); }
DI f32x2 up2(unsigned w) { return f32x2{bflo(w), bfhi(w)}; }
DI f32x2 rbf2(f32x2 v) { return up2(cvtpk(v[0], v[1])); }
#define TS(i)

DI unsigned long long code_range(unsigned& last) {
  unsigned long long pc; unsigned rel;
  asm volatile("s_getpc_b64 %0\n.Lct%=:\n\ts_mov_b32 %1, .Lct%=-.Lk1s\n\ts_mov_b32 %2, .Lk1e-.Lk1s-4" : "=s"(pc), "=s"(rel), "=s"(last));
  return pc - rel;
}
__global__ __launch_bounds__(512, 2) void k1_attn(const u16* __restrict__ x, const u16* __restrict__ wuk, const u16* __restrict__ wuv, const u16* __restrict__ wq,
                                                  const u16* __restrict__ wo, const u16* __restrict__ wg, const u16* __restrict__ wu, const u16* __restrict__ wd,
                                                  u16* __restrict__ out, char* __restrict__ ws) {
  asm volatile(".Lk1s:");
  extern __shared__ __attribute__((aligned(16))) char smem[];
  const unsigned lds0 = (unsigned)(uintptr_t)smem;
  const unsigned tid = threadIdx.x, lane = tid & 63u, wid = rfl(tid >> 6), grp = wid >> 2, w4 = wid & 3u;
  const unsigned g = lane >> 4;
  const int b = blockIdx.x, xcd = b & 7, idx = b >> 3;
  const int head = 4 * xcd + (idx >> 3), pi = idx & 7;
  const int nB = 2 * pi + 2, qbA = 15 - pi, qbB = pi;
  char* stampbuf = ws + WS_END;
  ST(0); RT(1, 0);
  bf16x8 Q[4][2];
  unsigned pollv;
  __amdgpu_buffer_rsrc_t srdS = mk_rsrc(ws, (unsigned)WS_END);
  const unsigned vL = lane * 16u;
  {
    __amdgpu_buffer_rsrc_t srdWk = mk_rsrc(wuk, 512 * HD * 2), srdWv = mk_rsrc(wuv, 512 * HD * 2), srdWq = mk_rsrc(wq, 512 * HD * 2);
    const unsigned vW = ((lane >> 2) & 3u) * 8192u + (lane >> 4) * 64u + (((lane & 3u) ^ (wid & 2u)) * 16u);
    const unsigned tv = ((unsigned)idx * 8u + wid) * 8192u + (unsigned)xcd * 1024u + (lane & 7u) * 128u;
    const unsigned sW = (4u * wid) * 8192u + (unsigned)head * 256u;
    const unsigned dW = lds0 + wid * 1024u;
    const unsigned qb = (unsigned)(grp ? qbB : qbA);
    const unsigned sC = (unsigned)WS_CF + (16u * pi + 2u * wid) * 16384u, sH = (unsigned)WS_XN + (qb * 8u + w4 * 2u) * 16384u;
    const unsigned sO0 = (unsigned)WS_KF + (unsigned)head * 524288u + (16u * pi + 2u * wid) * 4096u, sO1 = (unsigned)WS_VF + (unsigned)head * 524288u + (8u * pi + wid) * 8192u;
    const unsigned fH = (unsigned)WS_FLG + (unsigned)(b >> 4) * 128u, pH = (unsigned)WS_FLG + qb * 128u, fKV = (unsigned)WS_FLG + 2048u + (unsigned)head * 128u;
    unsigned wB0 = lds0 + g * 2048u + ((lane >> 2) & 3u) * 64u + (g & 1u) * 32u + (lane & 3u) * 8u, wB1 = wB0 ^ 32u;
    u32x32 R1; unsigned t0, t1, t2;
    const unsigned sKV = (unsigned)WS_KF + (unsigned)head * 524288u + wid * 2048u, dKV = lds0 + wid * 2048u;
    const unsigned row = 8u * b + wid;
    const char* pX = (const char*)x + row * 1024u;
    unsigned clast; const unsigned long long cbase = code_range(clast);
    const unsigned coff = (unsigned)idx * 2048u + (lane & 15u) * 128u, vK = coff < clast ? coff : clast;
    asm volatile(
      "s_add_u32 %[t0], %[sw], 0\n\t"
      "s_add_u32 m0, %[dw], 0\n\t"
      "s_nop 0\n\t"
      "buffer_load_dwordx4 %[vw], %[rw0], %[t0] offen lds\n\t"
      "s_add_u32 %[t0], %[sw], 262144\n\t"
      "s_add_u32 m0, %[dw], 8192\n\t"
      "s_nop 0\n\t"
      "buffer_load_dwordx4 %[vw], %[rw0], %[t0] offen lds\n\t"
      "s_add_u32 %[t1], %[sc], 0\n\t"
      "buffer_load_dwordx4 v[64:67], %[vh0], %[rs], %[t1] offen offset:0\n\t"
      "buffer_load_dwordx4 v[68:71], %[vh1], %[rs], %[t1] offen offset:0\n\t"
      "buffer_load_dwordx4 v[72:75], %[vh0], %[rs], %[t1] offen offset:1024\n\t"
      "buffer_load_dwordx4 v[76:79], %[vh1], %[rs], %[t1] offen offset:1024\n\t"
      : "=&{v[64:95]}"(R1), [t0] "=&s"(t0), [t1] "=&s"(t1)
      : [sw] "s"(sW), [dw] "s"(dW), [vw] "v"(vW), [rw0] "s"(srdWk), [sc] "s"(sC), [vh0] "v"(vL), [vh1] "v"(vL + 16384u), [rs] "s"(srdS)
      : "memory", "scc");
    SBAR();
    asm volatile(
      "s_cmp_lg_u32 %[wid], 7\n\t"
      "s_cbranch_scc1 .Lc%=\n\t"
      "global_load_dword %[pv], %[vk], %[cb]\n\t"
      ".Lc%=:\n\t"
      "s_barrier\n\t"
      "s_add_u32 %[t0], %[sw], 524288\n\t"
      "s_add_u32 m0, %[dw], 16384\n\t"
      "s_nop 0\n\t"
      "buffer_load_dwordx4 %[vw], %[rw0], %[t0] offen lds\n\t"
      "s_add_u32 %[t0], %[sw], 786432\n\t"
      "s_add_u32 m0, %[dw], 24576\n\t"
      "s_nop 0\n\t"
      "buffer_load_dwordx4 %[vw], %[rw0], %[t0] offen lds\n\t"
      "s_add_u32 %[t1], %[sc], 2048\n\t"
      "buffer_load_dwordx4 v[80:83], %[vh0], %[rs], %[t1] offen offset:0\n\t"
      "buffer_load_dwordx4 v[84:87], %[vh1], %[rs], %[t1] offen offset:0\n\t"
      "buffer_load_dwordx4 v[88:91], %[vh0], %[rs], %[t1] offen offset:1024\n\t"
      "buffer_load_dwordx4 v[92:95], %[vh1], %[rs], %[t1] offen offset:1024\n\t"
      : "+{v[64:95]}"(R1), [pv] "=&v"(pollv), [t0] "=&s"(t0), [t1] "=&s"(t1)
      : [sw] "s"(sW), [dw] "s"(dW), [vw] "v"(vW), [rw0] "s"(srdWk), [sc] "s"(sC), [vh0] "v"(vL), [vh1] "v"(vL + 16384u), [vk] "v"(vK), [cb] "s"(cbase), [rs] "s"(srdS), [wid] "s"(wid)
      : "memory", "scc");
    asm volatile(
      "s_waitcnt vmcnt(10)\n\t"
      "s_barrier\n\t"
      "ds_read_b64_tr_b16 v[32:33], %[w0] offset:0\n\t"
      "ds_read_b64_tr_b16 v[34:35], %[w0] offset:1024\n\t"
      "ds_read_b64_tr_b16 v[36:37], %[w1] offset:0\n\t"
      "ds_read_b64_tr_b16 v[38:39], %[w1] offset:1024\n\t"
      "ds_read_b64_tr_b16 v[40:41], %[w0] offset:256\n\t"
      "ds_read_b64_tr_b16 v[42:43], %[w0] offset:1280\n\t"
      "ds_read_b64_tr_b16 v[44:45], %[w1] offset:256\n\t"
      "ds_read_b64_tr_b16 v[46:47], %[w1] offset:1280\n\t"
      "ds_read_b64_tr_b16 v[48:49], %[w0] offset:512\n\t"
      "ds_read_b64_tr_b16 v[50:51], %[w0] offset:1536\n\t"
      "ds_read_b64_tr_b16 v[52:53], %[w1] offset:512\n\t"
      "ds_read_b64_tr_b16 v[54:55], %[w1] offset:1536\n\t"
      "s_waitcnt vmcnt(6)\n\t"
      "s_waitcnt lgkmcnt(10)\n\t"
      "v_mfma_f32_16x16x32_bf16 a[0:3], v[32:35], v[64:67], 0\n\t"
      "ds_read_b64_tr_b16 v[56:57], %[w0] offset:768\n\t"
      "v_mfma_f32_16x16x32_bf16 a[4:7], v[32:35], v[68:71], 0\n\t"
      "ds_read_b64_tr_b16 v[58:59], %[w0] offset:1792\n\t"
      "s_add_u32 %[t0], %[sw], 1048576\n\t"
      "s_add_u32 m0, %[dw], 32768\n\t"
      "s_nop 0\n\t"
      "buffer_load_dwordx4 %[vw], %[rw0], %[t0] offen lds\n\t"
      "s_waitcnt lgkmcnt(10)\n\t"
      "v_mfma_f32_16x16x32_bf16 a[8:11], v[36:39], v[64:67], 0\n\t"
      "ds_read_b64_tr_b16 v[60:61], %[w1] offset:768\n\t"
      "v_mfma_f32_16x16x32_bf16 a[12:15], v[36:39], v[68:71], 0\n\t"
      "ds_read_b64_tr_b16 v[62:63], %[w1] offset:1792\n\t"
      "s_add_u32 %[t0], %[sw], 1310720\n\t"
      "s_add_u32 m0, %[dw], 40960\n\t"
      "s_nop 0\n\t"
      "buffer_load_dwordx4 %[vw], %[rw0], %[t0] offen lds\n\t"
      "s_waitcnt lgkmcnt(10)\n\t"
      "v_mfma_f32_16x16x32_bf16 a[16:19], v[40:43], v[64:67], 0\n\t"
      "ds_read_b64_tr_b16 v[32:33], %[w0] offset:8192\n\t"
      "v_mfma_f32_16x16x32_bf16 a[20:23], v[40:43], v[68:71], 0\n\t"
      "ds_read_b64_tr_b16 v[34:35], %[w0] offset:9216\n\t"
      "s_add_u32 %[t1], %[sc], 4096\n\t"
      "buffer_load_dwordx4 v[96:99], %[vh0], %[rs], %[t1] offen offset:0\n\t"
      "s_waitcnt lgkmcnt(10)\n\t"
      "v_mfma_f32_16x16x32_bf16 a[24:27], v[44:47], v[64:67], 0\n\t"
      "ds_read_b64_tr_b16 v[36:37], %[w1] offset:8192\n\t"
      "v_mfma_f32_16x16x32_bf16 a[28:31], v[44:47], v[68:71], 0\n\t"
      "ds_read_b64_tr_b16 v[38:39], %[w1] offset:9216\n\t"
      "buffer_load_dwordx4 v[100:103], %[vh1], %[rs], %[t1] offen offset:0\n\t"
      "s_waitcnt lgkmcnt(10)\n\t"
      "v_mfma_f32_16x16x32_bf16 a[32:35], v[48:51], v[64:67], 0\n\t"
      "ds_read_b64_tr_b16 v[40:41], %[w0] offset:8448\n\t"
      "v_mfma_f32_16x16x32_bf16 a[36:39], v[48:51], v[68:71], 0\n\t"
      "ds_read_b64_tr_b16 v[42:43], %[w0] offset:9472\n\t"
      "buffer_load_dwordx4 v[104:107], %[vh0], %[rs], %[t1] offen offset:1024\n\t"
      "s_waitcnt lgkmcnt(10)\n\t"
      "v_mfma_f32_16x16x32_bf16 a[40:43], v[52:55], v[64:67], 0\n\t"
      "ds_read_b64_tr_b16 v[44:45], %[w1] offset:8448\n\t"
      "v_mfma_f32_16x16x32_bf16 a[44:47], v[52:55], v[68:71], 0\n\t"
      "ds_read_b64_tr_b16 v[46:47], %[w1] offset:9472\n\t"
      "buffer_load_dwordx4 v[108:111], %[vh1], %[rs], %[t1] offen offset:1024\n\t"
      "s_waitcnt lgkmcnt(10)\n\t"
      "v_mfma_f32_16x16x32_bf16 a[48:51], v[56:59], v[64:67], 0\n\t"
      "ds_read_b64_tr_b16 v[48:49], %[w0] offset:8704\n\t"
      "v_mfma_f32_16x16x32_bf16 a[52:55], v[56:59], v[68:71], 0\n\t"
      "ds_read_b64_tr_b16 v[50:51], %[w0] offset:9728\n\t"
      "v_lshlrev_b32 v31, 1, %[vh0]\n\t"
      "global_load_dwordx4 v[16:19], %[vh0], %[px]\n\t"
      "buffer_load_dwordx4 a[64:67], v31, %[rs], 0 offen offset:0\n\t"
      "buffer_load_dwordx4 a[68:71], v31, %[rs], 0 offen offset:16\n\t"
      "buffer_load_dwordx4 a[72:75], v31, %[rs], 0 offen offset:2048\n\t"
      "buffer_load_dwordx4 a[76:79], v31, %[rs], 0 offen offset:2064\n\t"
      "s_waitcnt lgkmcnt(10)\n\t"
      "v_mfma_f32_16x16x32_bf16 a[56:59], v[60:63], v[64:67], 0\n\t"
      "ds_read_b64_tr_b16 v[52:53], %[w1] offset:8704\n\t"
      "v_mfma_f32_16x16x32_bf16 a[60:63], v[60:63], v[68:71], 0\n\t"
      "ds_read_b64_tr_b16 v[54:55], %[w1] offset:9728\n\t"
      "s_waitcnt vmcnt(15)\n\t"
      "s_barrier\n\t"
      "s_waitcnt lgkmcnt(10)\n\t"
      "v_mfma_f32_16x16x32_bf16 a[0:3], v[32:35], v[72:75], a[0:3]\n\t"
      "ds_read_b64_tr_b16 v[56:57], %[w0] offset:8960\n\t"
      "v_mfma_f32_16x16x32_bf16 a[4:7], v[32:35], v[76:79], a[4:7]\n\t"
      "ds_read_b64_tr_b16 v[58:59], %[w0] offset:9984\n\t"
      "s_add_u32 %[t0], %[sw], 1572864\n\t"
      "s_add_u32 m0, %[dw], 49152\n\t"
      "s_nop 0\n\t"
      "buffer_load_dwordx4 %[vw], %[rw0], %[t0] offen lds\n\t"
      "s_waitcnt lgkmcnt(10)\n\t"
      "v_mfma_f32_16x16x32_bf16 a[8:11], v[36:39], v[72:75], a[8:11]\n\t"
      "ds_read_b64_tr_b16 v[60:61], %[w1] offset:8960\n\t"
      "v_mfma_f32_16x16x32_bf16 a[12:15], v[36:39], v[76:79], a[12:15]\n\t"
      "ds_read_b64_tr_b16 v[62:63], %[w1] offset:9984\n\t"
      "s_add_u32 %[t0], %[sw], 1835008\n\t"
      "s_add_u32 m0, %[dw], 57344\n\t"
      "s_nop 0\n\t"
      "buffer_load_dwordx4 %[vw], %[rw0], %[t0] offen lds\n\t"
      "s_waitcnt lgkmcnt(10)\n\t"
      "v_mfma_f32_16x16x32_bf16 a[16:19], v[40:43], v[72:75], a[16:19]\n\t"
      "ds_read_b64_tr_b16 v[32:33], %[w0] offset:16384\n\t"
      "v_mfma_f32_16x16x32_bf16 a[20:23], v[40:43], v[76:79], a[20:23]\n\t"
      "ds_read_b64_tr_b16 v[34:35], %[w0] offset:17408\n\t"
      "s_add_u32 %[t1], %[sc], 6144\n\t"
      "buffer_load_dwordx4 v[112:115], %[vh0], %[rs], %[t1] offen offset:0\n\t"
      "s_waitcnt lgkmcnt(10)\n\t"
      "v_mfma_f32_16x16x32_bf16 a[24:27], v[44:47], v[72:75], a[24:27]\n\t"
      "ds_read_b64_tr_b16 v[36:37], %[w1] offset:16384\n\t"
      "v_mfma_f32_16x16x32_bf16 a[28:31], v[44:47], v[76:79], a[28:31]\n\t"
      "ds_read_b64_tr_b16 v[38:39], %[w1] offset:17408\n\t"
      "buffer_load_dwordx4 v[116:119], %[vh1], %[rs], %[t1] offen offset:0\n\t"
      "s_waitcnt lgkmcnt(10)\n\t"
      "v_mfma_f32_16x16x32_bf16 a[32:35], v[48:51], v[72:75], a[32:35]\n\t"
      "ds_read_b64_tr_b16 v[40:41], %[w0] offset:16640\n\t"
      "v_mfma_f32_16x16x32_bf16 a[36:39], v[48:51], v[76:79], a[36:39]\n\t"
      "ds_read_b64_tr_b16 v[42:43], %[w0] offset:17664\n\t"
      "buffer_load_dwordx4 v[120:123], %[vh0], %[rs], %[t1] offen offset:1024\n\t"
      "s_waitcnt lgkmcnt(10)\n\t"
      "v_mfma_f32_16x16x32_bf16 a[40:43], v[52:55], v[72:75], a[40:43]\n\t"
      "ds_read_b64_tr_b16 v[44:45], %[w1] offset:16640\n\t"
      "v_mfma_f32_16x16x32_bf16 a[44:47], v[52:55], v[76:79], a[44:47]\n\t"
      "ds_read_b64_tr_b16 v[46:47], %[w1] offset:17664\n\t"
      "buffer_load_dwordx4 v[124:127], %[vh1], %[rs], %[t1] offen offset:1024\n\t"
      "s_waitcnt lgkmcnt(10)\n\t"
      "v_mfma_f32_16x16x32_bf16 a[48:51], v[56:59], v[72:75], a[48:51]\n\t"
      "ds_read_b64_tr_b16 v[48:49], %[w0] offset:16896\n\t"
      "v_mfma_f32_16x16x32_bf16 a[52:55], v[56:59], v[76:79], a[52:55]\n\t"
      "ds_read_b64_tr_b16 v[50:51], %[w0] offset:17920\n\t"
      "s_waitcnt lgkmcnt(10)\n\t"
      "v_mfma_f32_16x16x32_bf16 a[56:59], v[60:63], v[72:75], a[56:59]\n\t"
      "ds_read_b64_tr_b16 v[52:53], %[w1] offset:16896\n\t"
      "v_mfma_f32_16x16x32_bf16 a[60:63], v[60:63], v[76:79], a[60:63]\n\t"
      "ds_read_b64_tr_b16 v[54:55], %[w1] offset:17920\n\t"
      "s_waitcnt vmcnt(17)\n\t"
      "s_waitcnt lgkmcnt(10)\n\t"
      "v_mfma_f32_16x16x32_bf16 a[0:3], v[32:35], v[80:83], a[0:3]\n\t"
      "ds_read_b64_tr_b16 v[56:57], %[w0] offset:17152\n\t"
      "v_mfma_f32_16x16x32_bf16 a[4:7], v[32:35], v[84:87], a[4:7]\n\t"
      "ds_read_b64_tr_b16 v[58:59], %[w0] offset:18176\n\t"
      "s_waitcnt lgkmcnt(10)\n\t"
      "v_mfma_f32_16x16x32_bf16 a[8:11], v[36:39], v[80:83], a[8:11]\n\t"
      "ds_read_b64_tr_b16 v[60:61], %[w1] offset:17152\n\t"
      "v_mfma_f32_16x16x32_bf16 a[12:15], v[36:39], v[84:87], a[12:15]\n\t"
      "ds_read_b64_tr_b16 v[62:63], %[w1] offset:18176\n\t"
      "s_waitcnt lgkmcnt(10)\n\t"
      "v_mfma_f32_16x16x32_bf16 a[16:19], v[40:43], v[80:83], a[16:19]\n\t"
      "ds_read_b64_tr_b16 v[32:33], %[w0] offset:24576\n\t"
      "v_mfma_f32_16x16x32_bf16 a[20:23], v[40:43], v[84:87], a[20:23]\n\t"
      "ds_read_b64_tr_b16 v[34:35], %[w0] offset:25600\n\t"
      "s_waitcnt lgkmcnt(10)\n\t"
      "v_mfma_f32_16x16x32_bf16 a[24:27], v[44:47], v[80:83], a[24:27]\n\t"
      "ds_read_b64_tr_b16 v[36:37], %[w1] offset:24576\n\t"
      "v_mfma_f32_16x16x32_bf16 a[28:31], v[44:47], v[84:87], a[28:31]\n\t"
      "ds_read_b64_tr_b16 v[38:39], %[w1] offset:25600\n\t"
      "s_add_u32 %[t0], %[sw], 2097152\n\t"
      "s_add_u32 m0, %[dw], 65536\n\t"
      "s_nop 0\n\t"
      "buffer_load_dwordx4 %[vw], %[rw0], %[t0] offen lds\n\t"
      "s_waitcnt lgkmcnt(10)\n\t"
      "v_mfma_f32_16x16x32_bf16 a[32:35], v[48:51], v[80:83], a[32:35]\n\t"
      "ds_read_b64_tr_b16 v[40:41], %[w0] offset:24832\n\t"
      "v_mfma_f32_16x16x32_bf16 a[36:39], v[48:51], v[84:87], a[36:39]\n\t"
      "ds_read_b64_tr_b16 v[42:43], %[w0] offset:25856\n\t"
      "s_add_u32 %[t0], %[sw], 2359296\n\t"
      "s_add_u32 m0, %[dw], 73728\n\t"
      "s_nop 0\n\t"
      "buffer_load_dwordx4 %[vw], %[rw0], %[t0] offen lds\n\t"
      "s_waitcnt lgkmcnt(10)\n\t"
      "v_mfma_f32_16x16x32_bf16 a[40:43], v[52:55], v[80:83], a[40:43]\n\t"
      "ds_read_b64_tr_b16 v[44:45], %[w1] offset:24832\n\t"
      "v_mfma_f32_16x16x32_bf16 a[44:47], v[52:55], v[84:87], a[44:47]\n\t"
      "ds_read_b64_tr_b16 v[46:47], %[w1] offset:25856\n\t"
      "s_waitcnt lgkmcnt(10)\n\t"
      "v_mfma_f32_16x16x32_bf16 a[48:51], v[56:59], v[80:83], a[48:51]\n\t"
      "ds_read_b64_tr_b16 v[48:49], %[w0] offset:25088\n\t"
      "v_mfma_f32_16x16x32_bf16 a[52:55], v[56:59], v[84:87], a[52:55]\n\t"
      "ds_read_b64_tr_b16 v[50:51], %[w0] offset:26112\n\t"
      "s_waitcnt lgkmcnt(10)\n\t"
      "v_mfma_f32_16x16x32_bf16 a[56:59], v[60:63], v[80:83], a[56:59]\n\t"
      "ds_read_b64_tr_b16 v[52:53], %[w1] offset:25088\n\t"
      "v_mfma_f32_16x16x32_bf16 a[60:63], v[60:63], v[84:87], a[60:63]\n\t"
      "ds_read_b64_tr_b16 v[54:55], %[w1] offset:26112\n\t"
      "s_waitcnt vmcnt(17)\n\t"
      "s_barrier\n\t"
      "s_waitcnt lgkmcnt(10)\n\t"
      "v_mfma_f32_16x16x32_bf16 a[0:3], v[32:35], v[88:91], a[0:3]\n\t"
      "ds_read_b64_tr_b16 v[56:57], %[w0] offset:25344\n\t"
      "v_mfma_f32_16x16x32_bf16 a[4:7], v[32:35], v[92:95], a[4:7]\n\t"
      "ds_read_b64_tr_b16 v[58:59], %[w0] offset:26368\n\t"
      "s_waitcnt lgkmcnt(10)\n\t"
      "v_mfma_f32_16x16x32_bf16 a[8:11], v[36:39], v[88:91], a[8:11]\n\t"
      "ds_read_b64_tr_b16 v[60:61], %[w1] offset:25344\n\t"
      "v_mfma_f32_16x16x32_bf16 a[12:15], v[36:39], v[92:95], a[12:15]\n\t"
      "ds_read_b64_tr_b16 v[62:63], %[w1] offset:26368\n\t"
      "s_waitcnt lgkmcnt(10)\n\t"
      "v_mfma_f32_16x16x32_bf16 a[16:19], v[40:43], v[88:91], a[16:19]\n\t"
      "ds_read_b64_tr_b16 v[32:33], %[w0] offset:32768\n\t"
      "v_mfma_f32_16x16x32_bf16 a[20:23], v[40:43], v[92:95], a[20:23]\n\t"
      "ds_read_b64_tr_b16 v[34:35], %[w0] offset:33792\n\t"
      "s_waitcnt lgkmcnt(10)\n\t"
      "v_mfma_f32_16x16x32_bf16 a[24:27], v[44:47], v[88:91], a[24:27]\n\t"
      "ds_read_b64_tr_b16 v[36:37], %[w1] offset:32768\n\t"
      "v_mfma_f32_16x16x32_bf16 a[28:31], v[44:47], v[92:95], a[28:31]\n\t"
      "ds_read_b64_tr_b16 v[38:39], %[w1] offset:33792\n\t"
      "s_add_u32 %[t1], %[sc], 8192\n\t"
      "buffer_load_dwordx4 v[64:67], %[vh0], %[rs], %[t1] offen offset:0\n\t"
      "s_waitcnt lgkmcnt(10)\n\t"
      "v_mfma_f32_16x16x32_bf16 a[32:35], v[48:51], v[88:91], a[32:35]\n\t"
      "ds_read_b64_tr_b16 v[40:41], %[w0] offset:33024\n\t"
      "v_mfma_f32_16x16x32_bf16 a[36:39], v[48:51], v[92:95], a[36:39]\n\t"
      "ds_read_b64_tr_b16 v[42:43], %[w0] offset:34048\n\t"
      "buffer_load_dwordx4 v[68:71], %[vh1], %[rs], %[t1] offen offset:0\n\t"
      "s_waitcnt lgkmcnt(10)\n\t"
      "v_mfma_f32_16x16x32_bf16 a[40:43], v[52:55], v[88:91], a[40:43]\n\t"
      "ds_read_b64_tr_b16 v[44:45], %[w1] offset:33024\n\t"
      "v_mfma_f32_16x16x32_bf16 a[44:47], v[52:55], v[92:95], a[44:47]\n\t"
      "ds_read_b64_tr_b16 v[46:47], %[w1] offset:34048\n\t"
      "buffer_load_dwordx4 v[72:75], %[vh0], %[rs], %[t1] offen offset:1024\n\t"
      "s_waitcnt lgkmcnt(10)\n\t"
      "v_mfma_f32_16x16x32_bf16 a[48:51], v[56:59], v[88:91], a[48:51]\n\t"
      "ds_read_b64_tr_b16 v[48:49], %[w0] offset:33280\n\t"
      "v_mfma_f32_16x16x32_bf16 a[52:55], v[56:59], v[92:95], a[52:55]\n\t"
      "ds_read_b64_tr_b16 v[50:51], %[w0] offset:34304\n\t"
      "buffer_load_dwordx4 v[76:79], %[vh1], %[rs], %[t1] offen offset:1024\n\t"
      "s_waitcnt lgkmcnt(10)\n\t"
      "v_mfma_f32_16x16x32_bf16 a[56:59], v[60:63], v[88:91], a[56:59]\n\t"
      "ds_read_b64_tr_b16 v[52:53], %[w1] offset:33280\n\t"
      "v_mfma_f32_16x16x32_bf16 a[60:63], v[60:63], v[92:95], a[60:63]\n\t"
      "ds_read_b64_tr_b16 v[54:55], %[w1] offset:34304\n\t"
      "s_add_u32 %[t0], %[st], 0\n\t"
      "s_buffer_load_dword %[ts], %[rw2], %[t0]\n\t"
      "s_add_u32 %[t0], %[t0], 64\n\t"
      "s_buffer_load_dword %[ts], %[rw2], %[t0]\n\t"
      "s_waitcnt vmcnt(12)\n\t"
      "s_waitcnt lgkmcnt(10)\n\t"
      "v_mfma_f32_16x16x32_bf16 a[0:3], v[32:35], v[96:99], a[0:3]\n\t"
      "ds_read_b64_tr_b16 v[56:57], %[w0] offset:33536\n\t"
      "v_lshlrev_b32 v20, 16, v16\n\t"
      "v_and_b32 v21, 0xffff0000, v16\n\t"
      "v_mfma_f32_16x16x32_bf16 a[4:7], v[32:35], v[100:103], a[4:7]\n\t"
      "ds_read_b64_tr_b16 v[58:59], %[w0] offset:34560\n\t"
      "v_lshlrev_b32 v22, 16, v17\n\t"
      "v_and_b32 v23, 0xffff0000, v17\n\t"
      "s_waitcnt lgkmcnt(10)\n\t"
      "v_mfma_f32_16x16x32_bf16 a[8:11], v[36:39], v[96:99], a[8:11]\n\t"
      "ds_read_b64_tr_b16 v[60:61], %[w1] offset:33536\n\t"
      "v_lshlrev_b32 v24, 16, v18\n\t"
      "v_and_b32 v25, 0xffff0000, v18\n\t"
      "v_mfma_f32_16x16x32_bf16 a[12:15], v[36:39], v[100:103], a[12:15]\n\t"
      "ds_read_b64_tr_b16 v[62:63], %[w1] offset:34560\n\t"
      "v_lshlrev_b32 v26, 16, v19\n\t"
      "v_and_b32 v27, 0xffff0000, v19\n\t"
      "s_waitcnt lgkmcnt(10)\n\t"
      "v_mfma_f32_16x16x32_bf16 a[16:19], v[40:43], v[96:99], a[16:19]\n\t"
      "ds_read_b64_tr_b16 v[32:33], %[w0] offset:40960\n\t"
      "v_add_f32 v28, v20, v21\n\t"
      "v_add_f32 v29, v22, v23\n\t"
      "v_mfma_f32_16x16x32_bf16 a[20:23], v[40:43], v[100:103], a[20:23]\n\t"
      "ds_read_b64_tr_b16 v[34:35], %[w0] offset:41984\n\t"
      "v_add_f32 v16, v24, v25\n\t"
      "v_add_f32 v17, v26, v27\n\t"
      "s_waitcnt lgkmcnt(10)\n\t"
      "v_mfma_f32_16x16x32_bf16 a[24:27], v[44:47], v[96:99], a[24:27]\n\t"
      "ds_read_b64_tr_b16 v[36:37], %[w1] offset:40960\n\t"
      "v_add_f32 v28, v28, v29\n\t"
      "v_add_f32 v16, v16, v17\n\t"
      "v_mfma_f32_16x16x32_bf16 a[28:31], v[44:47], v[100:103], a[28:31]\n\t"
      "ds_read_b64_tr_b16 v[38:39], %[w1] offset:41984\n\t"
      "v_add_f32 v28, v28, v16\n\t"
      "s_add_u32 %[t0], %[sw], 2621440\n\t"
      "s_add_u32 m0, %[dw], 81920\n\t"
      "s_nop 0\n\t"
      "buffer_load_dwordx4 %[vw], %[rw0], %[t0] offen lds\n\t"
      "s_waitcnt lgkmcnt(10)\n\t"
      "v_mfma_f32_16x16x32_bf16 a[32:35], v[48:51], v[96:99], a[32:35]\n\t"
      "ds_read_b64_tr_b16 v[40:41], %[w0] offset:41216\n\t"
      "s_nop 0\n\tv_add_f32_dpp v28, v28, v28 row_ror:4 row_mask:0xf bank_mask:0xf\n\t"
      "v_mfma_f32_16x16x32_bf16 a[36:39], v[48:51], v[100:103], a[36:39]\n\t"
      "ds_read_b64_tr_b16 v[42:43], %[w0] offset:42240\n\t"
      "s_nop 0\n\tv_add_f32_dpp v28, v28, v28 row_ror:2 row_mask:0xf bank_mask:0xf\n\t"
      "s_add_u32 %[t0], %[sw], 2883584\n\t"
      "s_add_u32 m0, %[dw], 90112\n\t"
      "s_nop 0\n\t"
      "buffer_load_dwordx4 %[vw], %[rw0], %[t0] offen lds\n\t"
      "s_waitcnt lgkmcnt(10)\n\t"
      "v_mfma_f32_16x16x32_bf16 a[40:43], v[52:55], v[96:99], a[40:43]\n\t"
      "ds_read_b64_tr_b16 v[44:45], %[w1] offset:41216\n\t"
      "s_nop 0\n\tv_add_f32_dpp v28, v28, v28 row_ror:1 row_mask:0xf bank_mask:0xf\n\t"
      "v_mfma_f32_16x16x32_bf16 a[44:47], v[52:55], v[100:103], a[44:47]\n\t"
      "ds_read_b64_tr_b16 v[46:47], %[w1] offset:42240\n\t"
      "s_nop 0\n\tv_add_f32_dpp v28, v28, v28 row_ror:8 row_mask:0xf bank_mask:0xf\n\t"
      "s_add_u32 %[t0], %[st], 262144\n\t"
      "s_buffer_load_dword %[ts], %[rw2], %[t0]\n\t"
      "s_add_u32 %[t0], %[t0], 64\n\t"
      "s_buffer_load_dword %[ts], %[rw2], %[t0]\n\t"
      "s_waitcnt lgkmcnt(10)\n\t"
      "v_mfma_f32_16x16x32_bf16 a[48:51], v[56:59], v[96:99], a[48:51]\n\t"
      "ds_read_b64_tr_b16 v[48:49], %[w0] offset:41472\n\t"
      "v_mov_b32 v29, v28\n\t"
      "v_mfma_f32_16x16x32_bf16 a[52:55], v[56:59], v[100:103], a[52:55]\n\t"
      "ds_read_b64_tr_b16 v[50:51], %[w0] offset:42496\n\t"
      "s_nop 0\n\tv_permlane16_swap_b32 v28, v29\n\t"
      "s_waitcnt lgkmcnt(10)\n\t"
      "v_mfma_f32_16x16x32_bf16 a[56:59], v[60:63], v[96:99], a[56:59]\n\t"
      "ds_read_b64_tr_b16 v[52:53], %[w1] offset:41472\n\t"
      "v_add_f32 v28, v28, v29\n\t"
      "v_mfma_f32_16x16x32_bf16 a[60:63], v[60:63], v[100:103], a[60:63]\n\t"
      "ds_read_b64_tr_b16 v[54:55], %[w1] offset:42496\n\t"
      "v_mov_b32 v29, v28\n\t"
      "s_waitcnt vmcnt(12)\n\t"
      "s_barrier\n\t"
      "s_waitcnt lgkmcnt(10)\n\t"
      "v_mfma_f32_16x16x32_bf16 a[0:3], v[32:35], v[104:107], a[0:3]\n\t"
      "ds_read_b64_tr_b16 v[56:57], %[w0] offset:41728\n\t"
      "s_nop 0\n\tv_permlane32_swap_b32 v28, v29\n\t"
      "v_mfma_f32_16x16x32_bf16 a[4:7], v[32:35], v[108:111], a[4:7]\n\t"
      "ds_read_b64_tr_b16 v[58:59], %[w0] offset:42752\n\t"
      "v_add_f32 v28, v28, v29\n\t"
      "s_waitcnt lgkmcnt(10)\n\t"
      "v_mfma_f32_16x16x32_bf16 a[8:11], v[36:39], v[104:107], a[8:11]\n\t"
      "ds_read_b64_tr_b16 v[60:61], %[w1] offset:41728\n\t"
      "v_mul_f32 v28, 0x3b000000, v28\n\t"
      "v_mfma_f32_16x16x32_bf16 a[12:15], v[36:39], v[108:111], a[12:15]\n\t"
      "ds_read_b64_tr_b16 v[62:63], %[w1] offset:42752\n\t"
      "v_sub_f32 v20, v20, v28\n\t"
      "v_sub_f32 v21, v21, v28\n\t"
      "s_waitcnt lgkmcnt(10)\n\t"
      "v_mfma_f32_16x16x32_bf16 a[16:19], v[40:43], v[104:107], a[16:19]\n\t"
      "ds_read_b64_tr_b16 v[32:33], %[w0] offset:49152\n\t"
      "v_sub_f32 v22, v22, v28\n\t"
      "v_sub_f32 v23, v23, v28\n\t"
      "v_mfma_f32_16x16x32_bf16 a[20:23], v[40:43], v[108:111], a[20:23]\n\t"
      "ds_read_b64_tr_b16 v[34:35], %[w0] offset:50176\n\t"
      "v_sub_f32 v24, v24, v28\n\t"
      "v_sub_f32 v25, v25, v28\n\t"
      "s_waitcnt lgkmcnt(10)\n\t"
      "v_mfma_f32_16x16x32_bf16 a[24:27], v[44:47], v[104:107], a[24:27]\n\t"
      "ds_read_b64_tr_b16 v[36:37], %[w1] offset:49152\n\t"
      "v_sub_f32 v26, v26, v28\n\t"
      "v_sub_f32 v27, v27, v28\n\t"
      "v_mfma_f32_16x16x32_bf16 a[28:31], v[44:47], v[108:111], a[28:31]\n\t"
      "ds_read_b64_tr_b16 v[38:39], %[w1] offset:50176\n\t"
      "v_mul_f32 v28, v20, v20\n\t"
      "v_mul_f32 v29, v21, v21\n\t"
      "s_add_u32 %[t0], %[sw], 3145728\n\t"
      "s_add_u32 m0, %[dw], 98304\n\t"
      "s_nop 0\n\t"
      "buffer_load_dwordx4 %[vw], %[rw0], %[t0] offen lds\n\t"
      "s_waitcnt lgkmcnt(10)\n\t"
      "v_mfma_f32_16x16x32_bf16 a[32:35], v[48:51], v[104:107], a[32:35]\n\t"
      "ds_read_b64_tr_b16 v[40:41], %[w0] offset:49408\n\t"
      "v_fmac_f32 v28, v22, v22\n\t"
      "v_fmac_f32 v29, v23, v23\n\t"
      "s_add_u32 %[t0], %[sw], 3407872\n\t"
      "s_add_u32 m0, %[dw], 106496\n\t"
      "s_nop 0\n\t"
      "buffer_load_dwordx4 %[vw], %[rw0], %[t0] offen lds\n\t"
      "v_mfma_f32_16x16x32_bf16 a[36:39], v[48:51], v[108:111], a[36:39]\n\t"
      "ds_read_b64_tr_b16 v[42:43], %[w0] offset:50432\n\t"
      "v_fmac_f32 v28, v24, v24\n\t"
      "v_fmac_f32 v29, v25, v25\n\t"
      "s_add_u32 %[t1], %[sc], 10240\n\t"
      "buffer_load_dwordx4 v[80:83], %[vh0], %[rs], %[t1] offen offset:0\n\t"
      "s_waitcnt lgkmcnt(10)\n\t"
      "v_mfma_f32_16x16x32_bf16 a[40:43], v[52:55], v[104:107], a[40:43]\n\t"
      "ds_read_b64_tr_b16 v[44:45], %[w1] offset:49408\n\t"
      "v_fmac_f32 v28, v26, v26\n\t"
      "v_fmac_f32 v29, v27, v27\n\t"
      "buffer_load_dwordx4 v[84:87], %[vh1], %[rs], %[t1] offen offset:0\n\t"
      "v_mfma_f32_16x16x32_bf16 a[44:47], v[52:55], v[108:111], a[44:47]\n\t"
      "ds_read_b64_tr_b16 v[46:47], %[w1] offset:50432\n\t"
      "v_add_f32 v28, v28, v29\n\t"
      "buffer_load_dwordx4 v[88:91], %[vh0], %[rs], %[t1] offen offset:1024\n\t"
      "s_waitcnt lgkmcnt(10)\n\t"
      "v_mfma_f32_16x16x32_bf16 a[48:51], v[56:59], v[104:107], a[48:51]\n\t"
      "ds_read_b64_tr_b16 v[48:49], %[w0] offset:49664\n\t"
      "s_nop 0\n\tv_add_f32_dpp v28, v28, v28 row_ror:4 row_mask:0xf bank_mask:0xf\n\t"
      "v_mfma_f32_16x16x32_bf16 a[52:55], v[56:59], v[108:111], a[52:55]\n\t"
      "ds_read_b64_tr_b16 v[50:51], %[w0] offset:50688\n\t"
      "s_nop 0\n\tv_add_f32_dpp v28, v28, v28 row_ror:2 row_mask:0xf bank_mask:0xf\n\t"
      "buffer_load_dwordx4 v[92:95], %[vh1], %[rs], %[t1] offen offset:1024\n\t"
      "s_waitcnt lgkmcnt(10)\n\t"
      "v_mfma_f32_16x16x32_bf16 a[56:59], v[60:63], v[104:107], a[56:59]\n\t"
      "ds_read_b64_tr_b16 v[52:53], %[w1] offset:49664\n\t"
      "s_nop 0\n\tv_add_f32_dpp v28, v28, v28 row_ror:1 row_mask:0xf bank_mask:0xf\n\t"
      "v_mfma_f32_16x16x32_bf16 a[60:63], v[60:63], v[108:111], a[60:63]\n\t"
      "ds_read_b64_tr_b16 v[54:55], %[w1] offset:50688\n\t"
      "s_nop 0\n\tv_add_f32_dpp v28, v28, v28 row_ror:8 row_mask:0xf bank_mask:0xf\n\t"
      "s_add_u32 %[t0], %[st], 524288\n\t"
      "s_buffer_load_dword %[ts], %[rw2], %[t0]\n\t"
      "s_add_u32 %[t0], %[t0], 64\n\t"
      "s_buffer_load_dword %[ts], %[rw2], %[t0]\n\t"
      "s_waitcnt vmcnt(14)\n\t"
      "s_waitcnt lgkmcnt(10)\n\t"
      "v_mfma_f32_16x16x32_bf16 a[0:3], v[32:35], v[112:115], a[0:3]\n\t"
      "ds_read_b64_tr_b16 v[56:57], %[w0] offset:49920\n\t"
      "v_mov_b32 v29, v28\n\t"
      "v_mfma_f32_16x16x32_bf16 a[4:7], v[32:35], v[116:119], a[4:7]\n\t"
      "ds_read_b64_tr_b16 v[58:59], %[w0] offset:50944\n\t"
      "s_nop 0\n\tv_permlane16_swap_b32 v28, v29\n\t"
      "s_waitcnt lgkmcnt(10)\n\t"
      "v_mfma_f32_16x16x32_bf16 a[8:11], v[36:39], v[112:115], a[8:11]\n\t"
      "ds_read_b64_tr_b16 v[60:61], %[w1] offset:49920\n\t"
      "v_add_f32 v28, v28, v29\n\t"
      "v_mfma_f32_16x16x32_bf16 a[12:15], v[36:39], v[116:119], a[12:15]\n\t"
      "ds_read_b64_tr_b16 v[62:63], %[w1] offset:50944\n\t"
      "v_mov_b32 v29, v28\n\t"
      "s_waitcnt lgkmcnt(10)\n\t"
      "v_mfma_f32_16x16x32_bf16 a[16:19], v[40:43], v[112:115], a[16:19]\n\t"
      "ds_read_b64_tr_b16 v[32:33], %[w0] offset:57344\n\t"
      "s_nop 0\n\tv_permlane32_swap_b32 v28, v29\n\t"
      "v_mfma_f32_16x16x32_bf16 a[20:23], v[40:43], v[116:119], a[20:23]\n\t"
      "ds_read_b64_tr_b16 v[34:35], %[w0] offset:58368\n\t"
      "v_add_f32 v28, v28, v29\n\t"
      "v_mov_b32 v29, 0x3727c5ac\n\t"
      "s_waitcnt lgkmcnt(10)\n\t"
      "v_mfma_f32_16x16x32_bf16 a[24:27], v[44:47], v[112:115], a[24:27]\n\t"
      "ds_read_b64_tr_b16 v[36:37], %[w1] offset:57344\n\t"
      "v_fmac_f32 v29, 0x3b000000, v28\n\t"
      "v_mfma_f32_16x16x32_bf16 a[28:31], v[44:47], v[116:119], a[28:31]\n\t"
      "ds_read_b64_tr_b16 v[38:39], %[w1] offset:58368\n\t"
      "v_rsq_f32 v28, v29\n\t"
      "s_add_u32 %[t0], %[sw], 3670016\n\t"
      "s_add_u32 m0, %[dw], 114688\n\t"
      "s_nop 0\n\t"
      "buffer_load_dwordx4 %[vw], %[rw0], %[t0] offen lds\n\t"
      "s_waitcnt lgkmcnt(10)\n\t"
      "v_mfma_f32_16x16x32_bf16 a[32:35], v[48:51], v[112:115], a[32:35]\n\t"
      "ds_read_b64_tr_b16 v[40:41], %[w0] offset:57600\n\t"
      "v_mul_f32 v20, v20, v28\n\t"
      "v_mul_f32 v21, v21, v28\n\t"
      "v_mfma_f32_16x16x32_bf16 a[36:39], v[48:51], v[116:119], a[36:39]\n\t"
      "ds_read_b64_tr_b16 v[42:43], %[w0] offset:58624\n\t"
      "v_mul_f32 v22, v22, v28\n\t"
      "v_mul_f32 v23, v23, v28\n\t"
      "s_add_u32 %[t0], %[sw], 3932160\n\t"
      "s_add_u32 m0, %[dw], 122880\n\t"
      "s_nop 0\n\t"
      "buffer_load_dwordx4 %[vw], %[rw0], %[t0] offen lds\n\t"
      "s_waitcnt lgkmcnt(10)\n\t"
      "v_mfma_f32_16x16x32_bf16 a[40:43], v[52:55], v[112:115], a[40:43]\n\t"
      "ds_read_b64_tr_b16 v[44:45], %[w1] offset:57600\n\t"
      "v_mul_f32 v24, v24, v28\n\t"
      "v_mul_f32 v25, v25, v28\n\t"
      "v_mfma_f32_16x16x32_bf16 a[44:47], v[52:55], v[116:119], a[44:47]\n\t"
      "ds_read_b64_tr_b16 v[46:47], %[w1] offset:58624\n\t"
      "v_mul_f32 v26, v26, v28\n\t"
      "v_mul_f32 v27, v27, v28\n\t"
      "s_add_u32 %[t0], %[st], 786432\n\t"
      "s_buffer_load_dword %[ts], %[rw2], %[t0]\n\t"
      "s_add_u32 %[t0], %[t0], 64\n\t"
      "s_buffer_load_dword %[ts], %[rw2], %[t0]\n\t"
      "s_waitcnt lgkmcnt(10)\n\t"
      "v_mfma_f32_16x16x32_bf16 a[48:51], v[56:59], v[112:115], a[48:51]\n\t"
      "ds_read_b64_tr_b16 v[48:49], %[w0] offset:57856\n\t"
      "v_cvt_pk_bf16_f32 v16, v20, v21\n\t"
      "v_cvt_pk_bf16_f32 v17, v22, v23\n\t"
      "v_mfma_f32_16x16x32_bf16 a[52:55], v[56:59], v[116:119], a[52:55]\n\t"
      "ds_read_b64_tr_b16 v[50:51], %[w0] offset:58880\n\t"
      "v_cvt_pk_bf16_f32 v18, v24, v25\n\t"
      "v_cvt_pk_bf16_f32 v19, v26, v27\n\t"
      "s_waitcnt lgkmcnt(10)\n\t"
      "v_mfma_f32_16x16x32_bf16 a[56:59], v[60:63], v[112:115], a[56:59]\n\t"
      "ds_read_b64_tr_b16 v[52:53], %[w1] offset:57856\n\t"
      "v_lshlrev_b32 v20, 16, v16\n\t"
      "v_and_b32 v21, 0xffff0000, v16\n\t"
      "v_mfma_f32_16x16x32_bf16 a[60:63], v[60:63], v[116:119], a[60:63]\n\t"
      "ds_read_b64_tr_b16 v[54:55], %[w1] offset:58880\n\t"
      "v_lshlrev_b32 v22, 16, v17\n\t"
      "v_and_b32 v23, 0xffff0000, v17\n\t"
      "s_waitcnt vmcnt(14)\n\t"
      "s_barrier\n\t"
      "s_waitcnt lgkmcnt(10)\n\t"
      "v_mfma_f32_16x16x32_bf16 a[0:3], v[32:35], v[120:123], a[0:3]\n\t"
      "ds_read_b64_tr_b16 v[56:57], %[w0] offset:58112\n\t"
      "v_lshlrev_b32 v24, 16, v18\n\t"
      "v_and_b32 v25, 0xffff0000, v18\n\t"
      "v_mfma_f32_16x16x32_bf16 a[4:7], v[32:35], v[124:127], a[4:7]\n\t"
      "ds_read_b64_tr_b16 v[58:59], %[w0] offset:59136\n\t"
      "v_lshlrev_b32 v26, 16, v19\n\t"
      "v_and_b32 v27, 0xffff0000, v19\n\t"
      "s_waitcnt lgkmcnt(10)\n\t"
      "v_mfma_f32_16x16x32_bf16 a[8:11], v[36:39], v[120:123], a[8:11]\n\t"
      "ds_read_b64_tr_b16 v[60:61], %[w1] offset:58112\n\t"
      "v_accvgpr_read_b32 v16, a72\n\t"
      "v_accvgpr_read_b32 v17, a73\n\t"
      "v_mfma_f32_16x16x32_bf16 a[12:15], v[36:39], v[124:127], a[12:15]\n\t"
      "ds_read_b64_tr_b16 v[62:63], %[w1] offset:59136\n\t"
      "v_accvgpr_read_b32 v18, a74\n\t"
      "v_accvgpr_read_b32 v19, a75\n\t"
      "s_waitcnt lgkmcnt(10)\n\t"
      "v_mfma_f32_16x16x32_bf16 a[16:19], v[40:43], v[120:123], a[16:19]\n\t"
      "v_xor_b32 %[w0], 0x10000, %[w0]\n\t"
      "v_xor_b32 %[w1], 0x10000, %[w1]\n\t"
      "ds_read_b64_tr_b16 v[32:33], %[w0] offset:0\n\t"
      "v_accvgpr_read_b32 v28, a64\n\t"
      "v_accvgpr_read_b32 v29, a65\n\t"
      "v_mfma_f32_16x16x32_bf16 a[20:23], v[40:43], v[124:127], a[20:23]\n\t"
      "ds_read_b64_tr_b16 v[34:35], %[w0] offset:1024\n\t"
      "v_accvgpr_read_b32 v30, a66\n\t"
      "v_accvgpr_read_b32 v31, a67\n\t"
      "s_waitcnt lgkmcnt(10)\n\t"
      "v_mfma_f32_16x16x32_bf16 a[24:27], v[44:47], v[120:123], a[24:27]\n\t"
      "ds_read_b64_tr_b16 v[36:37], %[w1] offset:0\n\t"
      "v_fma_f32 v20, v20, v16, v28\n\t"
      "v_fma_f32 v21, v21, v17, v29\n\t"
      "v_mfma_f32_16x16x32_bf16 a[28:31], v[44:47], v[124:127], a[28:31]\n\t"
      "ds_read_b64_tr_b16 v[38:39], %[w1] offset:1024\n\t"
      "v_fma_f32 v22, v22, v18, v30\n\t"
      "v_fma_f32 v23, v23, v19, v31\n\t"
      "s_add_u32 %[t1], %[sc], 12288\n\t"
      "buffer_load_dwordx4 v[96:99], %[vh0], %[rs], %[t1] offen offset:0\n\t"
      "s_waitcnt lgkmcnt(10)\n\t"
      "v_mfma_f32_16x16x32_bf16 a[32:35], v[48:51], v[120:123], a[32:35]\n\t"
      "ds_read_b64_tr_b16 v[40:41], %[w0] offset:256\n\t"
      "v_accvgpr_read_b32 v16, a76\n\t"
      "v_accvgpr_read_b32 v17, a77\n\t"
      "v_mfma_f32_16x16x32_bf16 a[36:39], v[48:51], v[124:127], a[36:39]\n\t"
      "ds_read_b64_tr_b16 v[42:43], %[w0] offset:1280\n\t"
      "v_accvgpr_read_b32 v18, a78\n\t"
      "v_accvgpr_read_b32 v19, a79\n\t"
      "buffer_load_dwordx4 v[100:103], %[vh1], %[rs], %[t1] offen offset:0\n\t"
      "s_waitcnt lgkmcnt(10)\n\t"
      "v_mfma_f32_16x16x32_bf16 a[40:43], v[52:55], v[120:123], a[40:43]\n\t"
      "ds_read_b64_tr_b16 v[44:45], %[w1] offset:256\n\t"
      "v_accvgpr_read_b32 v28, a68\n\t"
      "v_accvgpr_read_b32 v29, a69\n\t"
      "v_mfma_f32_16x16x32_bf16 a[44:47], v[52:55], v[124:127], a[44:47]\n\t"
      "ds_read_b64_tr_b16 v[46:47], %[w1] offset:1280\n\t"
      "v_accvgpr_read_b32 v30, a70\n\t"
      "v_accvgpr_read_b32 v31, a71\n\t"
      "buffer_load_dwordx4 v[104:107], %[vh0], %[rs], %[t1] offen offset:1024\n\t"
      "s_waitcnt lgkmcnt(10)\n\t"
      "v_mfma_f32_16x16x32_bf16 a[48:51], v[56:59], v[120:123], a[48:51]\n\t"
      "ds_read_b64_tr_b16 v[48:49], %[w0] offset:512\n\t"
      "v_fma_f32 v24, v24, v16, v28\n\t"
      "v_fma_f32 v25, v25, v17, v29\n\t"
      "v_mfma_f32_16x16x32_bf16 a[52:55], v[56:59], v[124:127], a[52:55]\n\t"
      "ds_read_b64_tr_b16 v[50:51], %[w0] offset:1536\n\t"
      "v_fma_f32 v26, v26, v18, v30\n\t"
      "v_fma_f32 v27, v27, v19, v31\n\t"
      "buffer_load_dwordx4 v[108:111], %[vh1], %[rs], %[t1] offen offset:1024\n\t"
      "s_waitcnt lgkmcnt(10)\n\t"
      "v_mfma_f32_16x16x32_bf16 a[56:59], v[60:63], v[120:123], a[56:59]\n\t"
      "ds_read_b64_tr_b16 v[52:53], %[w1] offset:512\n\t"
      "v_cvt_pk_bf16_f32 v16, v20, v21\n\t"
      "v_cvt_pk_bf16_f32 v17, v22, v23\n\t"
      "v_mfma_f32_16x16x32_bf16 a[60:63], v[60:63], v[124:127], a[60:63]\n\t"
      "ds_read_b64_tr_b16 v[54:55], %[w1] offset:1536\n\t"
      "v_cvt_pk_bf16_f32 v18, v24, v25\n\t"
      "v_cvt_pk_bf16_f32 v19, v26, v27\n\t"
      "s_add_u32 %[t0], %[st], 1048576\n\t"
      "s_buffer_load_dword %[ts], %[rw2], %[t0]\n\t"
      "s_add_u32 %[t0], %[t0], 64\n\t"
      "s_buffer_load_dword %[ts], %[rw2], %[t0]\n\t"
      "s_waitcnt vmcnt(14)\n\t"
      "s_waitcnt lgkmcnt(10)\n\t"
      "v_mfma_f32_16x16x32_bf16 a[0:3], v[32:35], v[64:67], a[0:3]\n\t"
      "ds_read_b64_tr_b16 v[56:57], %[w0] offset:768\n\t"
      "ds_write_b128 %[vhw], v[16:19]\n\t"
      "v_mfma_f32_16x16x32_bf16 a[4:7], v[32:35], v[68:71], a[4:7]\n\t"
      "ds_read_b64_tr_b16 v[58:59], %[w0] offset:1792\n\t"
      "s_waitcnt lgkmcnt(10)\n\t"
      "v_mfma_f32_16x16x32_bf16 a[8:11], v[36:39], v[64:67], a[8:11]\n\t"
      "ds_read_b64_tr_b16 v[60:61], %[w1] offset:768\n\t"
      "v_mfma_f32_16x16x32_bf16 a[12:15], v[36:39], v[68:71], a[12:15]\n\t"
      "ds_read_b64_tr_b16 v[62:63], %[w1] offset:1792\n\t"
      "s_waitcnt lgkmcnt(10)\n\t"
      "v_mfma_f32_16x16x32_bf16 a[16:19], v[40:43], v[64:67], a[16:19]\n\t"
      "ds_read_b64_tr_b16 v[32:33], %[w0] offset:8192\n\t"
      "v_mfma_f32_16x16x32_bf16 a[20:23], v[40:43], v[68:71], a[20:23]\n\t"
      "ds_read_b64_tr_b16 v[34:35], %[w0] offset:9216\n\t"
      "s_waitcnt lgkmcnt(10)\n\t"
      "v_mfma_f32_16x16x32_bf16 a[24:27], v[44:47], v[64:67], a[24:27]\n\t"
      "ds_read_b64_tr_b16 v[36:37], %[w1] offset:8192\n\t"
      "v_mfma_f32_16x16x32_bf16 a[28:31], v[44:47], v[68:71], a[28:31]\n\t"
      "ds_read_b64_tr_b16 v[38:39], %[w1] offset:9216\n\t"
      "s_add_u32 %[t0], %[sw], 0\n\t"
      "s_add_u32 m0, %[dw], 0\n\t"
      "s_nop 0\n\t"
      "buffer_load_dwordx4 %[vw], %[rw1], %[t0] offen lds\n\t"
      "s_waitcnt lgkmcnt(10)\n\t"
      "v_mfma_f32_16x16x32_bf16 a[32:35], v[48:51], v[64:67], a[32:35]\n\t"
      "ds_read_b64_tr_b16 v[40:41], %[w0] offset:8448\n\t"
      "v_mfma_f32_16x16x32_bf16 a[36:39], v[48:51], v[68:71], a[36:39]\n\t"
      "ds_read_b64_tr_b16 v[42:43], %[w0] offset:9472\n\t"
      "s_add_u32 %[t0], %[sw], 262144\n\t"
      "s_add_u32 m0, %[dw], 8192\n\t"
      "s_nop 0\n\t"
      "buffer_load_dwordx4 %[vw], %[rw1], %[t0] offen lds\n\t"
      "s_waitcnt lgkmcnt(10)\n\t"
      "v_mfma_f32_16x16x32_bf16 a[40:43], v[52:55], v[64:67], a[40:43]\n\t"
      "ds_read_b64_tr_b16 v[44:45], %[w1] offset:8448\n\t"
      "v_mfma_f32_16x16x32_bf16 a[44:47], v[52:55], v[68:71], a[44:47]\n\t"
      "ds_read_b64_tr_b16 v[46:47], %[w1] offset:9472\n\t"
      "s_add_u32 %[t0], %[st], 1310720\n\t"
      "s_buffer_load_dword %[ts], %[rw2], %[t0]\n\t"
      "s_add_u32 %[t0], %[t0], 64\n\t"
      "s_buffer_load_dword %[ts], %[rw2], %[t0]\n\t"
      "s_waitcnt lgkmcnt(10)\n\t"
      "v_mfma_f32_16x16x32_bf16 a[48:51], v[56:59], v[64:67], a[48:51]\n\t"
      "ds_read_b64_tr_b16 v[48:49], %[w0] offset:8704\n\t"
      "v_mfma_f32_16x16x32_bf16 a[52:55], v[56:59], v[68:71], a[52:55]\n\t"
      "ds_read_b64_tr_b16 v[50:51], %[w0] offset:9728\n\t"
      "s_waitcnt lgkmcnt(10)\n\t"
      "v_mfma_f32_16x16x32_bf16 a[56:59], v[60:63], v[64:67], a[56:59]\n\t"
      "ds_read_b64_tr_b16 v[52:53], %[w1] offset:8704\n\t"
      "v_mfma_f32_16x16x32_bf16 a[60:63], v[60:63], v[68:71], a[60:63]\n\t"
      "ds_read_b64_tr_b16 v[54:55], %[w1] offset:9728\n\t"
      "s_waitcnt vmcnt(14)\n\t"
      "s_barrier\n\t"
      "ds_read_b128 v[16:19], %[vhr]\n\t"
      "s_waitcnt lgkmcnt(10)\n\t"
      "v_mfma_f32_16x16x32_bf16 a[0:3], v[32:35], v[72:75], a[0:3]\n\t"
      "ds_read_b64_tr_b16 v[56:57], %[w0] offset:8960\n\t"
      "v_mfma_f32_16x16x32_bf16 a[4:7], v[32:35], v[76:79], a[4:7]\n\t"
      "ds_read_b64_tr_b16 v[58:59], %[w0] offset:9984\n\t"
      "s_waitcnt lgkmcnt(10)\n\t"
      "v_mfma_f32_16x16x32_bf16 a[8:11], v[36:39], v[72:75], a[8:11]\n\t"
      "ds_read_b64_tr_b16 v[60:61], %[w1] offset:8960\n\t"
      "v_mfma_f32_16x16x32_bf16 a[12:15], v[36:39], v[76:79], a[12:15]\n\t"
      "ds_read_b64_tr_b16 v[62:63], %[w1] offset:9984\n\t"
      "s_waitcnt lgkmcnt(10)\n\t"
      "v_mfma_f32_16x16x32_bf16 a[16:19], v[40:43], v[72:75], a[16:19]\n\t"
      "ds_read_b64_tr_b16 v[32:33], %[w0] offset:16384\n\t"
      "v_mfma_f32_16x16x32_bf16 a[20:23], v[40:43], v[76:79], a[20:23]\n\t"
      "ds_read_b64_tr_b16 v[34:35], %[w0] offset:17408\n\t"
      "s_waitcnt lgkmcnt(10)\n\t"
      "v_mfma_f32_16x16x32_bf16 a[24:27], v[44:47], v[72:75], a[24:27]\n\t"
      "ds_read_b64_tr_b16 v[36:37], %[w1] offset:16384\n\t"
      "v_mfma_f32_16x16x32_bf16 a[28:31], v[44:47], v[76:79], a[28:31]\n\t"
      "ds_read_b64_tr_b16 v[38:39], %[w1] offset:17408\n\t"
      "s_add_u32 %[t0], %[sw], 524288\n\t"
      "s_add_u32 m0, %[dw], 16384\n\t"
      "s_nop 0\n\t"
      "buffer_load_dwordx4 %[vw], %[rw1], %[t0] offen lds\n\t"
      "s_waitcnt lgkmcnt(10)\n\t"
      "v_mfma_f32_16x16x32_bf16 a[32:35], v[48:51], v[72:75], a[32:35]\n\t"
      "ds_read_b64_tr_b16 v[40:41], %[w0] offset:16640\n\t"
      "s_add_u32 %[t0], %[sw], 786432\n\t"
      "s_add_u32 m0, %[dw], 24576\n\t"
      "s_nop 0\n\t"
      "buffer_load_dwordx4 %[vw], %[rw1], %[t0] offen lds\n\t"
      "v_mfma_f32_16x16x32_bf16 a[36:39], v[48:51], v[76:79], a[36:39]\n\t"
      "ds_read_b64_tr_b16 v[42:43], %[w0] offset:17664\n\t"
      "s_add_u32 %[t1], %[sc], 14336\n\t"
      "buffer_load_dwordx4 v[112:115], %[vh0], %[rs], %[t1] offen offset:0\n\t"
      "s_waitcnt lgkmcnt(10)\n\t"
      "v_mfma_f32_16x16x32_bf16 a[40:43], v[52:55], v[72:75], a[40:43]\n\t"
      "ds_read_b64_tr_b16 v[44:45], %[w1] offset:16640\n\t"
      "buffer_load_dwordx4 v[116:119], %[vh1], %[rs], %[t1] offen offset:0\n\t"
      "v_mfma_f32_16x16x32_bf16 a[44:47], v[52:55], v[76:79], a[44:47]\n\t"
      "ds_read_b64_tr_b16 v[46:47], %[w1] offset:17664\n\t"
      "buffer_load_dwordx4 v[120:123], %[vh0], %[rs], %[t1] offen offset:1024\n\t"
      "s_waitcnt lgkmcnt(10)\n\t"
      "v_mfma_f32_16x16x32_bf16 a[48:51], v[56:59], v[72:75], a[48:51]\n\t"
      "ds_read_b64_tr_b16 v[48:49], %[w0] offset:16896\n\t"
      "v_mfma_f32_16x16x32_bf16 a[52:55], v[56:59], v[76:79], a[52:55]\n\t"
      "ds_read_b64_tr_b16 v[50:51], %[w0] offset:17920\n\t"
      "buffer_load_dwordx4 v[124:127], %[vh1], %[rs], %[t1] offen offset:1024\n\t"
      "s_waitcnt lgkmcnt(10)\n\t"
      "v_mfma_f32_16x16x32_bf16 a[56:59], v[60:63], v[72:75], a[56:59]\n\t"
      "ds_read_b64_tr_b16 v[52:53], %[w1] offset:16896\n\t"
      "v_mfma_f32_16x16x32_bf16 a[60:63], v[60:63], v[76:79], a[60:63]\n\t"
      "ds_read_b64_tr_b16 v[54:55], %[w1] offset:17920\n\t"
      "s_add_u32 %[t0], %[st], 1572864\n\t"
      "s_buffer_load_dword %[ts], %[rw2], %[t0]\n\t"
      "s_add_u32 %[t0], %[t0], 64\n\t"
      "s_buffer_load_dword %[ts], %[rw2], %[t0]\n\t"
      "s_waitcnt vmcnt(14)\n\t"
      "s_waitcnt lgkmcnt(10)\n\t"
      "v_mfma_f32_16x16x32_bf16 a[0:3], v[32:35], v[80:83], a[0:3]\n\t"
      "ds_read_b64_tr_b16 v[56:57], %[w0] offset:17152\n\t"
      "buffer_store_dwordx4 v[16:19], %[vxn], %[rs], %[sxn] offen sc1\n\t"
      "v_mfma_f32_16x16x32_bf16 a[4:7], v[32:35], v[84:87], a[4:7]\n\t"
      "ds_read_b64_tr_b16 v[58:59], %[w0] offset:18176\n\t"
      "s_waitcnt lgkmcnt(10)\n\t"
      "v_mfma_f32_16x16x32_bf16 a[8:11], v[36:39], v[80:83], a[8:11]\n\t"
      "ds_read_b64_tr_b16 v[60:61], %[w1] offset:17152\n\t"
      "v_mfma_f32_16x16x32_bf16 a[12:15], v[36:39], v[84:87], a[12:15]\n\t"
      "ds_read_b64_tr_b16 v[62:63], %[w1] offset:18176\n\t"
      "s_waitcnt lgkmcnt(10)\n\t"
      "v_mfma_f32_16x16x32_bf16 a[16:19], v[40:43], v[80:83], a[16:19]\n\t"
      "ds_read_b64_tr_b16 v[32:33], %[w0] offset:24576\n\t"
      "v_mfma_f32_16x16x32_bf16 a[20:23], v[40:43], v[84:87], a[20:23]\n\t"
      "ds_read_b64_tr_b16 v[34:35], %[w0] offset:25600\n\t"
      "s_waitcnt lgkmcnt(10)\n\t"
      "v_mfma_f32_16x16x32_bf16 a[24:27], v[44:47], v[80:83], a[24:27]\n\t"
      "ds_read_b64_tr_b16 v[36:37], %[w1] offset:24576\n\t"
      "v_mfma_f32_16x16x32_bf16 a[28:31], v[44:47], v[84:87], a[28:31]\n\t"
      "ds_read_b64_tr_b16 v[38:39], %[w1] offset:25600\n\t"
      "s_add_u32 %[t0], %[sw], 1048576\n\t"
      "s_add_u32 m0, %[dw], 32768\n\t"
      "s_nop 0\n\t"
      "buffer_load_dwordx4 %[vw], %[rw1], %[t0] offen lds\n\t"
      "s_waitcnt lgkmcnt(10)\n\t"
      "v_mfma_f32_16x16x32_bf16 a[32:35], v[48:51], v[80:83], a[32:35]\n\t"
      "ds_read_b64_tr_b16 v[40:41], %[w0] offset:24832\n\t"
      "v_mfma_f32_16x16x32_bf16 a[36:39], v[48:51], v[84:87], a[36:39]\n\t"
      "ds_read_b64_tr_b16 v[42:43], %[w0] offset:25856\n\t"
      "s_add_u32 %[t0], %[sw], 1310720\n\t"
      "s_add_u32 m0, %[dw], 40960\n\t"
      "s_nop 0\n\t"
      "buffer_load_dwordx4 %[vw], %[rw1], %[t0] offen lds\n\t"
      "s_waitcnt lgkmcnt(10)\n\t"
      "v_mfma_f32_16x16x32_bf16 a[40:43], v[52:55], v[80:83], a[40:43]\n\t"
      "ds_read_b64_tr_b16 v[44:45], %[w1] offset:24832\n\t"
      "v_mfma_f32_16x16x32_bf16 a[44:47], v[52:55], v[84:87], a[44:47]\n\t"
      "ds_read_b64_tr_b16 v[46:47], %[w1] offset:25856\n\t"
      "s_add_u32 %[t0], %[st], 1835008\n\t"
      "s_buffer_load_dword %[ts], %[rw2], %[t0]\n\t"
      "s_add_u32 %[t0], %[t0], 64\n\t"
      "s_buffer_load_dword %[ts], %[rw2], %[t0]\n\t"
      "s_waitcnt lgkmcnt(10)\n\t"
      "v_mfma_f32_16x16x32_bf16 a[48:51], v[56:59], v[80:83], a[48:51]\n\t"
      "ds_read_b64_tr_b16 v[48:49], %[w0] offset:25088\n\t"
      "v_mfma_f32_16x16x32_bf16 a[52:55], v[56:59], v[84:87], a[52:55]\n\t"
      "ds_read_b64_tr_b16 v[50:51], %[w0] offset:26112\n\t"
      "s_waitcnt lgkmcnt(10)\n\t"
      "v_mfma_f32_16x16x32_bf16 a[56:59], v[60:63], v[80:83], a[56:59]\n\t"
      "ds_read_b64_tr_b16 v[52:53], %[w1] offset:25088\n\t"
      "v_mfma_f32_16x16x32_bf16 a[60:63], v[60:63], v[84:87], a[60:63]\n\t"
      "ds_read_b64_tr_b16 v[54:55], %[w1] offset:26112\n\t"
      "s_waitcnt vmcnt(21)\n\t"
      "s_barrier\n\t"
      "s_waitcnt lgkmcnt(10)\n\t"
      "v_mfma_f32_16x16x32_bf16 a[0:3], v[32:35], v[88:91], a[0:3]\n\t"
      "ds_read_b64_tr_b16 v[56:57], %[w0] offset:25344\n\t"
      "v_mfma_f32_16x16x32_bf16 a[4:7], v[32:35], v[92:95], a[4:7]\n\t"
      "ds_read_b64_tr_b16 v[58:59], %[w0] offset:26368\n\t"
      "s_waitcnt lgkmcnt(10)\n\t"
      "v_mfma_f32_16x16x32_bf16 a[8:11], v[36:39], v[88:91], a[8:11]\n\t"
      "ds_read_b64_tr_b16 v[60:61], %[w1] offset:25344\n\t"
      "v_mfma_f32_16x16x32_bf16 a[12:15], v[36:39], v[92:95], a[12:15]\n\t"
      "ds_read_b64_tr_b16 v[62:63], %[w1] offset:26368\n\t"
      "s_waitcnt lgkmcnt(10)\n\t"
      "v_mfma_f32_16x16x32_bf16 a[16:19], v[40:43], v[88:91], a[16:19]\n\t"
      "ds_read_b64_tr_b16 v[32:33], %[w0] offset:32768\n\t"
      "v_mfma_f32_16x16x32_bf16 a[20:23], v[40:43], v[92:95], a[20:23]\n\t"
      "ds_read_b64_tr_b16 v[34:35], %[w0] offset:33792\n\t"
      "s_waitcnt lgkmcnt(10)\n\t"
      "v_mfma_f32_16x16x32_bf16 a[24:27], v[44:47], v[88:91], a[24:27]\n\t"
      "ds_read_b64_tr_b16 v[36:37], %[w1] offset:32768\n\t"
      "v_mfma_f32_16x16x32_bf16 a[28:31], v[44:47], v[92:95], a[28:31]\n\t"
      "ds_read_b64_tr_b16 v[38:39], %[w1] offset:33792\n\t"
      "s_add_u32 %[t1], %[sc], 0\n\t"
      "buffer_load_dwordx4 v[64:67], %[vh0], %[rs], %[t1] offen offset:0\n\t"
      "s_waitcnt lgkmcnt(10)\n\t"
      "v_mfma_f32_16x16x32_bf16 a[32:35], v[48:51], v[88:91], a[32:35]\n\t"
      "ds_read_b64_tr_b16 v[40:41], %[w0] offset:33024\n\t"
      "v_mfma_f32_16x16x32_bf16 a[36:39], v[48:51], v[92:95], a[36:39]\n\t"
      "ds_read_b64_tr_b16 v[42:43], %[w0] offset:34048\n\t"
      "buffer_load_dwordx4 v[68:71], %[vh1], %[rs], %[t1] offen offset:0\n\t"
      "s_waitcnt lgkmcnt(10)\n\t"
      "v_mfma_f32_16x16x32_bf16 a[40:43], v[52:55], v[88:91], a[40:43]\n\t"
      "ds_read_b64_tr_b16 v[44:45], %[w1] offset:33024\n\t"
      "v_mfma_f32_16x16x32_bf16 a[44:47], v[52:55], v[92:95], a[44:47]\n\t"
      "ds_read_b64_tr_b16 v[46:47], %[w1] offset:34048\n\t"
      "buffer_load_dwordx4 v[72:75], %[vh0], %[rs], %[t1] offen offset:1024\n\t"
      "s_waitcnt lgkmcnt(10)\n\t"
      "v_mfma_f32_16x16x32_bf16 a[48:51], v[56:59], v[88:91], a[48:51]\n\t"
      "ds_read_b64_tr_b16 v[48:49], %[w0] offset:33280\n\t"
      "v_mfma_f32_16x16x32_bf16 a[52:55], v[56:59], v[92:95], a[52:55]\n\t"
      "ds_read_b64_tr_b16 v[50:51], %[w0] offset:34304\n\t"
      "buffer_load_dwordx4 v[76:79], %[vh1], %[rs], %[t1] offen offset:1024\n\t"
      "s_waitcnt lgkmcnt(10)\n\t"
      "v_mfma_f32_16x16x32_bf16 a[56:59], v[60:63], v[88:91], a[56:59]\n\t"
      "ds_read_b64_tr_b16 v[52:53], %[w1] offset:33280\n\t"
      "v_mfma_f32_16x16x32_bf16 a[60:63], v[60:63], v[92:95], a[60:63]\n\t"
      "ds_read_b64_tr_b16 v[54:55], %[w1] offset:34304\n\t"
      "s_add_u32 %[t0], %[st], 2097152\n\t"
      "s_buffer_load_dword %[ts], %[rw2], %[t0]\n\t"
      "s_add_u32 %[t0], %[t0], 64\n\t"
      "s_buffer_load_dword %[ts], %[rw2], %[t0]\n\t"
      "s_waitcnt vmcnt(15)\n\t"
      "s_waitcnt lgkmcnt(10)\n\t"
      "v_mfma_f32_16x16x32_bf16 a[0:3], v[32:35], v[96:99], a[0:3]\n\t"
      "ds_read_b64_tr_b16 v[56:57], %[w0] offset:33536\n\t"
      "v_mfma_f32_16x16x32_bf16 a[4:7], v[32:35], v[100:103], a[4:7]\n\t"
      "ds_read_b64_tr_b16 v[58:59], %[w0] offset:34560\n\t"
      "s_waitcnt lgkmcnt(10)\n\t"
      "v_mfma_f32_16x16x32_bf16 a[8:11], v[36:39], v[96:99], a[8:11]\n\t"
      "ds_read_b64_tr_b16 v[60:61], %[w1] offset:33536\n\t"
      "v_mfma_f32_16x16x32_bf16 a[12:15], v[36:39], v[100:103], a[12:15]\n\t"
      "ds_read_b64_tr_b16 v[62:63], %[w1] offset:34560\n\t"
      "s_waitcnt lgkmcnt(10)\n\t"
      "v_mfma_f32_16x16x32_bf16 a[16:19], v[40:43], v[96:99], a[16:19]\n\t"
      "ds_read_b64_tr_b16 v[32:33], %[w0] offset:40960\n\t"
      "v_mfma_f32_16x16x32_bf16 a[20:23], v[40:43], v[100:103], a[20:23]\n\t"
      "ds_read_b64_tr_b16 v[34:35], %[w0] offset:41984\n\t"
      "s_waitcnt lgkmcnt(10)\n\t"
      "v_mfma_f32_16x16x32_bf16 a[24:27], v[44:47], v[96:99], a[24:27]\n\t"
      "ds_read_b64_tr_b16 v[36:37], %[w1] offset:40960\n\t"
      "v_mfma_f32_16x16x32_bf16 a[28:31], v[44:47], v[100:103], a[28:31]\n\t"
      "ds_read_b64_tr_b16 v[38:39], %[w1] offset:41984\n\t"
      "s_add_u32 %[t0], %[sw], 1572864\n\t"
      "s_add_u32 m0, %[dw], 49152\n\t"
      "s_nop 0\n\t"
      "buffer_load_dwordx4 %[vw], %[rw1], %[t0] offen lds\n\t"
      "s_waitcnt lgkmcnt(10)\n\t"
      "v_mfma_f32_16x16x32_bf16 a[32:35], v[48:51], v[96:99], a[32:35]\n\t"
      "ds_read_b64_tr_b16 v[40:41], %[w0] offset:41216\n\t"
      "v_mfma_f32_16x16x32_bf16 a[36:39], v[48:51], v[100:103], a[36:39]\n\t"
      "ds_read_b64_tr_b16 v[42:43], %[w0] offset:42240\n\t"
      "s_add_u32 %[t0], %[sw], 1835008\n\t"
      "s_add_u32 m0, %[dw], 57344\n\t"
      "s_nop 0\n\t"
      "buffer_load_dwordx4 %[vw], %[rw1], %[t0] offen lds\n\t"
      "s_waitcnt lgkmcnt(10)\n\t"
      "v_mfma_f32_16x16x32_bf16 a[40:43], v[52:55], v[96:99], a[40:43]\n\t"
      "ds_read_b64_tr_b16 v[44:45], %[w1] offset:41216\n\t"
      "v_mfma_f32_16x16x32_bf16 a[44:47], v[52:55], v[100:103], a[44:47]\n\t"
      "ds_read_b64_tr_b16 v[46:47], %[w1] offset:42240\n\t"
      "s_add_u32 %[t0], %[st], 2359296\n\t"
      "s_buffer_load_dword %[ts], %[rw2], %[t0]\n\t"
      "s_add_u32 %[t0], %[t0], 64\n\t"
      "s_buffer_load_dword %[ts], %[rw2], %[t0]\n\t"
      "s_waitcnt lgkmcnt(10)\n\t"
      "v_mfma_f32_16x16x32_bf16 a[48:51], v[56:59], v[96:99], a[48:51]\n\t"
      "ds_read_b64_tr_b16 v[48:49], %[w0] offset:41472\n\t"
      "v_mfma_f32_16x16x32_bf16 a[52:55], v[56:59], v[100:103], a[52:55]\n\t"
      "ds_read_b64_tr_b16 v[50:51], %[w0] offset:42496\n\t"
      "s_waitcnt lgkmcnt(10)\n\t"
      "v_mfma_f32_16x16x32_bf16 a[56:59], v[60:63], v[96:99], a[56:59]\n\t"
      "ds_read_b64_tr_b16 v[52:53], %[w1] offset:41472\n\t"
      "v_mfma_f32_16x16x32_bf16 a[60:63], v[60:63], v[100:103], a[60:63]\n\t"
      "ds_read_b64_tr_b16 v[54:55], %[w1] offset:42496\n\t"
      "s_waitcnt vmcnt(21)\n\t"
      "s_barrier\n\t"
      "s_waitcnt lgkmcnt(10)\n\t"
      "v_mfma_f32_16x16x32_bf16 a[0:3], v[32:35], v[104:107], a[0:3]\n\t"
      "ds_read_b64_tr_b16 v[56:57], %[w0] offset:41728\n\t"
      "v_mfma_f32_16x16x32_bf16 a[4:7], v[32:35], v[108:111], a[4:7]\n\t"
      "ds_read_b64_tr_b16 v[58:59], %[w0] offset:42752\n\t"
      "s_waitcnt lgkmcnt(10)\n\t"
      "v_mfma_f32_16x16x32_bf16 a[8:11], v[36:39], v[104:107], a[8:11]\n\t"
      "ds_read_b64_tr_b16 v[60:61], %[w1] offset:41728\n\t"
      "v_mfma_f32_16x16x32_bf16 a[12:15], v[36:39], v[108:111], a[12:15]\n\t"
      "ds_read_b64_tr_b16 v[62:63], %[w1] offset:42752\n\t"
      "s_waitcnt lgkmcnt(10)\n\t"
      "v_mfma_f32_16x16x32_bf16 a[16:19], v[40:43], v[104:107], a[16:19]\n\t"
      "ds_read_b64_tr_b16 v[32:33], %[w0] offset:49152\n\t"
      "v_mfma_f32_16x16x32_bf16 a[20:23], v[40:43], v[108:111], a[20:23]\n\t"
      "ds_read_b64_tr_b16 v[34:35], %[w0] offset:50176\n\t"
      "s_waitcnt lgkmcnt(10)\n\t"
      "v_mfma_f32_16x16x32_bf16 a[24:27], v[44:47], v[104:107], a[24:27]\n\t"
      "ds_read_b64_tr_b16 v[36:37], %[w1] offset:49152\n\t"
      "v_mfma_f32_16x16x32_bf16 a[28:31], v[44:47], v[108:111], a[28:31]\n\t"
      "ds_read_b64_tr_b16 v[38:39], %[w1] offset:50176\n\t"
      "s_add_u32 %[t1], %[sc], 2048\n\t"
      "buffer_load_dwordx4 v[80:83], %[vh0], %[rs], %[t1] offen offset:0\n\t"
      "s_waitcnt lgkmcnt(10)\n\t"
      "v_mfma_f32_16x16x32_bf16 a[32:35], v[48:51], v[104:107], a[32:35]\n\t"
      "ds_read_b64_tr_b16 v[40:41], %[w0] offset:49408\n\t"
      "v_mfma_f32_16x16x32_bf16 a[36:39], v[48:51], v[108:111], a[36:39]\n\t"
      "ds_read_b64_tr_b16 v[42:43], %[w0] offset:50432\n\t"
      "buffer_load_dwordx4 v[84:87], %[vh1], %[rs], %[t1] offen offset:0\n\t"
      "s_waitcnt lgkmcnt(10)\n\t"
      "v_mfma_f32_16x16x32_bf16 a[40:43], v[52:55], v[104:107], a[40:43]\n\t"
      "ds_read_b64_tr_b16 v[44:45], %[w1] offset:49408\n\t"
      "v_mfma_f32_16x16x32_bf16 a[44:47], v[52:55], v[108:111], a[44:47]\n\t"
      "ds_read_b64_tr_b16 v[46:47], %[w1] offset:50432\n\t"
      "buffer_load_dwordx4 v[88:91], %[vh0], %[rs], %[t1] offen offset:1024\n\t"
      "s_waitcnt lgkmcnt(10)\n\t"
      "v_mfma_f32_16x16x32_bf16 a[48:51], v[56:59], v[104:107], a[48:51]\n\t"
      "ds_read_b64_tr_b16 v[48:49], %[w0] offset:49664\n\t"
      "v_mfma_f32_16x16x32_bf16 a[52:55], v[56:59], v[108:111], a[52:55]\n\t"
      "ds_read_b64_tr_b16 v[50:51], %[w0] offset:50688\n\t"
      "buffer_load_dwordx4 v[92:95], %[vh1], %[rs], %[t1] offen offset:1024\n\t"
      "s_waitcnt lgkmcnt(10)\n\t"
      "v_mfma_f32_16x16x32_bf16 a[56:59], v[60:63], v[104:107], a[56:59]\n\t"
      "ds_read_b64_tr_b16 v[52:53], %[w1] offset:49664\n\t"
      "v_mfma_f32_16x16x32_bf16 a[60:63], v[60:63], v[108:111], a[60:63]\n\t"
      "ds_read_b64_tr_b16 v[54:55], %[w1] offset:50688\n\t"
      "s_add_u32 %[t0], %[st], 2621440\n\t"
      "s_buffer_load_dword %[ts], %[rw2], %[t0]\n\t"
      "s_add_u32 %[t0], %[t0], 64\n\t"
      "s_buffer_load_dword %[ts], %[rw2], %[t0]\n\t"
      "s_waitcnt vmcnt(13)\n\t"
      "s_waitcnt lgkmcnt(10)\n\t"
      "v_mfma_f32_16x16x32_bf16 a[0:3], v[32:35], v[112:115], a[0:3]\n\t"
      "ds_read_b64_tr_b16 v[56:57], %[w0] offset:49920\n\t"
      "v_mfma_f32_16x16x32_bf16 a[4:7], v[32:35], v[116:119], a[4:7]\n\t"
      "ds_read_b64_tr_b16 v[58:59], %[w0] offset:50944\n\t"
      "s_waitcnt lgkmcnt(10)\n\t"
      "v_mfma_f32_16x16x32_bf16 a[8:11], v[36:39], v[112:115], a[8:11]\n\t"
      "ds_read_b64_tr_b16 v[60:61], %[w1] offset:49920\n\t"
      "v_mfma_f32_16x16x32_bf16 a[12:15], v[36:39], v[116:119], a[12:15]\n\t"
      "ds_read_b64_tr_b16 v[62:63], %[w1] offset:50944\n\t"
      "s_waitcnt lgkmcnt(10)\n\t"
      "v_mfma_f32_16x16x32_bf16 a[16:19], v[40:43], v[112:115], a[16:19]\n\t"
      "ds_read_b64_tr_b16 v[32:33], %[w0] offset:57344\n\t"
      "v_mfma_f32_16x16x32_bf16 a[20:23], v[40:43], v[116:119], a[20:23]\n\t"
      "ds_read_b64_tr_b16 v[34:35], %[w0] offset:58368\n\t"
      "s_waitcnt lgkmcnt(10)\n\t"
      "v_mfma_f32_16x16x32_bf16 a[24:27], v[44:47], v[112:115], a[24:27]\n\t"
      "ds_read_b64_tr_b16 v[36:37], %[w1] offset:57344\n\t"
      "v_mfma_f32_16x16x32_bf16 a[28:31], v[44:47], v[116:119], a[28:31]\n\t"
      "ds_read_b64_tr_b16 v[38:39], %[w1] offset:58368\n\t"
      "s_add_u32 %[t0], %[sw], 2097152\n\t"
      "s_add_u32 m0, %[dw], 65536\n\t"
      "s_nop 0\n\t"
      "buffer_load_dwordx4 %[vw], %[rw1], %[t0] offen lds\n\t"
      "s_waitcnt lgkmcnt(10)\n\t"
      "v_mfma_f32_16x16x32_bf16 a[32:35], v[48:51], v[112:115], a[32:35]\n\t"
      "ds_read_b64_tr_b16 v[40:41], %[w0] offset:57600\n\t"
      "v_mfma_f32_16x16x32_bf16 a[36:39], v[48:51], v[116:119], a[36:39]\n\t"
      "ds_read_b64_tr_b16 v[42:43], %[w0] offset:58624\n\t"
      "s_add_u32 %[t0], %[sw], 2359296\n\t"
      "s_add_u32 m0, %[dw], 73728\n\t"
      "s_nop 0\n\t"
      "buffer_load_dwordx4 %[vw], %[rw1], %[t0] offen lds\n\t"
      "s_waitcnt lgkmcnt(10)\n\t"
      "v_mfma_f32_16x16x32_bf16 a[40:43], v[52:55], v[112:115], a[40:43]\n\t"
      "ds_read_b64_tr_b16 v[44:45], %[w1] offset:57600\n\t"
      "v_mfma_f32_16x16x32_bf16 a[44:47], v[52:55], v[116:119], a[44:47]\n\t"
      "ds_read_b64_tr_b16 v[46:47], %[w1] offset:58624\n\t"
      "s_add_u32 %[t0], %[st], 2883584\n\t"
      "s_buffer_load_dword %[ts], %[rw2], %[t0]\n\t"
      "s_add_u32 %[t0], %[t0], 64\n\t"
      "s_buffer_load_dword %[ts], %[rw2], %[t0]\n\t"
      "s_waitcnt lgkmcnt(10)\n\t"
      "v_mfma_f32_16x16x32_bf16 a[48:51], v[56:59], v[112:115], a[48:51]\n\t"
      "ds_read_b64_tr_b16 v[48:49], %[w0] offset:57856\n\t"
      "v_mfma_f32_16x16x32_bf16 a[52:55], v[56:59], v[116:119], a[52:55]\n\t"
      "ds_read_b64_tr_b16 v[50:51], %[w0] offset:58880\n\t"
      "s_waitcnt lgkmcnt(10)\n\t"
      "v_mfma_f32_16x16x32_bf16 a[56:59], v[60:63], v[112:115], a[56:59]\n\t"
      "ds_read_b64_tr_b16 v[52:53], %[w1] offset:57856\n\t"
      "v_mfma_f32_16x16x32_bf16 a[60:63], v[60:63], v[116:119], a[60:63]\n\t"
      "ds_read_b64_tr_b16 v[54:55], %[w1] offset:58880\n\t"
      "s_waitcnt vmcnt(14)\n\t"
      "s_barrier\n\t"
      "s_cmp_lg_u32 %[wid], 0\n\t"
      "s_cbranch_scc1 .Ls0%=\n\t"
      "s_mov_b64 exec, 1\n\t"
      "v_mov_b32 v31, 1\n\t"
      "buffer_atomic_add v31, off, %[rs], %[fh]\n\t"
      "s_mov_b64 exec, -1\n\t"
      ".Ls0%=:\n\t"
      "s_waitcnt lgkmcnt(10)\n\t"
      "v_mfma_f32_16x16x32_bf16 a[0:3], v[32:35], v[120:123], a[0:3]\n\t"
      "ds_read_b64_tr_b16 v[56:57], %[w0] offset:58112\n\t"
      "v_mfma_f32_16x16x32_bf16 a[4:7], v[32:35], v[124:127], a[4:7]\n\t"
      "ds_read_b64_tr_b16 v[58:59], %[w0] offset:59136\n\t"
      "s_waitcnt lgkmcnt(10)\n\t"
      "v_mfma_f32_16x16x32_bf16 a[8:11], v[36:39], v[120:123], a[8:11]\n\t"
      "ds_read_b64_tr_b16 v[60:61], %[w1] offset:58112\n\t"
      "v_mfma_f32_16x16x32_bf16 a[12:15], v[36:39], v[124:127], a[12:15]\n\t"
      "ds_read_b64_tr_b16 v[62:63], %[w1] offset:59136\n\t"
      "s_waitcnt lgkmcnt(10)\n\t"
      "v_mfma_f32_16x16x32_bf16 a[16:19], v[40:43], v[120:123], a[16:19]\n\t"
      "v_xor_b32 %[w0], 0x10000, %[w0]\n\t"
      "v_xor_b32 %[w1], 0x10000, %[w1]\n\t"
      "ds_read_b64_tr_b16 v[32:33], %[w0] offset:0\n\t"
      "v_mfma_f32_16x16x32_bf16 a[20:23], v[40:43], v[124:127], a[20:23]\n\t"
      "ds_read_b64_tr_b16 v[34:35], %[w0] offset:1024\n\t"
      "s_waitcnt lgkmcnt(10)\n\t"
      "v_mfma_f32_16x16x32_bf16 a[24:27], v[44:47], v[120:123], a[24:27]\n\t"
      "ds_read_b64_tr_b16 v[36:37], %[w1] offset:0\n\t"
      "v_mfma_f32_16x16x32_bf16 a[28:31], v[44:47], v[124:127], a[28:31]\n\t"
      "ds_read_b64_tr_b16 v[38:39], %[w1] offset:1024\n\t"
      "s_add_u32 %[t1], %[sc], 4096\n\t"
      "buffer_load_dwordx4 v[96:99], %[vh0], %[rs], %[t1] offen offset:0\n\t"
      "s_waitcnt lgkmcnt(10)\n\t"
      "v_mfma_f32_16x16x32_bf16 a[32:35], v[48:51], v[120:123], a[32:35]\n\t"
      "ds_read_b64_tr_b16 v[40:41], %[w0] offset:256\n\t"
      "v_mfma_f32_16x16x32_bf16 a[36:39], v[48:51], v[124:127], a[36:39]\n\t"
      "ds_read_b64_tr_b16 v[42:43], %[w0] offset:1280\n\t"
      "buffer_load_dwordx4 v[100:103], %[vh1], %[rs], %[t1] offen offset:0\n\t"
      "s_waitcnt lgkmcnt(10)\n\t"
      "v_mfma_f32_16x16x32_bf16 a[40:43], v[52:55], v[120:123], a[40:43]\n\t"
      "ds_read_b64_tr_b16 v[44:45], %[w1] offset:256\n\t"
      "v_mfma_f32_16x16x32_bf16 a[44:47], v[52:55], v[124:127], a[44:47]\n\t"
      "ds_read_b64_tr_b16 v[46:47], %[w1] offset:1280\n\t"
      "buffer_load_dwordx4 v[104:107], %[vh0], %[rs], %[t1] offen offset:1024\n\t"
      "s_waitcnt lgkmcnt(10)\n\t"
      "v_mfma_f32_16x16x32_bf16 a[48:51], v[56:59], v[120:123], a[48:51]\n\t"
      "ds_read_b64_tr_b16 v[48:49], %[w0] offset:512\n\t"
      "v_mfma_f32_16x16x32_bf16 a[52:55], v[56:59], v[124:127], a[52:55]\n\t"
      "ds_read_b64_tr_b16 v[50:51], %[w0] offset:1536\n\t"
      "buffer_load_dwordx4 v[108:111], %[vh1], %[rs], %[t1] offen offset:1024\n\t"
      "s_waitcnt lgkmcnt(10)\n\t"
      "v_mfma_f32_16x16x32_bf16 a[56:59], v[60:63], v[120:123], a[56:59]\n\t"
      "ds_read_b64_tr_b16 v[52:53], %[w1] offset:512\n\t"
      "v_mfma_f32_16x16x32_bf16 a[60:63], v[60:63], v[124:127], a[60:63]\n\t"
      "ds_read_b64_tr_b16 v[54:55], %[w1] offset:1536\n\t"
      "s_add_u32 %[t0], %[st], 3145728\n\t"
      "s_buffer_load_dword %[ts], %[rw2], %[t0]\n\t"
      "s_add_u32 %[t0], %[t0], 64\n\t"
      "s_buffer_load_dword %[ts], %[rw2], %[t0]\n\t"
      "s_waitcnt vmcnt(12)\n\t"
      "s_waitcnt lgkmcnt(10)\n\t"
      "v_mfma_f32_16x16x32_bf16 a[64:67], v[64:67], v[32:35], 0\n\t"
      "ds_read_b64_tr_b16 v[56:57], %[w0] offset:768\n\t"
      "v_mfma_f32_16x16x32_bf16 a[68:71], v[68:71], v[32:35], 0\n\t"
      "ds_read_b64_tr_b16 v[58:59], %[w0] offset:1792\n\t"
      "s_waitcnt lgkmcnt(10)\n\t"
      "v_mfma_f32_16x16x32_bf16 a[72:75], v[64:67], v[36:39], 0\n\t"
      "ds_read_b64_tr_b16 v[60:61], %[w1] offset:768\n\t"
      "v_accvgpr_read_b32 v16, a0\n\t"
      "v_accvgpr_read_b32 v17, a1\n\t"
      "v_mfma_f32_16x16x32_bf16 a[76:79], v[68:71], v[36:39], 0\n\t"
      "ds_read_b64_tr_b16 v[62:63], %[w1] offset:1792\n\t"
      "s_waitcnt lgkmcnt(10)\n\t"
      "v_mfma_f32_16x16x32_bf16 a[80:83], v[64:67], v[40:43], 0\n\t"
      "ds_read_b64_tr_b16 v[32:33], %[w0] offset:8192\n\t"
      "v_accvgpr_read_b32 v18, a2\n\t"
      "v_accvgpr_read_b32 v19, a3\n\t"
      "v_mfma_f32_16x16x32_bf16 a[84:87], v[68:71], v[40:43], 0\n\t"
      "ds_read_b64_tr_b16 v[34:35], %[w0] offset:9216\n\t"
      "s_waitcnt lgkmcnt(10)\n\t"
      "v_mfma_f32_16x16x32_bf16 a[88:91], v[64:67], v[44:47], 0\n\t"
      "ds_read_b64_tr_b16 v[36:37], %[w1] offset:8192\n\t"
      "v_accvgpr_read_b32 v20, a8\n\t"
      "v_accvgpr_read_b32 v21, a9\n\t"
      "v_mfma_f32_16x16x32_bf16 a[92:95], v[68:71], v[44:47], 0\n\t"
      "ds_read_b64_tr_b16 v[38:39], %[w1] offset:9216\n\t"
      "s_waitcnt lgkmcnt(10)\n\t"
      "v_mfma_f32_16x16x32_bf16 a[96:99], v[64:67], v[48:51], 0\n\t"
      "ds_read_b64_tr_b16 v[40:41], %[w0] offset:8448\n\t"
      "v_accvgpr_read_b32 v22, a10\n\t"
      "v_accvgpr_read_b32 v23, a11\n\t"
      "v_mfma_f32_16x16x32_bf16 a[100:103], v[68:71], v[48:51], 0\n\t"
      "ds_read_b64_tr_b16 v[42:43], %[w0] offset:9472\n\t"
      "s_waitcnt lgkmcnt(10)\n\t"
      "v_mfma_f32_16x16x32_bf16 a[104:107], v[64:67], v[52:55], 0\n\t"
      "ds_read_b64_tr_b16 v[44:45], %[w1] offset:8448\n\t"
      "v_cvt_pk_bf16_f32 v16, v16, v17\n\t"
      "v_cvt_pk_bf16_f32 v17, v18, v19\n\t"
      "v_mfma_f32_16x16x32_bf16 a[108:111], v[68:71], v[52:55], 0\n\t"
      "ds_read_b64_tr_b16 v[46:47], %[w1] offset:9472\n\t"
      "s_waitcnt lgkmcnt(10)\n\t"
      "v_mfma_f32_16x16x32_bf16 a[112:115], v[64:67], v[56:59], 0\n\t"
      "ds_read_b64_tr_b16 v[48:49], %[w0] offset:8704\n\t"
      "v_cvt_pk_bf16_f32 v18, v20, v21\n\t"
      "v_cvt_pk_bf16_f32 v19, v22, v23\n\t"
      "s_add_u32 %[t0], %[so0], 0\n\t"
      "buffer_store_dwordx4 v[16:19], %[vh0], %[rs], %[t0] offen sc1\n\t"
      "v_mfma_f32_16x16x32_bf16 a[116:119], v[68:71], v[56:59], 0\n\t"
      "ds_read_b64_tr_b16 v[50:51], %[w0] offset:9728\n\t"
      "s_add_u32 %[t0], %[sw], 2621440\n\t"
      "s_add_u32 m0, %[dw], 81920\n\t"
      "s_nop 0\n\t"
      "buffer_load_dwordx4 %[vw], %[rw1], %[t0] offen lds\n\t"
      "s_waitcnt lgkmcnt(10)\n\t"
      "v_mfma_f32_16x16x32_bf16 a[120:123], v[64:67], v[60:63], 0\n\t"
      "ds_read_b64_tr_b16 v[52:53], %[w1] offset:8704\n\t"
      "s_add_u32 %[t0], %[sw], 2883584\n\t"
      "s_add_u32 m0, %[dw], 90112\n\t"
      "s_nop 0\n\t"
      "buffer_load_dwordx4 %[vw], %[rw1], %[t0] offen lds\n\t"
      "v_mfma_f32_16x16x32_bf16 a[124:127], v[68:71], v[60:63], 0\n\t"
      "ds_read_b64_tr_b16 v[54:55], %[w1] offset:9728\n\t"
      "s_add_u32 %[t0], %[st], 3407872\n\t"
      "s_buffer_load_dword %[ts], %[rw2], %[t0]\n\t"
      "s_add_u32 %[t0], %[t0], 64\n\t"
      "s_buffer_load_dword %[ts], %[rw2], %[t0]\n\t"
      "s_waitcnt vmcnt(26)\n\t"
      "s_barrier\n\t"
      "s_waitcnt lgkmcnt(10)\n\t"
      "v_mfma_f32_16x16x32_bf16 a[64:67], v[72:75], v[32:35], a[64:67]\n\t"
      "ds_read_b64_tr_b16 v[56:57], %[w0] offset:8960\n\t"
      "v_mfma_f32_16x16x32_bf16 a[68:71], v[76:79], v[32:35], a[68:71]\n\t"
      "ds_read_b64_tr_b16 v[58:59], %[w0] offset:9984\n\t"
      "s_waitcnt lgkmcnt(10)\n\t"
      "v_mfma_f32_16x16x32_bf16 a[72:75], v[72:75], v[36:39], a[72:75]\n\t"
      "ds_read_b64_tr_b16 v[60:61], %[w1] offset:8960\n\t"
      "v_mfma_f32_16x16x32_bf16 a[76:79], v[76:79], v[36:39], a[76:79]\n\t"
      "ds_read_b64_tr_b16 v[62:63], %[w1] offset:9984\n\t"
      "s_waitcnt lgkmcnt(10)\n\t"
      "v_mfma_f32_16x16x32_bf16 a[80:83], v[72:75], v[40:43], a[80:83]\n\t"
      "ds_read_b64_tr_b16 v[32:33], %[w0] offset:16384\n\t"
      "v_mfma_f32_16x16x32_bf16 a[84:87], v[76:79], v[40:43], a[84:87]\n\t"
      "ds_read_b64_tr_b16 v[34:35], %[w0] offset:17408\n\t"
      "s_waitcnt lgkmcnt(10)\n\t"
      "v_mfma_f32_16x16x32_bf16 a[88:91], v[72:75], v[44:47], a[88:91]\n\t"
      "ds_read_b64_tr_b16 v[36:37], %[w1] offset:16384\n\t"
      "v_mfma_f32_16x16x32_bf16 a[92:95], v[76:79], v[44:47], a[92:95]\n\t"
      "ds_read_b64_tr_b16 v[38:39], %[w1] offset:17408\n\t"
      "s_add_u32 %[t1], %[sc], 6144\n\t"
      "buffer_load_dwordx4 v[112:115], %[vh0], %[rs], %[t1] offen offset:0\n\t"
      "s_waitcnt lgkmcnt(10)\n\t"
      "v_mfma_f32_16x16x32_bf16 a[96:99], v[72:75], v[48:51], a[96:99]\n\t"
      "ds_read_b64_tr_b16 v[40:41], %[w0] offset:16640\n\t"
      "v_mfma_f32_16x16x32_bf16 a[100:103], v[76:79], v[48:51], a[100:103]\n\t"
      "ds_read_b64_tr_b16 v[42:43], %[w0] offset:17664\n\t"
      "buffer_load_dwordx4 v[116:119], %[vh1], %[rs], %[t1] offen offset:0\n\t"
      "s_waitcnt lgkmcnt(10)\n\t"
      "v_mfma_f32_16x16x32_bf16 a[104:107], v[72:75], v[52:55], a[104:107]\n\t"
      "ds_read_b64_tr_b16 v[44:45], %[w1] offset:16640\n\t"
      "v_mfma_f32_16x16x32_bf16 a[108:111], v[76:79], v[52:55], a[108:111]\n\t"
      "ds_read_b64_tr_b16 v[46:47], %[w1] offset:17664\n\t"
      "buffer_load_dwordx4 v[120:123], %[vh0], %[rs], %[t1] offen offset:1024\n\t"
      "s_waitcnt lgkmcnt(10)\n\t"
      "v_mfma_f32_16x16x32_bf16 a[112:115], v[72:75], v[56:59], a[112:115]\n\t"
      "ds_read_b64_tr_b16 v[48:49], %[w0] offset:16896\n\t"
      "v_mfma_f32_16x16x32_bf16 a[116:119], v[76:79], v[56:59], a[116:119]\n\t"
      "ds_read_b64_tr_b16 v[50:51], %[w0] offset:17920\n\t"
      "buffer_load_dwordx4 v[124:127], %[vh1], %[rs], %[t1] offen offset:1024\n\t"
      "s_waitcnt lgkmcnt(10)\n\t"
      "v_mfma_f32_16x16x32_bf16 a[120:123], v[72:75], v[60:63], a[120:123]\n\t"
      "ds_read_b64_tr_b16 v[52:53], %[w1] offset:16896\n\t"
      "v_mfma_f32_16x16x32_bf16 a[124:127], v[76:79], v[60:63], a[124:127]\n\t"
      "ds_read_b64_tr_b16 v[54:55], %[w1] offset:17920\n\t"
      "s_add_u32 %[t0], %[st], 3670016\n\t"
      "s_buffer_load_dword %[ts], %[rw2], %[t0]\n\t"
      "s_add_u32 %[t0], %[t0], 64\n\t"
      "s_buffer_load_dword %[ts], %[rw2], %[t0]\n\t"
      "s_waitcnt vmcnt(13)\n\t"
      "s_waitcnt lgkmcnt(10)\n\t"
      "v_mfma_f32_16x16x32_bf16 a[64:67], v[80:83], v[32:35], a[64:67]\n\t"
      "ds_read_b64_tr_b16 v[56:57], %[w0] offset:17152\n\t"
      "v_mfma_f32_16x16x32_bf16 a[68:71], v[84:87], v[32:35], a[68:71]\n\t"
      "ds_read_b64_tr_b16 v[58:59], %[w0] offset:18176\n\t"
      "s_waitcnt lgkmcnt(10)\n\t"
      "v_mfma_f32_16x16x32_bf16 a[72:75], v[80:83], v[36:39], a[72:75]\n\t"
      "ds_read_b64_tr_b16 v[60:61], %[w1] offset:17152\n\t"
      "v_accvgpr_read_b32 v24, a16\n\t"
      "v_accvgpr_read_b32 v25, a17\n\t"
      "v_mfma_f32_16x16x32_bf16 a[76:79], v[84:87], v[36:39], a[76:79]\n\t"
      "ds_read_b64_tr_b16 v[62:63], %[w1] offset:18176\n\t"
      "s_waitcnt lgkmcnt(10)\n\t"
      "v_mfma_f32_16x16x32_bf16 a[80:83], v[80:83], v[40:43], a[80:83]\n\t"
      "ds_read_b64_tr_b16 v[32:33], %[w0] offset:24576\n\t"
      "v_accvgpr_read_b32 v26, a18\n\t"
      "v_accvgpr_read_b32 v27, a19\n\t"
      "v_mfma_f32_16x16x32_bf16 a[84:87], v[84:87], v[40:43], a[84:87]\n\t"
      "ds_read_b64_tr_b16 v[34:35], %[w0] offset:25600\n\t"
      "s_waitcnt lgkmcnt(10)\n\t"
      "v_mfma_f32_16x16x32_bf16 a[88:91], v[80:83], v[44:47], a[88:91]\n\t"
      "ds_read_b64_tr_b16 v[36:37], %[w1] offset:24576\n\t"
      "v_accvgpr_read_b32 v28, a24\n\t"
      "v_accvgpr_read_b32 v29, a25\n\t"
      "v_mfma_f32_16x16x32_bf16 a[92:95], v[84:87], v[44:47], a[92:95]\n\t"
      "ds_read_b64_tr_b16 v[38:39], %[w1] offset:25600\n\t"
      "s_waitcnt lgkmcnt(10)\n\t"
      "v_mfma_f32_16x16x32_bf16 a[96:99], v[80:83], v[48:51], a[96:99]\n\t"
      "ds_read_b64_tr_b16 v[40:41], %[w0] offset:24832\n\t"
      "v_accvgpr_read_b32 v30, a26\n\t"
      "v_accvgpr_read_b32 v31, a27\n\t"
      "v_mfma_f32_16x16x32_bf16 a[100:103], v[84:87], v[48:51], a[100:103]\n\t"
      "ds_read_b64_tr_b16 v[42:43], %[w0] offset:25856\n\t"
      "s_waitcnt lgkmcnt(10)\n\t"
      "v_mfma_f32_16x16x32_bf16 a[104:107], v[80:83], v[52:55], a[104:107]\n\t"
      "ds_read_b64_tr_b16 v[44:45], %[w1] offset:24832\n\t"
      "v_cvt_pk_bf16_f32 v24, v24, v25\n\t"
      "v_cvt_pk_bf16_f32 v25, v26, v27\n\t"
      "v_mfma_f32_16x16x32_bf16 a[108:111], v[84:87], v[52:55], a[108:111]\n\t"
      "ds_read_b64_tr_b16 v[46:47], %[w1] offset:25856\n\t"
      "s_waitcnt lgkmcnt(10)\n\t"
      "v_mfma_f32_16x16x32_bf16 a[112:115], v[80:83], v[56:59], a[112:115]\n\t"
      "ds_read_b64_tr_b16 v[48:49], %[w0] offset:25088\n\t"
      "v_cvt_pk_bf16_f32 v26, v28, v29\n\t"
      "v_cvt_pk_bf16_f32 v27, v30, v31\n\t"
      "s_add_u32 %[t0], %[so0], 1024\n\t"
      "buffer_store_dwordx4 v[24:27], %[vh0], %[rs], %[t0] offen sc1\n\t"
      "v_mfma_f32_16x16x32_bf16 a[116:119], v[84:87], v[56:59], a[116:119]\n\t"
      "ds_read_b64_tr_b16 v[50:51], %[w0] offset:26112\n\t"
      "s_add_u32 %[t0], %[sw], 3145728\n\t"
      "s_add_u32 m0, %[dw], 98304\n\t"
      "s_nop 0\n\t"
      "buffer_load_dwordx4 %[vw], %[rw1], %[t0] offen lds\n\t"
      "s_waitcnt lgkmcnt(10)\n\t"
      "v_mfma_f32_16x16x32_bf16 a[120:123], v[80:83], v[60:63], a[120:123]\n\t"
      "ds_read_b64_tr_b16 v[52:53], %[w1] offset:25088\n\t"
      "s_add_u32 %[t0], %[sw], 3407872\n\t"
      "s_add_u32 m0, %[dw], 106496\n\t"
      "s_nop 0\n\t"
      "buffer_load_dwordx4 %[vw], %[rw1], %[t0] offen lds\n\t"
      "v_mfma_f32_16x16x32_bf16 a[124:127], v[84:87], v[60:63], a[124:127]\n\t"
      "ds_read_b64_tr_b16 v[54:55], %[w1] offset:26112\n\t"
      "s_add_u32 %[t0], %[st], 3932160\n\t"
      "s_buffer_load_dword %[ts], %[rw2], %[t0]\n\t"
      "s_add_u32 %[t0], %[t0], 64\n\t"
      "s_buffer_load_dword %[ts], %[rw2], %[t0]\n\t"
      "s_waitcnt vmcnt(26)\n\t"
      "s_barrier\n\t"
      "s_waitcnt lgkmcnt(10)\n\t"
      "v_mfma_f32_16x16x32_bf16 a[64:67], v[88:91], v[32:35], a[64:67]\n\t"
      "ds_read_b64_tr_b16 v[56:57], %[w0] offset:25344\n\t"
      "v_mfma_f32_16x16x32_bf16 a[68:71], v[92:95], v[32:35], a[68:71]\n\t"
      "ds_read_b64_tr_b16 v[58:59], %[w0] offset:26368\n\t"
      "s_waitcnt lgkmcnt(10)\n\t"
      "v_mfma_f32_16x16x32_bf16 a[72:75], v[88:91], v[36:39], a[72:75]\n\t"
      "ds_read_b64_tr_b16 v[60:61], %[w1] offset:25344\n\t"
      "v_mfma_f32_16x16x32_bf16 a[76:79], v[92:95], v[36:39], a[76:79]\n\t"
      "ds_read_b64_tr_b16 v[62:63], %[w1] offset:26368\n\t"
      "s_waitcnt lgkmcnt(10)\n\t"
      "v_mfma_f32_16x16x32_bf16 a[80:83], v[88:91], v[40:43], a[80:83]\n\t"
      "ds_read_b64_tr_b16 v[32:33], %[w0] offset:32768\n\t"
      "v_mfma_f32_16x16x32_bf16 a[84:87], v[92:95], v[40:43], a[84:87]\n\t"
      "ds_read_b64_tr_b16 v[34:35], %[w0] offset:33792\n\t"
      "s_waitcnt lgkmcnt(10)\n\t"
      "v_mfma_f32_16x16x32_bf16 a[88:91], v[88:91], v[44:47], a[88:91]\n\t"
      "ds_read_b64_tr_b16 v[36:37], %[w1] offset:32768\n\t"
      "v_mfma_f32_16x16x32_bf16 a[92:95], v[92:95], v[44:47], a[92:95]\n\t"
      "ds_read_b64_tr_b16 v[38:39], %[w1] offset:33792\n\t"
      "s_add_u32 %[t1], %[sc], 8192\n\t"
      "buffer_load_dwordx4 v[64:67], %[vh0], %[rs], %[t1] offen offset:0\n\t"
      "s_waitcnt lgkmcnt(10)\n\t"
      "v_mfma_f32_16x16x32_bf16 a[96:99], v[88:91], v[48:51], a[96:99]\n\t"
      "ds_read_b64_tr_b16 v[40:41], %[w0] offset:33024\n\t"
      "v_mfma_f32_16x16x32_bf16 a[100:103], v[92:95], v[48:51], a[100:103]\n\t"
      "ds_read_b64_tr_b16 v[42:43], %[w0] offset:34048\n\t"
      "buffer_load_dwordx4 v[68:71], %[vh1], %[rs], %[t1] offen offset:0\n\t"
      "s_waitcnt lgkmcnt(10)\n\t"
      "v_mfma_f32_16x16x32_bf16 a[104:107], v[88:91], v[52:55], a[104:107]\n\t"
      "ds_read_b64_tr_b16 v[44:45], %[w1] offset:33024\n\t"
      "v_mfma_f32_16x16x32_bf16 a[108:111], v[92:95], v[52:55], a[108:111]\n\t"
      "ds_read_b64_tr_b16 v[46:47], %[w1] offset:34048\n\t"
      "buffer_load_dwordx4 v[72:75], %[vh0], %[rs], %[t1] offen offset:1024\n\t"
      "s_waitcnt lgkmcnt(10)\n\t"
      "v_mfma_f32_16x16x32_bf16 a[112:115], v[88:91], v[56:59], a[112:115]\n\t"
      "ds_read_b64_tr_b16 v[48:49], %[w0] offset:33280\n\t"
      "v_mfma_f32_16x16x32_bf16 a[116:119], v[92:95], v[56:59], a[116:119]\n\t"
      "ds_read_b64_tr_b16 v[50:51], %[w0] offset:34304\n\t"
      "buffer_load_dwordx4 v[76:79], %[vh1], %[rs], %[t1] offen offset:1024\n\t"
      "s_waitcnt lgkmcnt(10)\n\t"
      "v_mfma_f32_16x16x32_bf16 a[120:123], v[88:91], v[60:63], a[120:123]\n\t"
      "ds_read_b64_tr_b16 v[52:53], %[w1] offset:33280\n\t"
      "v_mfma_f32_16x16x32_bf16 a[124:127], v[92:95], v[60:63], a[124:127]\n\t"
      "ds_read_b64_tr_b16 v[54:55], %[w1] offset:34304\n\t"
      "s_waitcnt vmcnt(14)\n\t"
      "s_waitcnt lgkmcnt(10)\n\t"
      "v_mfma_f32_16x16x32_bf16 a[64:67], v[96:99], v[32:35], a[64:67]\n\t"
      "ds_read_b64_tr_b16 v[56:57], %[w0] offset:33536\n\t"
      "v_mfma_f32_16x16x32_bf16 a[68:71], v[100:103], v[32:35], a[68:71]\n\t"
      "ds_read_b64_tr_b16 v[58:59], %[w0] offset:34560\n\t"
      "s_waitcnt lgkmcnt(10)\n\t"
      "v_mfma_f32_16x16x32_bf16 a[72:75], v[96:99], v[36:39], a[72:75]\n\t"
      "ds_read_b64_tr_b16 v[60:61], %[w1] offset:33536\n\t"
      "v_accvgpr_read_b32 v16, a32\n\t"
      "v_accvgpr_read_b32 v17, a33\n\t"
      "v_mfma_f32_16x16x32_bf16 a[76:79], v[100:103], v[36:39], a[76:79]\n\t"
      "ds_read_b64_tr_b16 v[62:63], %[w1] offset:34560\n\t"
      "s_waitcnt lgkmcnt(10)\n\t"
      "v_mfma_f32_16x16x32_bf16 a[80:83], v[96:99], v[40:43], a[80:83]\n\t"
      "ds_read_b64_tr_b16 v[32:33], %[w0] offset:40960\n\t"
      "v_accvgpr_read_b32 v18, a34\n\t"
      "v_accvgpr_read_b32 v19, a35\n\t"
      "v_mfma_f32_16x16x32_bf16 a[84:87], v[100:103], v[40:43], a[84:87]\n\t"
      "ds_read_b64_tr_b16 v[34:35], %[w0] offset:41984\n\t"
      "s_waitcnt lgkmcnt(10)\n\t"
      "v_mfma_f32_16x16x32_bf16 a[88:91], v[96:99], v[44:47], a[88:91]\n\t"
      "ds_read_b64_tr_b16 v[36:37], %[w1] offset:40960\n\t"
      "v_accvgpr_read_b32 v20, a40\n\t"
      "v_accvgpr_read_b32 v21, a41\n\t"
      "v_mfma_f32_16x16x32_bf16 a[92:95], v[100:103], v[44:47], a[92:95]\n\t"
      "ds_read_b64_tr_b16 v[38:39], %[w1] offset:41984\n\t"
      "s_waitcnt lgkmcnt(10)\n\t"
      "v_mfma_f32_16x16x32_bf16 a[96:99], v[96:99], v[48:51], a[96:99]\n\t"
      "ds_read_b64_tr_b16 v[40:41], %[w0] offset:41216\n\t"
      "v_accvgpr_read_b32 v22, a42\n\t"
      "v_accvgpr_read_b32 v23, a43\n\t"
      "v_mfma_f32_16x16x32_bf16 a[100:103], v[100:103], v[48:51], a[100:103]\n\t"
      "ds_read_b64_tr_b16 v[42:43], %[w0] offset:42240\n\t"
      "s_waitcnt lgkmcnt(10)\n\t"
      "v_mfma_f32_16x16x32_bf16 a[104:107], v[96:99], v[52:55], a[104:107]\n\t"
      "ds_read_b64_tr_b16 v[44:45], %[w1] offset:41216\n\t"
      "v_cvt_pk_bf16_f32 v16, v16, v17\n\t"
      "v_cvt_pk_bf16_f32 v17, v18, v19\n\t"
      "v_mfma_f32_16x16x32_bf16 a[108:111], v[100:103], v[52:55], a[108:111]\n\t"
      "ds_read_b64_tr_b16 v[46:47], %[w1] offset:42240\n\t"
      "s_waitcnt lgkmcnt(10)\n\t"
      "v_mfma_f32_16x16x32_bf16 a[112:115], v[96:99], v[56:59], a[112:115]\n\t"
      "ds_read_b64_tr_b16 v[48:49], %[w0] offset:41472\n\t"
      "v_cvt_pk_bf16_f32 v18, v20, v21\n\t"
      "v_cvt_pk_bf16_f32 v19, v22, v23\n\t"
      "s_add_u32 %[t0], %[so0], 2048\n\t"
      "buffer_store_dwordx4 v[16:19], %[vh0], %[rs], %[t0] offen sc1\n\t"
      "v_mfma_f32_16x16x32_bf16 a[116:119], v[100:103], v[56:59], a[116:119]\n\t"
      "ds_read_b64_tr_b16 v[50:51], %[w0] offset:42496\n\t"
      "s_add_u32 %[t0], %[sw], 3670016\n\t"
      "s_add_u32 m0, %[dw], 114688\n\t"
      "s_nop 0\n\t"
      "buffer_load_dwordx4 %[vw], %[rw1], %[t0] offen lds\n\t"
      "s_waitcnt lgkmcnt(10)\n\t"
      "v_mfma_f32_16x16x32_bf16 a[120:123], v[96:99], v[60:63], a[120:123]\n\t"
      "ds_read_b64_tr_b16 v[52:53], %[w1] offset:41472\n\t"
      "v_mfma_f32_16x16x32_bf16 a[124:127], v[100:103], v[60:63], a[124:127]\n\t"
      "ds_read_b64_tr_b16 v[54:55], %[w1] offset:42496\n\t"
      "s_add_u32 %[t0], %[sw], 3932160\n\t"
      "s_add_u32 m0, %[dw], 122880\n\t"
      "s_nop 0\n\t"
      "buffer_load_dwordx4 %[vw], %[rw1], %[t0] offen lds\n\t"
      "s_waitcnt vmcnt(27)\n\t"
      "s_barrier\n\t"
      "s_waitcnt lgkmcnt(10)\n\t"
      "v_mfma_f32_16x16x32_bf16 a[64:67], v[104:107], v[32:35], a[64:67]\n\t"
      "ds_read_b64_tr_b16 v[56:57], %[w0] offset:41728\n\t"
      "v_mfma_f32_16x16x32_bf16 a[68:71], v[108:111], v[32:35], a[68:71]\n\t"
      "ds_read_b64_tr_b16 v[58:59], %[w0] offset:42752\n\t"
      "s_waitcnt lgkmcnt(10)\n\t"
      "v_mfma_f32_16x16x32_bf16 a[72:75], v[104:107], v[36:39], a[72:75]\n\t"
      "ds_read_b64_tr_b16 v[60:61], %[w1] offset:41728\n\t"
      "v_mfma_f32_16x16x32_bf16 a[76:79], v[108:111], v[36:39], a[76:79]\n\t"
      "ds_read_b64_tr_b16 v[62:63], %[w1] offset:42752\n\t"
      "s_waitcnt lgkmcnt(10)\n\t"
      "v_mfma_f32_16x16x32_bf16 a[80:83], v[104:107], v[40:43], a[80:83]\n\t"
      "ds_read_b64_tr_b16 v[32:33], %[w0] offset:49152\n\t"
      "v_mfma_f32_16x16x32_bf16 a[84:87], v[108:111], v[40:43], a[84:87]\n\t"
      "ds_read_b64_tr_b16 v[34:35], %[w0] offset:50176\n\t"
      "s_waitcnt lgkmcnt(10)\n\t"
      "v_mfma_f32_16x16x32_bf16 a[88:91], v[104:107], v[44:47], a[88:91]\n\t"
      "ds_read_b64_tr_b16 v[36:37], %[w1] offset:49152\n\t"
      "v_mfma_f32_16x16x32_bf16 a[92:95], v[108:111], v[44:47], a[92:95]\n\t"
      "ds_read_b64_tr_b16 v[38:39], %[w1] offset:50176\n\t"
      "s_add_u32 %[t1], %[sc], 10240\n\t"
      "buffer_load_dwordx4 v[80:83], %[vh0], %[rs], %[t1] offen offset:0\n\t"
      "s_waitcnt lgkmcnt(10)\n\t"
      "v_mfma_f32_16x16x32_bf16 a[96:99], v[104:107], v[48:51], a[96:99]\n\t"
      "ds_read_b64_tr_b16 v[40:41], %[w0] offset:49408\n\t"
      "v_mfma_f32_16x16x32_bf16 a[100:103], v[108:111], v[48:51], a[100:103]\n\t"
      "ds_read_b64_tr_b16 v[42:43], %[w0] offset:50432\n\t"
      "buffer_load_dwordx4 v[84:87], %[vh1], %[rs], %[t1] offen offset:0\n\t"
      "s_waitcnt lgkmcnt(10)\n\t"
      "v_mfma_f32_16x16x32_bf16 a[104:107], v[104:107], v[52:55], a[104:107]\n\t"
      "ds_read_b64_tr_b16 v[44:45], %[w1] offset:49408\n\t"
      "v_mfma_f32_16x16x32_bf16 a[108:111], v[108:111], v[52:55], a[108:111]\n\t"
      "ds_read_b64_tr_b16 v[46:47], %[w1] offset:50432\n\t"
      "buffer_load_dwordx4 v[88:91], %[vh0], %[rs], %[t1] offen offset:1024\n\t"
      "s_waitcnt lgkmcnt(10)\n\t"
      "v_mfma_f32_16x16x32_bf16 a[112:115], v[104:107], v[56:59], a[112:115]\n\t"
      "ds_read_b64_tr_b16 v[48:49], %[w0] offset:49664\n\t"
      "v_mfma_f32_16x16x32_bf16 a[116:119], v[108:111], v[56:59], a[116:119]\n\t"
      "ds_read_b64_tr_b16 v[50:51], %[w0] offset:50688\n\t"
      "buffer_load_dwordx4 v[92:95], %[vh1], %[rs], %[t1] offen offset:1024\n\t"
      "s_waitcnt lgkmcnt(10)\n\t"
      "v_mfma_f32_16x16x32_bf16 a[120:123], v[104:107], v[60:63], a[120:123]\n\t"
      "ds_read_b64_tr_b16 v[52:53], %[w1] offset:49664\n\t"
      "v_mfma_f32_16x16x32_bf16 a[124:127], v[108:111], v[60:63], a[124:127]\n\t"
      "ds_read_b64_tr_b16 v[54:55], %[w1] offset:50688\n\t"
      "s_waitcnt vmcnt(14)\n\t"
      "s_waitcnt lgkmcnt(10)\n\t"
      "v_mfma_f32_16x16x32_bf16 a[64:67], v[112:115], v[32:35], a[64:67]\n\t"
      "ds_read_b64_tr_b16 v[56:57], %[w0] offset:49920\n\t"
      "v_mfma_f32_16x16x32_bf16 a[68:71], v[116:119], v[32:35], a[68:71]\n\t"
      "ds_read_b64_tr_b16 v[58:59], %[w0] offset:50944\n\t"
      "s_waitcnt lgkmcnt(10)\n\t"
      "v_mfma_f32_16x16x32_bf16 a[72:75], v[112:115], v[36:39], a[72:75]\n\t"
      "ds_read_b64_tr_b16 v[60:61], %[w1] offset:49920\n\t"
      "v_accvgpr_read_b32 v24, a48\n\t"
      "v_accvgpr_read_b32 v25, a49\n\t"
      "v_mfma_f32_16x16x32_bf16 a[76:79], v[116:119], v[36:39], a[76:79]\n\t"
      "ds_read_b64_tr_b16 v[62:63], %[w1] offset:50944\n\t"
      "s_waitcnt lgkmcnt(10)\n\t"
      "v_mfma_f32_16x16x32_bf16 a[80:83], v[112:115], v[40:43], a[80:83]\n\t"
      "ds_read_b64_tr_b16 v[32:33], %[w0] offset:57344\n\t"
      "v_accvgpr_read_b32 v26, a50\n\t"
      "v_accvgpr_read_b32 v27, a51\n\t"
      "v_mfma_f32_16x16x32_bf16 a[84:87], v[116:119], v[40:43], a[84:87]\n\t"
      "ds_read_b64_tr_b16 v[34:35], %[w0] offset:58368\n\t"
      "s_waitcnt lgkmcnt(10)\n\t"
      "v_mfma_f32_16x16x32_bf16 a[88:91], v[112:115], v[44:47], a[88:91]\n\t"
      "ds_read_b64_tr_b16 v[36:37], %[w1] offset:57344\n\t"
      "v_accvgpr_read_b32 v28, a56\n\t"
      "v_accvgpr_read_b32 v29, a57\n\t"
      "v_mfma_f32_16x16x32_bf16 a[92:95], v[116:119], v[44:47], a[92:95]\n\t"
      "ds_read_b64_tr_b16 v[38:39], %[w1] offset:58368\n\t"
      "s_waitcnt lgkmcnt(10)\n\t"
      "v_mfma_f32_16x16x32_bf16 a[96:99], v[112:115], v[48:51], a[96:99]\n\t"
      "ds_read_b64_tr_b16 v[40:41], %[w0] offset:57600\n\t"
      "v_accvgpr_read_b32 v30, a58\n\t"
      "v_accvgpr_read_b32 v31, a59\n\t"
      "v_mfma_f32_16x16x32_bf16 a[100:103], v[116:119], v[48:51], a[100:103]\n\t"
      "ds_read_b64_tr_b16 v[42:43], %[w0] offset:58624\n\t"
      "s_waitcnt lgkmcnt(10)\n\t"
      "v_mfma_f32_16x16x32_bf16 a[104:107], v[112:115], v[52:55], a[104:107]\n\t"
      "ds_read_b64_tr_b16 v[44:45], %[w1] offset:57600\n\t"
      "v_cvt_pk_bf16_f32 v24, v24, v25\n\t"
      "v_cvt_pk_bf16_f32 v25, v26, v27\n\t"
      "v_mfma_f32_16x16x32_bf16 a[108:111], v[116:119], v[52:55], a[108:111]\n\t"
      "ds_read_b64_tr_b16 v[46:47], %[w1] offset:58624\n\t"
      "s_waitcnt lgkmcnt(10)\n\t"
      "v_mfma_f32_16x16x32_bf16 a[112:115], v[112:115], v[56:59], a[112:115]\n\t"
      "ds_read_b64_tr_b16 v[48:49], %[w0] offset:57856\n\t"
      "v_cvt_pk_bf16_f32 v26, v28, v29\n\t"
      "v_cvt_pk_bf16_f32 v27, v30, v31\n\t"
      "s_add_u32 %[t0], %[so0], 3072\n\t"
      "buffer_store_dwordx4 v[24:27], %[vh0], %[rs], %[t0] offen sc1\n\t"
      "v_mfma_f32_16x16x32_bf16 a[116:119], v[116:119], v[56:59], a[116:119]\n\t"
      "ds_read_b64_tr_b16 v[50:51], %[w0] offset:58880\n\t"
      "s_add_u32 %[t0], %[sw], 0\n\t"
      "s_add_u32 m0, %[dw], 0\n\t"
      "s_nop 0\n\t"
      "buffer_load_dwordx4 %[vw], %[rw2], %[t0] offen lds\n\t"
      "s_waitcnt lgkmcnt(10)\n\t"
      "v_mfma_f32_16x16x32_bf16 a[120:123], v[112:115], v[60:63], a[120:123]\n\t"
      "ds_read_b64_tr_b16 v[52:53], %[w1] offset:57856\n\t"
      "v_mfma_f32_16x16x32_bf16 a[124:127], v[116:119], v[60:63], a[124:127]\n\t"
      "ds_read_b64_tr_b16 v[54:55], %[w1] offset:58880\n\t"
      "s_add_u32 %[t0], %[sw], 262144\n\t"
      "s_add_u32 m0, %[dw], 8192\n\t"
      "s_nop 0\n\t"
      "buffer_load_dwordx4 %[vw], %[rw2], %[t0] offen lds\n\t"
      "s_waitcnt vmcnt(28)\n\t"
      "s_barrier\n\t"
      "s_waitcnt lgkmcnt(10)\n\t"
      "v_mfma_f32_16x16x32_bf16 a[64:67], v[120:123], v[32:35], a[64:67]\n\t"
      "ds_read_b64_tr_b16 v[56:57], %[w0] offset:58112\n\t"
      "v_mfma_f32_16x16x32_bf16 a[68:71], v[124:127], v[32:35], a[68:71]\n\t"
      "ds_read_b64_tr_b16 v[58:59], %[w0] offset:59136\n\t"
      "s_waitcnt lgkmcnt(10)\n\t"
      "v_mfma_f32_16x16x32_bf16 a[72:75], v[120:123], v[36:39], a[72:75]\n\t"
      "ds_read_b64_tr_b16 v[60:61], %[w1] offset:58112\n\t"
      "v_mfma_f32_16x16x32_bf16 a[76:79], v[124:127], v[36:39], a[76:79]\n\t"
      "ds_read_b64_tr_b16 v[62:63], %[w1] offset:59136\n\t"
      "s_waitcnt lgkmcnt(10)\n\t"
      "v_mfma_f32_16x16x32_bf16 a[80:83], v[120:123], v[40:43], a[80:83]\n\t"
      "v_xor_b32 %[w0], 0x10000, %[w0]\n\t"
      "v_xor_b32 %[w1], 0x10000, %[w1]\n\t"
      "ds_read_b64_tr_b16 v[32:33], %[w0] offset:0\n\t"
      "v_mfma_f32_16x16x32_bf16 a[84:87], v[124:127], v[40:43], a[84:87]\n\t"
      "ds_read_b64_tr_b16 v[34:35], %[w0] offset:1024\n\t"
      "s_waitcnt lgkmcnt(10)\n\t"
      "v_mfma_f32_16x16x32_bf16 a[88:91], v[120:123], v[44:47], a[88:91]\n\t"
      "ds_read_b64_tr_b16 v[36:37], %[w1] offset:0\n\t"
      "v_mfma_f32_16x16x32_bf16 a[92:95], v[124:127], v[44:47], a[92:95]\n\t"
      "ds_read_b64_tr_b16 v[38:39], %[w1] offset:1024\n\t"
      "s_add_u32 %[t1], %[sc], 12288\n\t"
      "buffer_load_dwordx4 v[96:99], %[vh0], %[rs], %[t1] offen offset:0\n\t"
      "s_waitcnt lgkmcnt(10)\n\t"
      "v_mfma_f32_16x16x32_bf16 a[96:99], v[120:123], v[48:51], a[96:99]\n\t"
      "ds_read_b64_tr_b16 v[40:41], %[w0] offset:256\n\t"
      "v_mfma_f32_16x16x32_bf16 a[100:103], v[124:127], v[48:51], a[100:103]\n\t"
      "ds_read_b64_tr_b16 v[42:43], %[w0] offset:1280\n\t"
      "buffer_load_dwordx4 v[100:103], %[vh1], %[rs], %[t1] offen offset:0\n\t"
      "s_waitcnt lgkmcnt(10)\n\t"
      "v_mfma_f32_16x16x32_bf16 a[104:107], v[120:123], v[52:55], a[104:107]\n\t"
      "ds_read_b64_tr_b16 v[44:45], %[w1] offset:256\n\t"
      "v_mfma_f32_16x16x32_bf16 a[108:111], v[124:127], v[52:55], a[108:111]\n\t"
      "ds_read_b64_tr_b16 v[46:47], %[w1] offset:1280\n\t"
      "buffer_load_dwordx4 v[104:107], %[vh0], %[rs], %[t1] offen offset:1024\n\t"
      "s_waitcnt lgkmcnt(10)\n\t"
      "v_mfma_f32_16x16x32_bf16 a[112:115], v[120:123], v[56:59], a[112:115]\n\t"
      "ds_read_b64_tr_b16 v[48:49], %[w0] offset:512\n\t"
      "v_mfma_f32_16x16x32_bf16 a[116:119], v[124:127], v[56:59], a[116:119]\n\t"
      "ds_read_b64_tr_b16 v[50:51], %[w0] offset:1536\n\t"
      "buffer_load_dwordx4 v[108:111], %[vh1], %[rs], %[t1] offen offset:1024\n\t"
      "s_waitcnt lgkmcnt(10)\n\t"
      "v_mfma_f32_16x16x32_bf16 a[120:123], v[120:123], v[60:63], a[120:123]\n\t"
      "ds_read_b64_tr_b16 v[52:53], %[w1] offset:512\n\t"
      "v_mfma_f32_16x16x32_bf16 a[124:127], v[124:127], v[60:63], a[124:127]\n\t"
      "ds_read_b64_tr_b16 v[54:55], %[w1] offset:1536\n\t"
      "s_waitcnt vmcnt(14)\n\t"
      "s_waitcnt lgkmcnt(10)\n\t"
      "v_mfma_f32_16x16x32_bf16 a[64:67], v[64:67], v[32:35], a[64:67]\n\t"
      "ds_read_b64_tr_b16 v[56:57], %[w0] offset:768\n\t"
      "v_mfma_f32_16x16x32_bf16 a[68:71], v[68:71], v[32:35], a[68:71]\n\t"
      "ds_read_b64_tr_b16 v[58:59], %[w0] offset:1792\n\t"
      "s_waitcnt lgkmcnt(10)\n\t"
      "v_mfma_f32_16x16x32_bf16 a[72:75], v[64:67], v[36:39], a[72:75]\n\t"
      "ds_read_b64_tr_b16 v[60:61], %[w1] offset:768\n\t"
      "v_accvgpr_read_b32 v16, a4\n\t"
      "v_accvgpr_read_b32 v17, a5\n\t"
      "v_mfma_f32_16x16x32_bf16 a[76:79], v[68:71], v[36:39], a[76:79]\n\t"
      "ds_read_b64_tr_b16 v[62:63], %[w1] offset:1792\n\t"
      "s_waitcnt lgkmcnt(10)\n\t"
      "v_mfma_f32_16x16x32_bf16 a[80:83], v[64:67], v[40:43], a[80:83]\n\t"
      "ds_read_b64_tr_b16 v[32:33], %[w0] offset:8192\n\t"
      "v_accvgpr_read_b32 v18, a6\n\t"
      "v_accvgpr_read_b32 v19, a7\n\t"
      "v_mfma_f32_16x16x32_bf16 a[84:87], v[68:71], v[40:43], a[84:87]\n\t"
      "ds_read_b64_tr_b16 v[34:35], %[w0] offset:9216\n\t"
      "s_waitcnt lgkmcnt(10)\n\t"
      "v_mfma_f32_16x16x32_bf16 a[88:91], v[64:67], v[44:47], a[88:91]\n\t"
      "ds_read_b64_tr_b16 v[36:37], %[w1] offset:8192\n\t"
      "v_accvgpr_read_b32 v20, a12\n\t"
      "v_accvgpr_read_b32 v21, a13\n\t"
      "v_mfma_f32_16x16x32_bf16 a[92:95], v[68:71], v[44:47], a[92:95]\n\t"
      "ds_read_b64_tr_b16 v[38:39], %[w1] offset:9216\n\t"
      "s_waitcnt lgkmcnt(10)\n\t"
      "v_mfma_f32_16x16x32_bf16 a[96:99], v[64:67], v[48:51], a[96:99]\n\t"
      "ds_read_b64_tr_b16 v[40:41], %[w0] offset:8448\n\t"
      "v_accvgpr_read_b32 v22, a14\n\t"
      "v_accvgpr_read_b32 v23, a15\n\t"
      "v_mfma_f32_16x16x32_bf16 a[100:103], v[68:71], v[48:51], a[100:103]\n\t"
      "ds_read_b64_tr_b16 v[42:43], %[w0] offset:9472\n\t"
      "s_waitcnt lgkmcnt(10)\n\t"
      "v_mfma_f32_16x16x32_bf16 a[104:107], v[64:67], v[52:55], a[104:107]\n\t"
      "ds_read_b64_tr_b16 v[44:45], %[w1] offset:8448\n\t"
      "v_cvt_pk_bf16_f32 v16, v16, v17\n\t"
      "v_cvt_pk_bf16_f32 v17, v18, v19\n\t"
      "v_mfma_f32_16x16x32_bf16 a[108:111], v[68:71], v[52:55], a[108:111]\n\t"
      "ds_read_b64_tr_b16 v[46:47], %[w1] offset:9472\n\t"
      "s_waitcnt lgkmcnt(10)\n\t"
      "v_mfma_f32_16x16x32_bf16 a[112:115], v[64:67], v[56:59], a[112:115]\n\t"
      "ds_read_b64_tr_b16 v[48:49], %[w0] offset:8704\n\t"
      "v_cvt_pk_bf16_f32 v18, v20, v21\n\t"
      "v_cvt_pk_bf16_f32 v19, v22, v23\n\t"
      "s_add_u32 %[t0], %[so0], 4096\n\t"
      "buffer_store_dwordx4 v[16:19], %[vh0], %[rs], %[t0] offen sc1\n\t"
      "v_mfma_f32_16x16x32_bf16 a[116:119], v[68:71], v[56:59], a[116:119]\n\t"
      "ds_read_b64_tr_b16 v[50:51], %[w0] offset:9728\n\t"
      "s_add_u32 %[t0], %[sw], 524288\n\t"
      "s_add_u32 m0, %[dw], 16384\n\t"
      "s_nop 0\n\t"
      "buffer_load_dwordx4 %[vw], %[rw2], %[t0] offen lds\n\t"
      "s_waitcnt lgkmcnt(10)\n\t"
      "v_mfma_f32_16x16x32_bf16 a[120:123], v[64:67], v[60:63], a[120:123]\n\t"
      "ds_read_b64_tr_b16 v[52:53], %[w1] offset:8704\n\t"
      "v_mfma_f32_16x16x32_bf16 a[124:127], v[68:71], v[60:63], a[124:127]\n\t"
      "ds_read_b64_tr_b16 v[54:55], %[w1] offset:9728\n\t"
      "s_add_u32 %[t0], %[sw], 786432\n\t"
      "s_add_u32 m0, %[dw], 24576\n\t"
      "s_nop 0\n\t"
      "buffer_load_dwordx4 %[vw], %[rw2], %[t0] offen lds\n\t"
      "s_waitcnt vmcnt(28)\n\t"
      "s_barrier\n\t"
      "s_waitcnt lgkmcnt(10)\n\t"
      "v_mfma_f32_16x16x32_bf16 a[64:67], v[72:75], v[32:35], a[64:67]\n\t"
      "ds_read_b64_tr_b16 v[56:57], %[w0] offset:8960\n\t"
      "v_mfma_f32_16x16x32_bf16 a[68:71], v[76:79], v[32:35], a[68:71]\n\t"
      "ds_read_b64_tr_b16 v[58:59], %[w0] offset:9984\n\t"
      "s_waitcnt lgkmcnt(10)\n\t"
      "v_mfma_f32_16x16x32_bf16 a[72:75], v[72:75], v[36:39], a[72:75]\n\t"
      "ds_read_b64_tr_b16 v[60:61], %[w1] offset:8960\n\t"
      "v_mfma_f32_16x16x32_bf16 a[76:79], v[76:79], v[36:39], a[76:79]\n\t"
      "ds_read_b64_tr_b16 v[62:63], %[w1] offset:9984\n\t"
      "s_waitcnt lgkmcnt(10)\n\t"
      "v_mfma_f32_16x16x32_bf16 a[80:83], v[72:75], v[40:43], a[80:83]\n\t"
      "ds_read_b64_tr_b16 v[32:33], %[w0] offset:16384\n\t"
      "v_mfma_f32_16x16x32_bf16 a[84:87], v[76:79], v[40:43], a[84:87]\n\t"
      "ds_read_b64_tr_b16 v[34:35], %[w0] offset:17408\n\t"
      "s_waitcnt lgkmcnt(10)\n\t"
      "v_mfma_f32_16x16x32_bf16 a[88:91], v[72:75], v[44:47], a[88:91]\n\t"
      "ds_read_b64_tr_b16 v[36:37], %[w1] offset:16384\n\t"
      "v_mfma_f32_16x16x32_bf16 a[92:95], v[76:79], v[44:47], a[92:95]\n\t"
      "ds_read_b64_tr_b16 v[38:39], %[w1] offset:17408\n\t"
      "buffer_load_dword %[pv], off, %[rs], %[ph] sc1\n\t"
      "s_waitcnt lgkmcnt(10)\n\t"
      "v_mfma_f32_16x16x32_bf16 a[96:99], v[72:75], v[48:51], a[96:99]\n\t"
      "ds_read_b64_tr_b16 v[40:41], %[w0] offset:16640\n\t"
      "v_mfma_f32_16x16x32_bf16 a[100:103], v[76:79], v[48:51], a[100:103]\n\t"
      "ds_read_b64_tr_b16 v[42:43], %[w0] offset:17664\n\t"
      "s_add_u32 %[t1], %[sc], 14336\n\t"
      "buffer_load_dwordx4 v[112:115], %[vh0], %[rs], %[t1] offen offset:0\n\t"
      "s_waitcnt lgkmcnt(10)\n\t"
      "v_mfma_f32_16x16x32_bf16 a[104:107], v[72:75], v[52:55], a[104:107]\n\t"
      "ds_read_b64_tr_b16 v[44:45], %[w1] offset:16640\n\t"
      "v_mfma_f32_16x16x32_bf16 a[108:111], v[76:79], v[52:55], a[108:111]\n\t"
      "ds_read_b64_tr_b16 v[46:47], %[w1] offset:17664\n\t"
      "buffer_load_dwordx4 v[116:119], %[vh1], %[rs], %[t1] offen offset:0\n\t"
      "s_waitcnt lgkmcnt(10)\n\t"
      "v_mfma_f32_16x16x32_bf16 a[112:115], v[72:75], v[56:59], a[112:115]\n\t"
      "ds_read_b64_tr_b16 v[48:49], %[w0] offset:16896\n\t"
      "v_mfma_f32_16x16x32_bf16 a[116:119], v[76:79], v[56:59], a[116:119]\n\t"
      "ds_read_b64_tr_b16 v[50:51], %[w0] offset:17920\n\t"
      "buffer_load_dwordx4 v[120:123], %[vh0], %[rs], %[t1] offen offset:1024\n\t"
      "s_waitcnt lgkmcnt(10)\n\t"
      "v_mfma_f32_16x16x32_bf16 a[120:123], v[72:75], v[60:63], a[120:123]\n\t"
      "ds_read_b64_tr_b16 v[52:53], %[w1] offset:16896\n\t"
      "v_mfma_f32_16x16x32_bf16 a[124:127], v[76:79], v[60:63], a[124:127]\n\t"
      "ds_read_b64_tr_b16 v[54:55], %[w1] offset:17920\n\t"
      "buffer_load_dwordx4 v[124:127], %[vh1], %[rs], %[t1] offen offset:1024\n\t"
      "s_waitcnt vmcnt(15)\n\t"
      "s_waitcnt vmcnt(4)\n\t"
      "v_readfirstlane_b32 %[t0], %[pv]\n\t"
      "s_cmp_lt_u32 %[t0], 16\n\t"
      "s_cbranch_scc1 .Lw1%=\n\t"
      ".Lr1%=:\n\t"
      "s_waitcnt lgkmcnt(10)\n\t"
      "v_mfma_f32_16x16x32_bf16 a[64:67], v[80:83], v[32:35], a[64:67]\n\t"
      "ds_read_b64_tr_b16 v[56:57], %[w0] offset:17152\n\t"
      "v_mfma_f32_16x16x32_bf16 a[68:71], v[84:87], v[32:35], a[68:71]\n\t"
      "ds_read_b64_tr_b16 v[58:59], %[w0] offset:18176\n\t"
      "s_waitcnt lgkmcnt(10)\n\t"
      "v_mfma_f32_16x16x32_bf16 a[72:75], v[80:83], v[36:39], a[72:75]\n\t"
      "ds_read_b64_tr_b16 v[60:61], %[w1] offset:17152\n\t"
      "v_accvgpr_read_b32 v24, a20\n\t"
      "v_accvgpr_read_b32 v25, a21\n\t"
      "v_mfma_f32_16x16x32_bf16 a[76:79], v[84:87], v[36:39], a[76:79]\n\t"
      "ds_read_b64_tr_b16 v[62:63], %[w1] offset:18176\n\t"
      "s_waitcnt lgkmcnt(10)\n\t"
      "v_mfma_f32_16x16x32_bf16 a[80:83], v[80:83], v[40:43], a[80:83]\n\t"
      "ds_read_b64_tr_b16 v[32:33], %[w0] offset:24576\n\t"
      "v_accvgpr_read_b32 v26, a22\n\t"
      "v_accvgpr_read_b32 v27, a23\n\t"
      "v_mfma_f32_16x16x32_bf16 a[84:87], v[84:87], v[40:43], a[84:87]\n\t"
      "ds_read_b64_tr_b16 v[34:35], %[w0] offset:25600\n\t"
      "s_waitcnt lgkmcnt(10)\n\t"
      "v_mfma_f32_16x16x32_bf16 a[88:91], v[80:83], v[44:47], a[88:91]\n\t"
      "ds_read_b64_tr_b16 v[36:37], %[w1] offset:24576\n\t"
      "v_accvgpr_read_b32 v28, a28\n\t"
      "v_accvgpr_read_b32 v29, a29\n\t"
      "v_mfma_f32_16x16x32_bf16 a[92:95], v[84:87], v[44:47], a[92:95]\n\t"
      "ds_read_b64_tr_b16 v[38:39], %[w1] offset:25600\n\t"
      "s_waitcnt lgkmcnt(10)\n\t"
      "v_mfma_f32_16x16x32_bf16 a[96:99], v[80:83], v[48:51], a[96:99]\n\t"
      "ds_read_b64_tr_b16 v[40:41], %[w0] offset:24832\n\t"
      "v_accvgpr_read_b32 v30, a30\n\t"
      "v_accvgpr_read_b32 v31, a31\n\t"
      "v_mfma_f32_16x16x32_bf16 a[100:103], v[84:87], v[48:51], a[100:103]\n\t"
      "ds_read_b64_tr_b16 v[42:43], %[w0] offset:25856\n\t"
      "s_waitcnt lgkmcnt(10)\n\t"
      "v_mfma_f32_16x16x32_bf16 a[104:107], v[80:83], v[52:55], a[104:107]\n\t"
      "ds_read_b64_tr_b16 v[44:45], %[w1] offset:24832\n\t"
      "v_cvt_pk_bf16_f32 v24, v24, v25\n\t"
      "v_cvt_pk_bf16_f32 v25, v26, v27\n\t"
      "v_mfma_f32_16x16x32_bf16 a[108:111], v[84:87], v[52:55], a[108:111]\n\t"
      "ds_read_b64_tr_b16 v[46:47], %[w1] offset:25856\n\t"
      "s_waitcnt lgkmcnt(10)\n\t"
      "v_mfma_f32_16x16x32_bf16 a[112:115], v[80:83], v[56:59], a[112:115]\n\t"
      "ds_read_b64_tr_b16 v[48:49], %[w0] offset:25088\n\t"
      "v_cvt_pk_bf16_f32 v26, v28, v29\n\t"
      "v_cvt_pk_bf16_f32 v27, v30, v31\n\t"
      "s_add_u32 %[t0], %[so0], 5120\n\t"
      "buffer_store_dwordx4 v[24:27], %[vh0], %[rs], %[t0] offen sc1\n\t"
      "v_mfma_f32_16x16x32_bf16 a[116:119], v[84:87], v[56:59], a[116:119]\n\t"
      "ds_read_b64_tr_b16 v[50:51], %[w0] offset:26112\n\t"
      "s_add_u32 %[t0], %[sw], 1048576\n\t"
      "s_add_u32 m0, %[dw], 32768\n\t"
      "s_nop 0\n\t"
      "buffer_load_dwordx4 %[vw], %[rw2], %[t0] offen lds\n\t"
      "s_waitcnt lgkmcnt(10)\n\t"
      "v_mfma_f32_16x16x32_bf16 a[120:123], v[80:83], v[60:63], a[120:123]\n\t"
      "ds_read_b64_tr_b16 v[52:53], %[w1] offset:25088\n\t"
      "v_mfma_f32_16x16x32_bf16 a[124:127], v[84:87], v[60:63], a[124:127]\n\t"
      "ds_read_b64_tr_b16 v[54:55], %[w1] offset:26112\n\t"
      "s_add_u32 %[t0], %[sw], 1310720\n\t"
      "s_add_u32 m0, %[dw], 40960\n\t"
      "s_nop 0\n\t"
      "buffer_load_dwordx4 %[vw], %[rw2], %[t0] offen lds\n\t"
      "s_waitcnt vmcnt(29)\n\t"
      "s_barrier\n\t"
      "s_waitcnt lgkmcnt(10)\n\t"
      "v_mfma_f32_16x16x32_bf16 a[64:67], v[88:91], v[32:35], a[64:67]\n\t"
      "ds_read_b64_tr_b16 v[56:57], %[w0] offset:25344\n\t"
      "v_mfma_f32_16x16x32_bf16 a[68:71], v[92:95], v[32:35], a[68:71]\n\t"
      "ds_read_b64_tr_b16 v[58:59], %[w0] offset:26368\n\t"
      "s_waitcnt lgkmcnt(10)\n\t"
      "v_mfma_f32_16x16x32_bf16 a[72:75], v[88:91], v[36:39], a[72:75]\n\t"
      "ds_read_b64_tr_b16 v[60:61], %[w1] offset:25344\n\t"
      "v_mfma_f32_16x16x32_bf16 a[76:79], v[92:95], v[36:39], a[76:79]\n\t"
      "ds_read_b64_tr_b16 v[62:63], %[w1] offset:26368\n\t"
      "s_waitcnt lgkmcnt(10)\n\t"
      "v_mfma_f32_16x16x32_bf16 a[80:83], v[88:91], v[40:43], a[80:83]\n\t"
      "ds_read_b64_tr_b16 v[32:33], %[w0] offset:32768\n\t"
      "v_mfma_f32_16x16x32_bf16 a[84:87], v[92:95], v[40:43], a[84:87]\n\t"
      "ds_read_b64_tr_b16 v[34:35], %[w0] offset:33792\n\t"
      "s_waitcnt lgkmcnt(10)\n\t"
      "v_mfma_f32_16x16x32_bf16 a[88:91], v[88:91], v[44:47], a[88:91]\n\t"
      "ds_read_b64_tr_b16 v[36:37], %[w1] offset:32768\n\t"
      "v_mfma_f32_16x16x32_bf16 a[92:95], v[92:95], v[44:47], a[92:95]\n\t"
      "ds_read_b64_tr_b16 v[38:39], %[w1] offset:33792\n\t"
      "s_add_u32 %[t1], %[sh], 0\n\t"
      "buffer_load_dwordx4 v[64:67], %[vh0], %[rs], %[t1] offen offset:0 sc1\n\t"
      "s_waitcnt lgkmcnt(10)\n\t"
      "v_mfma_f32_16x16x32_bf16 a[96:99], v[88:91], v[48:51], a[96:99]\n\t"
      "ds_read_b64_tr_b16 v[40:41], %[w0] offset:33024\n\t"
      "v_mfma_f32_16x16x32_bf16 a[100:103], v[92:95], v[48:51], a[100:103]\n\t"
      "ds_read_b64_tr_b16 v[42:43], %[w0] offset:34048\n\t"
      "buffer_load_dwordx4 v[68:71], %[vh1], %[rs], %[t1] offen offset:0 sc1\n\t"
      "s_waitcnt lgkmcnt(10)\n\t"
      "v_mfma_f32_16x16x32_bf16 a[104:107], v[88:91], v[52:55], a[104:107]\n\t"
      "ds_read_b64_tr_b16 v[44:45], %[w1] offset:33024\n\t"
      "v_mfma_f32_16x16x32_bf16 a[108:111], v[92:95], v[52:55], a[108:111]\n\t"
      "ds_read_b64_tr_b16 v[46:47], %[w1] offset:34048\n\t"
      "buffer_load_dwordx4 v[72:75], %[vh0], %[rs], %[t1] offen offset:1024 sc1\n\t"
      "s_waitcnt lgkmcnt(10)\n\t"
      "v_mfma_f32_16x16x32_bf16 a[112:115], v[88:91], v[56:59], a[112:115]\n\t"
      "ds_read_b64_tr_b16 v[48:49], %[w0] offset:33280\n\t"
      "v_mfma_f32_16x16x32_bf16 a[116:119], v[92:95], v[56:59], a[116:119]\n\t"
      "ds_read_b64_tr_b16 v[50:51], %[w0] offset:34304\n\t"
      "buffer_load_dwordx4 v[76:79], %[vh1], %[rs], %[t1] offen offset:1024 sc1\n\t"
      "s_waitcnt lgkmcnt(10)\n\t"
      "v_mfma_f32_16x16x32_bf16 a[120:123], v[88:91], v[60:63], a[120:123]\n\t"
      "ds_read_b64_tr_b16 v[52:53], %[w1] offset:33280\n\t"
      "v_mfma_f32_16x16x32_bf16 a[124:127], v[92:95], v[60:63], a[124:127]\n\t"
      "ds_read_b64_tr_b16 v[54:55], %[w1] offset:34304\n\t"
      "s_waitcnt vmcnt(15)\n\t"
      "s_waitcnt lgkmcnt(10)\n\t"
      "v_mfma_f32_16x16x32_bf16 a[64:67], v[96:99], v[32:35], a[64:67]\n\t"
      "ds_read_b64_tr_b16 v[56:57], %[w0] offset:33536\n\t"
      "v_mfma_f32_16x16x32_bf16 a[68:71], v[100:103], v[32:35], a[68:71]\n\t"
      "ds_read_b64_tr_b16 v[58:59], %[w0] offset:34560\n\t"
      "s_waitcnt lgkmcnt(10)\n\t"
      "v_mfma_f32_16x16x32_bf16 a[72:75], v[96:99], v[36:39], a[72:75]\n\t"
      "ds_read_b64_tr_b16 v[60:61], %[w1] offset:33536\n\t"
      "v_accvgpr_read_b32 v16, a36\n\t"
      "v_accvgpr_read_b32 v17, a37\n\t"
      "v_mfma_f32_16x16x32_bf16 a[76:79], v[100:103], v[36:39], a[76:79]\n\t"
      "ds_read_b64_tr_b16 v[62:63], %[w1] offset:34560\n\t"
      "s_waitcnt lgkmcnt(10)\n\t"
      "v_mfma_f32_16x16x32_bf16 a[80:83], v[96:99], v[40:43], a[80:83]\n\t"
      "ds_read_b64_tr_b16 v[32:33], %[w0] offset:40960\n\t"
      "v_accvgpr_read_b32 v18, a38\n\t"
      "v_accvgpr_read_b32 v19, a39\n\t"
      "v_mfma_f32_16x16x32_bf16 a[84:87], v[100:103], v[40:43], a[84:87]\n\t"
      "ds_read_b64_tr_b16 v[34:35], %[w0] offset:41984\n\t"
      "s_waitcnt lgkmcnt(10)\n\t"
      "v_mfma_f32_16x16x32_bf16 a[88:91], v[96:99], v[44:47], a[88:91]\n\t"
      "ds_read_b64_tr_b16 v[36:37], %[w1] offset:40960\n\t"
      "v_accvgpr_read_b32 v20, a44\n\t"
      "v_accvgpr_read_b32 v21, a45\n\t"
      "v_mfma_f32_16x16x32_bf16 a[92:95], v[100:103], v[44:47], a[92:95]\n\t"
      "ds_read_b64_tr_b16 v[38:39], %[w1] offset:41984\n\t"
      "s_waitcnt lgkmcnt(10)\n\t"
      "v_mfma_f32_16x16x32_bf16 a[96:99], v[96:99], v[48:51], a[96:99]\n\t"
      "ds_read_b64_tr_b16 v[40:41], %[w0] offset:41216\n\t"
      "v_accvgpr_read_b32 v22, a46\n\t"
      "v_accvgpr_read_b32 v23, a47\n\t"
      "v_mfma_f32_16x16x32_bf16 a[100:103], v[100:103], v[48:51], a[100:103]\n\t"
      "ds_read_b64_tr_b16 v[42:43], %[w0] offset:42240\n\t"
      "s_waitcnt lgkmcnt(10)\n\t"
      "v_mfma_f32_16x16x32_bf16 a[104:107], v[96:99], v[52:55], a[104:107]\n\t"
      "ds_read_b64_tr_b16 v[44:45], %[w1] offset:41216\n\t"
      "v_cvt_pk_bf16_f32 v16, v16, v17\n\t"
      "v_cvt_pk_bf16_f32 v17, v18, v19\n\t"
      "v_mfma_f32_16x16x32_bf16 a[108:111], v[100:103], v[52:55], a[108:111]\n\t"
      "ds_read_b64_tr_b16 v[46:47], %[w1] offset:42240\n\t"
      "s_waitcnt lgkmcnt(10)\n\t"
      "v_mfma_f32_16x16x32_bf16 a[112:115], v[96:99], v[56:59], a[112:115]\n\t"
      "ds_read_b64_tr_b16 v[48:49], %[w0] offset:41472\n\t"
      "v_cvt_pk_bf16_f32 v18, v20, v21\n\t"
      "v_cvt_pk_bf16_f32 v19, v22, v23\n\t"
      "s_add_u32 %[t0], %[so0], 6144\n\t"
      "buffer_store_dwordx4 v[16:19], %[vh0], %[rs], %[t0] offen sc1\n\t"
      "v_mfma_f32_16x16x32_bf16 a[116:119], v[100:103], v[56:59], a[116:119]\n\t"
      "ds_read_b64_tr_b16 v[50:51], %[w0] offset:42496\n\t"
      "s_add_u32 %[t0], %[sw], 1572864\n\t"
      "s_add_u32 m0, %[dw], 49152\n\t"
      "s_nop 0\n\t"
      "buffer_load_dwordx4 %[vw], %[rw2], %[t0] offen lds\n\t"
      "s_waitcnt lgkmcnt(10)\n\t"
      "v_mfma_f32_16x16x32_bf16 a[120:123], v[96:99], v[60:63], a[120:123]\n\t"
      "ds_read_b64_tr_b16 v[52:53], %[w1] offset:41472\n\t"
      "v_mfma_f32_16x16x32_bf16 a[124:127], v[100:103], v[60:63], a[124:127]\n\t"
      "ds_read_b64_tr_b16 v[54:55], %[w1] offset:42496\n\t"
      "s_add_u32 %[t0], %[sw], 1835008\n\t"
      "s_add_u32 m0, %[dw], 57344\n\t"
      "s_nop 0\n\t"
      "buffer_load_dwordx4 %[vw], %[rw2], %[t0] offen lds\n\t"
      "s_waitcnt vmcnt(29)\n\t"
      "s_barrier\n\t"
      "s_waitcnt lgkmcnt(10)\n\t"
      "v_mfma_f32_16x16x32_bf16 a[64:67], v[104:107], v[32:35], a[64:67]\n\t"
      "ds_read_b64_tr_b16 v[56:57], %[w0] offset:41728\n\t"
      "v_mfma_f32_16x16x32_bf16 a[68:71], v[108:111], v[32:35], a[68:71]\n\t"
      "ds_read_b64_tr_b16 v[58:59], %[w0] offset:42752\n\t"
      "s_waitcnt lgkmcnt(10)\n\t"
      "v_mfma_f32_16x16x32_bf16 a[72:75], v[104:107], v[36:39], a[72:75]\n\t"
      "ds_read_b64_tr_b16 v[60:61], %[w1] offset:41728\n\t"
      "v_mfma_f32_16x16x32_bf16 a[76:79], v[108:111], v[36:39], a[76:79]\n\t"
      "ds_read_b64_tr_b16 v[62:63], %[w1] offset:42752\n\t"
      "s_waitcnt lgkmcnt(10)\n\t"
      "v_mfma_f32_16x16x32_bf16 a[80:83], v[104:107], v[40:43], a[80:83]\n\t"
      "ds_read_b64_tr_b16 v[32:33], %[w0] offset:49152\n\t"
      "v_mfma_f32_16x16x32_bf16 a[84:87], v[108:111], v[40:43], a[84:87]\n\t"
      "ds_read_b64_tr_b16 v[34:35], %[w0] offset:50176\n\t"
      "s_waitcnt lgkmcnt(10)\n\t"
      "v_mfma_f32_16x16x32_bf16 a[88:91], v[104:107], v[44:47], a[88:91]\n\t"
      "ds_read_b64_tr_b16 v[36:37], %[w1] offset:49152\n\t"
      "v_mfma_f32_16x16x32_bf16 a[92:95], v[108:111], v[44:47], a[92:95]\n\t"
      "ds_read_b64_tr_b16 v[38:39], %[w1] offset:50176\n\t"
      "s_add_u32 %[t1], %[sh], 2048\n\t"
      "buffer_load_dwordx4 v[80:83], %[vh0], %[rs], %[t1] offen offset:0 sc1\n\t"
      "s_waitcnt lgkmcnt(10)\n\t"
      "v_mfma_f32_16x16x32_bf16 a[96:99], v[104:107], v[48:51], a[96:99]\n\t"
      "ds_read_b64_tr_b16 v[40:41], %[w0] offset:49408\n\t"
      "v_mfma_f32_16x16x32_bf16 a[100:103], v[108:111], v[48:51], a[100:103]\n\t"
      "ds_read_b64_tr_b16 v[42:43], %[w0] offset:50432\n\t"
      "buffer_load_dwordx4 v[84:87], %[vh1], %[rs], %[t1] offen offset:0 sc1\n\t"
      "s_waitcnt lgkmcnt(10)\n\t"
      "v_mfma_f32_16x16x32_bf16 a[104:107], v[104:107], v[52:55], a[104:107]\n\t"
      "ds_read_b64_tr_b16 v[44:45], %[w1] offset:49408\n\t"
      "v_mfma_f32_16x16x32_bf16 a[108:111], v[108:111], v[52:55], a[108:111]\n\t"
      "ds_read_b64_tr_b16 v[46:47], %[w1] offset:50432\n\t"
      "buffer_load_dwordx4 v[88:91], %[vh0], %[rs], %[t1] offen offset:1024 sc1\n\t"
      "s_waitcnt lgkmcnt(10)\n\t"
      "v_mfma_f32_16x16x32_bf16 a[112:115], v[104:107], v[56:59], a[112:115]\n\t"
      "ds_read_b64_tr_b16 v[48:49], %[w0] offset:49664\n\t"
      "v_mfma_f32_16x16x32_bf16 a[116:119], v[108:111], v[56:59], a[116:119]\n\t"
      "ds_read_b64_tr_b16 v[50:51], %[w0] offset:50688\n\t"
      "buffer_load_dwordx4 v[92:95], %[vh1], %[rs], %[t1] offen offset:1024 sc1\n\t"
      "s_waitcnt lgkmcnt(10)\n\t"
      "v_mfma_f32_16x16x32_bf16 a[120:123], v[104:107], v[60:63], a[120:123]\n\t"
      "ds_read_b64_tr_b16 v[52:53], %[w1] offset:49664\n\t"
      "v_mfma_f32_16x16x32_bf16 a[124:127], v[108:111], v[60:63], a[124:127]\n\t"
      "ds_read_b64_tr_b16 v[54:55], %[w1] offset:50688\n\t"
      "s_waitcnt vmcnt(14)\n\t"
      "s_waitcnt lgkmcnt(10)\n\t"
      "v_mfma_f32_16x16x32_bf16 a[64:67], v[112:115], v[32:35], a[64:67]\n\t"
      "ds_read_b64_tr_b16 v[56:57], %[w0] offset:49920\n\t"
      "v_mfma_f32_16x16x32_bf16 a[68:71], v[116:119], v[32:35], a[68:71]\n\t"
      "ds_read_b64_tr_b16 v[58:59], %[w0] offset:50944\n\t"
      "s_waitcnt lgkmcnt(10)\n\t"
      "v_mfma_f32_16x16x32_bf16 a[72:75], v[112:115], v[36:39], a[72:75]\n\t"
      "ds_read_b64_tr_b16 v[60:61], %[w1] offset:49920\n\t"
      "v_accvgpr_read_b32 v24, a52\n\t"
      "v_accvgpr_read_b32 v25, a53\n\t"
      "v_mfma_f32_16x16x32_bf16 a[76:79], v[116:119], v[36:39], a[76:79]\n\t"
      "ds_read_b64_tr_b16 v[62:63], %[w1] offset:50944\n\t"
      "s_waitcnt lgkmcnt(10)\n\t"
      "v_mfma_f32_16x16x32_bf16 a[80:83], v[112:115], v[40:43], a[80:83]\n\t"
      "ds_read_b64_tr_b16 v[32:33], %[w0] offset:57344\n\t"
      "v_accvgpr_read_b32 v26, a54\n\t"
      "v_accvgpr_read_b32 v27, a55\n\t"
      "v_mfma_f32_16x16x32_bf16 a[84:87], v[116:119], v[40:43], a[84:87]\n\t"
      "ds_read_b64_tr_b16 v[34:35], %[w0] offset:58368\n\t"
      "s_waitcnt lgkmcnt(10)\n\t"
      "v_mfma_f32_16x16x32_bf16 a[88:91], v[112:115], v[44:47], a[88:91]\n\t"
      "ds_read_b64_tr_b16 v[36:37], %[w1] offset:57344\n\t"
      "v_accvgpr_read_b32 v28, a60\n\t"
      "v_accvgpr_read_b32 v29, a61\n\t"
      "v_mfma_f32_16x16x32_bf16 a[92:95], v[116:119], v[44:47], a[92:95]\n\t"
      "ds_read_b64_tr_b16 v[38:39], %[w1] offset:58368\n\t"
      "s_waitcnt lgkmcnt(10)\n\t"
      "v_mfma_f32_16x16x32_bf16 a[96:99], v[112:115], v[48:51], a[96:99]\n\t"
      "ds_read_b64_tr_b16 v[40:41], %[w0] offset:57600\n\t"
      "v_accvgpr_read_b32 v30, a62\n\t"
      "v_accvgpr_read_b32 v31, a63\n\t"
      "v_mfma_f32_16x16x32_bf16 a[100:103], v[116:119], v[48:51], a[100:103]\n\t"
      "ds_read_b64_tr_b16 v[42:43], %[w0] offset:58624\n\t"
      "s_waitcnt lgkmcnt(10)\n\t"
      "v_mfma_f32_16x16x32_bf16 a[104:107], v[112:115], v[52:55], a[104:107]\n\t"
      "ds_read_b64_tr_b16 v[44:45], %[w1] offset:57600\n\t"
      "v_cvt_pk_bf16_f32 v24, v24, v25\n\t"
      "v_cvt_pk_bf16_f32 v25, v26, v27\n\t"
      "v_mfma_f32_16x16x32_bf16 a[108:111], v[116:119], v[52:55], a[108:111]\n\t"
      "ds_read_b64_tr_b16 v[46:47], %[w1] offset:58624\n\t"
      "s_waitcnt lgkmcnt(10)\n\t"
      "v_mfma_f32_16x16x32_bf16 a[112:115], v[112:115], v[56:59], a[112:115]\n\t"
      "ds_read_b64_tr_b16 v[48:49], %[w0] offset:57856\n\t"
      "v_cvt_pk_bf16_f32 v26, v28, v29\n\t"
      "v_cvt_pk_bf16_f32 v27, v30, v31\n\t"
      "s_add_u32 %[t0], %[so0], 7168\n\t"
      "buffer_store_dwordx4 v[24:27], %[vh0], %[rs], %[t0] offen sc1\n\t"
      "v_mfma_f32_16x16x32_bf16 a[116:119], v[116:119], v[56:59], a[116:119]\n\t"
      "ds_read_b64_tr_b16 v[50:51], %[w0] offset:58880\n\t"
      "s_add_u32 %[t0], %[sw], 2097152\n\t"
      "s_add_u32 m0, %[dw], 65536\n\t"
      "s_nop 0\n\t"
      "buffer_load_dwordx4 %[vw], %[rw2], %[t0] offen lds\n\t"
      "s_waitcnt lgkmcnt(10)\n\t"
      "v_mfma_f32_16x16x32_bf16 a[120:123], v[112:115], v[60:63], a[120:123]\n\t"
      "ds_read_b64_tr_b16 v[52:53], %[w1] offset:57856\n\t"
      "v_mfma_f32_16x16x32_bf16 a[124:127], v[116:119], v[60:63], a[124:127]\n\t"
      "ds_read_b64_tr_b16 v[54:55], %[w1] offset:58880\n\t"
      "s_add_u32 %[t0], %[sw], 2359296\n\t"
      "s_add_u32 m0, %[dw], 73728\n\t"
      "s_nop 0\n\t"
      "buffer_load_dwordx4 %[vw], %[rw2], %[t0] offen lds\n\t"
      "s_waitcnt vmcnt(29)\n\t"
      "s_barrier\n\t"
      "s_waitcnt lgkmcnt(10)\n\t"
      "v_mfma_f32_16x16x32_bf16 a[64:67], v[120:123], v[32:35], a[64:67]\n\t"
      "ds_read_b64_tr_b16 v[56:57], %[w0] offset:58112\n\t"
      "v_mfma_f32_16x16x32_bf16 a[68:71], v[124:127], v[32:35], a[68:71]\n\t"
      "ds_read_b64_tr_b16 v[58:59], %[w0] offset:59136\n\t"
      "s_waitcnt lgkmcnt(10)\n\t"
      "v_mfma_f32_16x16x32_bf16 a[72:75], v[120:123], v[36:39], a[72:75]\n\t"
      "ds_read_b64_tr_b16 v[60:61], %[w1] offset:58112\n\t"
      "v_mfma_f32_16x16x32_bf16 a[76:79], v[124:127], v[36:39], a[76:79]\n\t"
      "ds_read_b64_tr_b16 v[62:63], %[w1] offset:59136\n\t"
      "s_waitcnt lgkmcnt(10)\n\t"
      "v_mfma_f32_16x16x32_bf16 a[80:83], v[120:123], v[40:43], a[80:83]\n\t"
      "v_xor_b32 %[w0], 0x10000, %[w0]\n\t"
      "v_xor_b32 %[w1], 0x10000, %[w1]\n\t"
      "ds_read_b64_tr_b16 v[32:33], %[w0] offset:0\n\t"
      "v_mfma_f32_16x16x32_bf16 a[84:87], v[124:127], v[40:43], a[84:87]\n\t"
      "ds_read_b64_tr_b16 v[34:35], %[w0] offset:1024\n\t"
      "s_waitcnt lgkmcnt(10)\n\t"
      "v_mfma_f32_16x16x32_bf16 a[88:91], v[120:123], v[44:47], a[88:91]\n\t"
      "ds_read_b64_tr_b16 v[36:37], %[w1] offset:0\n\t"
      "v_mfma_f32_16x16x32_bf16 a[92:95], v[124:127], v[44:47], a[92:95]\n\t"
      "ds_read_b64_tr_b16 v[38:39], %[w1] offset:1024\n\t"
      "s_add_u32 %[t1], %[sh], 4096\n\t"
      "buffer_load_dwordx4 v[96:99], %[vh0], %[rs], %[t1] offen offset:0 sc1\n\t"
      "s_waitcnt lgkmcnt(10)\n\t"
      "v_mfma_f32_16x16x32_bf16 a[96:99], v[120:123], v[48:51], a[96:99]\n\t"
      "ds_read_b64_tr_b16 v[40:41], %[w0] offset:256\n\t"
      "v_mfma_f32_16x16x32_bf16 a[100:103], v[124:127], v[48:51], a[100:103]\n\t"
      "ds_read_b64_tr_b16 v[42:43], %[w0] offset:1280\n\t"
      "buffer_load_dwordx4 v[100:103], %[vh1], %[rs], %[t1] offen offset:0 sc1\n\t"
      "s_waitcnt lgkmcnt(10)\n\t"
      "v_mfma_f32_16x16x32_bf16 a[104:107], v[120:123], v[52:55], a[104:107]\n\t"
      "ds_read_b64_tr_b16 v[44:45], %[w1] offset:256\n\t"
      "v_mfma_f32_16x16x32_bf16 a[108:111], v[124:127], v[52:55], a[108:111]\n\t"
      "ds_read_b64_tr_b16 v[46:47], %[w1] offset:1280\n\t"
      "buffer_load_dwordx4 v[104:107], %[vh0], %[rs], %[t1] offen offset:1024 sc1\n\t"
      "s_waitcnt lgkmcnt(10)\n\t"
      "v_mfma_f32_16x16x32_bf16 a[112:115], v[120:123], v[56:59], a[112:115]\n\t"
      "ds_read_b64_tr_b16 v[48:49], %[w0] offset:512\n\t"
      "v_mfma_f32_16x16x32_bf16 a[116:119], v[124:127], v[56:59], a[116:119]\n\t"
      "ds_read_b64_tr_b16 v[50:51], %[w0] offset:1536\n\t"
      "buffer_load_dwordx4 v[108:111], %[vh1], %[rs], %[t1] offen offset:1024 sc1\n\t"
      "s_waitcnt lgkmcnt(10)\n\t"
      "v_mfma_f32_16x16x32_bf16 a[120:123], v[120:123], v[60:63], a[120:123]\n\t"
      "ds_read_b64_tr_b16 v[52:53], %[w1] offset:512\n\t"
      "v_mfma_f32_16x16x32_bf16 a[124:127], v[124:127], v[60:63], a[124:127]\n\t"
      "ds_read_b64_tr_b16 v[54:55], %[w1] offset:1536\n\t"
      "s_waitcnt vmcnt(14)\n\t"
      "s_waitcnt lgkmcnt(10)\n\t"
      "v_mfma_f32_16x16x32_bf16 a[0:3], v[32:35], v[64:67], 0\n\t"
      "ds_read_b64_tr_b16 v[56:57], %[w0] offset:768\n\t"
      "v_mfma_f32_16x16x32_bf16 a[4:7], v[32:35], v[68:71], 0\n\t"
      "ds_read_b64_tr_b16 v[58:59], %[w0] offset:1792\n\t"
      "s_waitcnt lgkmcnt(10)\n\t"
      "v_mfma_f32_16x16x32_bf16 a[8:11], v[36:39], v[64:67], 0\n\t"
      "ds_read_b64_tr_b16 v[60:61], %[w1] offset:768\n\t"
      "v_accvgpr_read_b32 v16, a64\n\t"
      "v_accvgpr_read_b32 v17, a65\n\t"
      "v_mfma_f32_16x16x32_bf16 a[12:15], v[36:39], v[68:71], 0\n\t"
      "ds_read_b64_tr_b16 v[62:63], %[w1] offset:1792\n\t"
      "s_waitcnt lgkmcnt(10)\n\t"
      "v_mfma_f32_16x16x32_bf16 a[16:19], v[40:43], v[64:67], 0\n\t"
      "ds_read_b64_tr_b16 v[32:33], %[w0] offset:8192\n\t"
      "v_accvgpr_read_b32 v18, a66\n\t"
      "v_accvgpr_read_b32 v19, a67\n\t"
      "v_mfma_f32_16x16x32_bf16 a[20:23], v[40:43], v[68:71], 0\n\t"
      "ds_read_b64_tr_b16 v[34:35], %[w0] offset:9216\n\t"
      "s_waitcnt lgkmcnt(10)\n\t"
      "v_mfma_f32_16x16x32_bf16 a[24:27], v[44:47], v[64:67], 0\n\t"
      "ds_read_b64_tr_b16 v[36:37], %[w1] offset:8192\n\t"
      "v_accvgpr_read_b32 v20, a68\n\t"
      "v_accvgpr_read_b32 v21, a69\n\t"
      "v_mfma_f32_16x16x32_bf16 a[28:31], v[44:47], v[68:71], 0\n\t"
      "ds_read_b64_tr_b16 v[38:39], %[w1] offset:9216\n\t"
      "s_waitcnt lgkmcnt(10)\n\t"
      "v_mfma_f32_16x16x32_bf16 a[32:35], v[48:51], v[64:67], 0\n\t"
      "ds_read_b64_tr_b16 v[40:41], %[w0] offset:8448\n\t"
      "v_accvgpr_read_b32 v22, a70\n\t"
      "v_accvgpr_read_b32 v23, a71\n\t"
      "v_mfma_f32_16x16x32_bf16 a[36:39], v[48:51], v[68:71], 0\n\t"
      "ds_read_b64_tr_b16 v[42:43], %[w0] offset:9472\n\t"
      "s_waitcnt lgkmcnt(10)\n\t"
      "v_mfma_f32_16x16x32_bf16 a[40:43], v[52:55], v[64:67], 0\n\t"
      "ds_read_b64_tr_b16 v[44:45], %[w1] offset:8448\n\t"
      "v_cvt_pk_bf16_f32 v16, v16, v17\n\t"
      "v_cvt_pk_bf16_f32 v17, v18, v19\n\t"
      "v_mfma_f32_16x16x32_bf16 a[44:47], v[52:55], v[68:71], 0\n\t"
      "ds_read_b64_tr_b16 v[46:47], %[w1] offset:9472\n\t"
      "s_waitcnt lgkmcnt(10)\n\t"
      "v_mfma_f32_16x16x32_bf16 a[48:51], v[56:59], v[64:67], 0\n\t"
      "ds_read_b64_tr_b16 v[48:49], %[w0] offset:8704\n\t"
      "v_cvt_pk_bf16_f32 v18, v20, v21\n\t"
      "v_cvt_pk_bf16_f32 v19, v22, v23\n\t"
      "s_add_u32 %[t0], %[so1], 0\n\t"
      "buffer_store_dwordx4 v[16:19], %[vh0], %[rs], %[t0] offen sc1\n\t"
      "v_mfma_f32_16x16x32_bf16 a[52:55], v[56:59], v[68:71], 0\n\t"
      "ds_read_b64_tr_b16 v[50:51], %[w0] offset:9728\n\t"
      "s_add_u32 %[t0], %[sw], 2621440\n\t"
      "s_add_u32 m0, %[dw], 81920\n\t"
      "s_nop 0\n\t"
      "buffer_load_dwordx4 %[vw], %[rw2], %[t0] offen lds\n\t"
      "s_waitcnt lgkmcnt(10)\n\t"
      "v_mfma_f32_16x16x32_bf16 a[56:59], v[60:63], v[64:67], 0\n\t"
      "ds_read_b64_tr_b16 v[52:53], %[w1] offset:8704\n\t"
      "v_mfma_f32_16x16x32_bf16 a[60:63], v[60:63], v[68:71], 0\n\t"
      "ds_read_b64_tr_b16 v[54:55], %[w1] offset:9728\n\t"
      "s_add_u32 %[t0], %[sw], 2883584\n\t"
      "s_add_u32 m0, %[dw], 90112\n\t"
      "s_nop 0\n\t"
      "buffer_load_dwordx4 %[vw], %[rw2], %[t0] offen lds\n\t"
      "s_waitcnt vmcnt(29)\n\t"
      "s_barrier\n\t"
      "s_waitcnt lgkmcnt(10)\n\t"
      "v_mfma_f32_16x16x32_bf16 a[0:3], v[32:35], v[72:75], a[0:3]\n\t"
      "ds_read_b64_tr_b16 v[56:57], %[w0] offset:8960\n\t"
      "v_mfma_f32_16x16x32_bf16 a[4:7], v[32:35], v[76:79], a[4:7]\n\t"
      "ds_read_b64_tr_b16 v[58:59], %[w0] offset:9984\n\t"
      "s_waitcnt lgkmcnt(10)\n\t"
      "v_mfma_f32_16x16x32_bf16 a[8:11], v[36:39], v[72:75], a[8:11]\n\t"
      "ds_read_b64_tr_b16 v[60:61], %[w1] offset:8960\n\t"
      "v_accvgpr_read_b32 v24, a72\n\t"
      "v_accvgpr_read_b32 v25, a73\n\t"
      "v_mfma_f32_16x16x32_bf16 a[12:15], v[36:39], v[76:79], a[12:15]\n\t"
      "ds_read_b64_tr_b16 v[62:63], %[w1] offset:9984\n\t"
      "s_waitcnt lgkmcnt(10)\n\t"
      "v_mfma_f32_16x16x32_bf16 a[16:19], v[40:43], v[72:75], a[16:19]\n\t"
      "ds_read_b64_tr_b16 v[32:33], %[w0] offset:16384\n\t"
      "v_accvgpr_read_b32 v26, a74\n\t"
      "v_accvgpr_read_b32 v27, a75\n\t"
      "v_mfma_f32_16x16x32_bf16 a[20:23], v[40:43], v[76:79], a[20:23]\n\t"
      "ds_read_b64_tr_b16 v[34:35], %[w0] offset:17408\n\t"
      "s_waitcnt lgkmcnt(10)\n\t"
      "v_mfma_f32_16x16x32_bf16 a[24:27], v[44:47], v[72:75], a[24:27]\n\t"
      "ds_read_b64_tr_b16 v[36:37], %[w1] offset:16384\n\t"
      "v_accvgpr_read_b32 v28, a76\n\t"
      "v_accvgpr_read_b32 v29, a77\n\t"
      "v_mfma_f32_16x16x32_bf16 a[28:31], v[44:47], v[76:79], a[28:31]\n\t"
      "ds_read_b64_tr_b16 v[38:39], %[w1] offset:17408\n\t"
      "s_waitcnt lgkmcnt(10)\n\t"
      "v_mfma_f32_16x16x32_bf16 a[32:35], v[48:51], v[72:75], a[32:35]\n\t"
      "ds_read_b64_tr_b16 v[40:41], %[w0] offset:16640\n\t"
      "v_accvgpr_read_b32 v30, a78\n\t"
      "v_accvgpr_read_b32 v31, a79\n\t"
      "v_mfma_f32_16x16x32_bf16 a[36:39], v[48:51], v[76:79], a[36:39]\n\t"
      "ds_read_b64_tr_b16 v[42:43], %[w0] offset:17664\n\t"
      "s_waitcnt lgkmcnt(10)\n\t"
      "v_mfma_f32_16x16x32_bf16 a[40:43], v[52:55], v[72:75], a[40:43]\n\t"
      "ds_read_b64_tr_b16 v[44:45], %[w1] offset:16640\n\t"
      "v_cvt_pk_bf16_f32 v24, v24, v25\n\t"
      "v_cvt_pk_bf16_f32 v25, v26, v27\n\t"
      "v_mfma_f32_16x16x32_bf16 a[44:47], v[52:55], v[76:79], a[44:47]\n\t"
      "ds_read_b64_tr_b16 v[46:47], %[w1] offset:17664\n\t"
      "s_waitcnt lgkmcnt(10)\n\t"
      "v_mfma_f32_16x16x32_bf16 a[48:51], v[56:59], v[72:75], a[48:51]\n\t"
      "ds_read_b64_tr_b16 v[48:49], %[w0] offset:16896\n\t"
      "v_cvt_pk_bf16_f32 v26, v28, v29\n\t"
      "v_cvt_pk_bf16_f32 v27, v30, v31\n\t"
      "s_add_u32 %[t0], %[so1], 1024\n\t"
      "buffer_store_dwordx4 v[24:27], %[vh0], %[rs], %[t0] offen sc1\n\t"
      "v_mfma_f32_16x16x32_bf16 a[52:55], v[56:59], v[76:79], a[52:55]\n\t"
      "ds_read_b64_tr_b16 v[50:51], %[w0] offset:17920\n\t"
      "s_add_u32 %[t1], %[sh], 6144\n\t"
      "buffer_load_dwordx4 v[112:115], %[vh0], %[rs], %[t1] offen offset:0 sc1\n\t"
      "s_waitcnt lgkmcnt(10)\n\t"
      "v_mfma_f32_16x16x32_bf16 a[56:59], v[60:63], v[72:75], a[56:59]\n\t"
      "ds_read_b64_tr_b16 v[52:53], %[w1] offset:16896\n\t"
      "buffer_load_dwordx4 v[116:119], %[vh1], %[rs], %[t1] offen offset:0 sc1\n\t"
      "v_mfma_f32_16x16x32_bf16 a[60:63], v[60:63], v[76:79], a[60:63]\n\t"
      "ds_read_b64_tr_b16 v[54:55], %[w1] offset:17920\n\t"
      "buffer_load_dwordx4 v[120:123], %[vh0], %[rs], %[t1] offen offset:1024 sc1\n\t"
      "buffer_load_dwordx4 v[124:127], %[vh1], %[rs], %[t1] offen offset:1024 sc1\n\t"
      "s_waitcnt vmcnt(15)\n\t"
      "s_waitcnt lgkmcnt(10)\n\t"
      "v_mfma_f32_16x16x32_bf16 a[0:3], v[32:35], v[80:83], a[0:3]\n\t"
      "ds_read_b64_tr_b16 v[56:57], %[w0] offset:17152\n\t"
      "v_mfma_f32_16x16x32_bf16 a[4:7], v[32:35], v[84:87], a[4:7]\n\t"
      "ds_read_b64_tr_b16 v[58:59], %[w0] offset:18176\n\t"
      "s_waitcnt lgkmcnt(10)\n\t"
      "v_mfma_f32_16x16x32_bf16 a[8:11], v[36:39], v[80:83], a[8:11]\n\t"
      "ds_read_b64_tr_b16 v[60:61], %[w1] offset:17152\n\t"
      "v_accvgpr_read_b32 v16, a80\n\t"
      "v_accvgpr_read_b32 v17, a81\n\t"
      "v_mfma_f32_16x16x32_bf16 a[12:15], v[36:39], v[84:87], a[12:15]\n\t"
      "ds_read_b64_tr_b16 v[62:63], %[w1] offset:18176\n\t"
      "s_waitcnt lgkmcnt(10)\n\t"
      "v_mfma_f32_16x16x32_bf16 a[16:19], v[40:43], v[80:83], a[16:19]\n\t"
      "ds_read_b64_tr_b16 v[32:33], %[w0] offset:24576\n\t"
      "v_accvgpr_read_b32 v18, a82\n\t"
      "v_accvgpr_read_b32 v19, a83\n\t"
      "v_mfma_f32_16x16x32_bf16 a[20:23], v[40:43], v[84:87], a[20:23]\n\t"
      "ds_read_b64_tr_b16 v[34:35], %[w0] offset:25600\n\t"
      "s_waitcnt lgkmcnt(10)\n\t"
      "v_mfma_f32_16x16x32_bf16 a[24:27], v[44:47], v[80:83], a[24:27]\n\t"
      "ds_read_b64_tr_b16 v[36:37], %[w1] offset:24576\n\t"
      "v_accvgpr_read_b32 v20, a84\n\t"
      "v_accvgpr_read_b32 v21, a85\n\t"
      "v_mfma_f32_16x16x32_bf16 a[28:31], v[44:47], v[84:87], a[28:31]\n\t"
      "ds_read_b64_tr_b16 v[38:39], %[w1] offset:25600\n\t"
      "s_waitcnt lgkmcnt(10)\n\t"
      "v_mfma_f32_16x16x32_bf16 a[32:35], v[48:51], v[80:83], a[32:35]\n\t"
      "ds_read_b64_tr_b16 v[40:41], %[w0] offset:24832\n\t"
      "v_accvgpr_read_b32 v22, a86\n\t"
      "v_accvgpr_read_b32 v23, a87\n\t"
      "v_mfma_f32_16x16x32_bf16 a[36:39], v[48:51], v[84:87], a[36:39]\n\t"
      "ds_read_b64_tr_b16 v[42:43], %[w0] offset:25856\n\t"
      "s_waitcnt lgkmcnt(10)\n\t"
      "v_mfma_f32_16x16x32_bf16 a[40:43], v[52:55], v[80:83], a[40:43]\n\t"
      "ds_read_b64_tr_b16 v[44:45], %[w1] offset:24832\n\t"
      "v_cvt_pk_bf16_f32 v16, v16, v17\n\t"
      "v_cvt_pk_bf16_f32 v17, v18, v19\n\t"
      "v_mfma_f32_16x16x32_bf16 a[44:47], v[52:55], v[84:87], a[44:47]\n\t"
      "ds_read_b64_tr_b16 v[46:47], %[w1] offset:25856\n\t"
      "s_waitcnt lgkmcnt(10)\n\t"
      "v_mfma_f32_16x16x32_bf16 a[48:51], v[56:59], v[80:83], a[48:51]\n\t"
      "ds_read_b64_tr_b16 v[48:49], %[w0] offset:25088\n\t"
      "v_cvt_pk_bf16_f32 v18, v20, v21\n\t"
      "v_cvt_pk_bf16_f32 v19, v22, v23\n\t"
      "s_add_u32 %[t0], %[so1], 2048\n\t"
      "buffer_store_dwordx4 v[16:19], %[vh0], %[rs], %[t0] offen sc1\n\t"
      "v_mfma_f32_16x16x32_bf16 a[52:55], v[56:59], v[84:87], a[52:55]\n\t"
      "ds_read_b64_tr_b16 v[50:51], %[w0] offset:26112\n\t"
      "s_add_u32 %[t0], %[sw], 3145728\n\t"
      "s_add_u32 m0, %[dw], 98304\n\t"
      "s_nop 0\n\t"
      "buffer_load_dwordx4 %[vw], %[rw2], %[t0] offen lds\n\t"
      "s_waitcnt lgkmcnt(10)\n\t"
      "v_mfma_f32_16x16x32_bf16 a[56:59], v[60:63], v[80:83], a[56:59]\n\t"
      "ds_read_b64_tr_b16 v[52:53], %[w1] offset:25088\n\t"
      "v_mfma_f32_16x16x32_bf16 a[60:63], v[60:63], v[84:87], a[60:63]\n\t"
      "ds_read_b64_tr_b16 v[54:55], %[w1] offset:26112\n\t"
      "s_add_u32 %[t0], %[sw], 3407872\n\t"
      "s_add_u32 m0, %[dw], 106496\n\t"
      "s_nop 0\n\t"
      "buffer_load_dwordx4 %[vw], %[rw2], %[t0] offen lds\n\t"
      "s_waitcnt vmcnt(17)\n\t"
      "s_barrier\n\t"
      "s_cmp_lg_u32 %[wid], 0\n\t"
      "s_cbranch_scc1 .Ls3%=\n\t"
      "s_mov_b64 exec, 1\n\t"
      "v_mov_b32 v31, 1\n\t"
      "buffer_atomic_add v31, off, %[rs], %[fk]\n\t"
      "s_mov_b64 exec, -1\n\t"
      ".Ls3%=:\n\t"
      "s_waitcnt lgkmcnt(10)\n\t"
      "v_mfma_f32_16x16x32_bf16 a[0:3], v[32:35], v[88:91], a[0:3]\n\t"
      "ds_read_b64_tr_b16 v[56:57], %[w0] offset:25344\n\t"
      "v_mfma_f32_16x16x32_bf16 a[4:7], v[32:35], v[92:95], a[4:7]\n\t"
      "ds_read_b64_tr_b16 v[58:59], %[w0] offset:26368\n\t"
      "s_waitcnt lgkmcnt(10)\n\t"
      "v_mfma_f32_16x16x32_bf16 a[8:11], v[36:39], v[88:91], a[8:11]\n\t"
      "ds_read_b64_tr_b16 v[60:61], %[w1] offset:25344\n\t"
      "v_mfma_f32_16x16x32_bf16 a[12:15], v[36:39], v[92:95], a[12:15]\n\t"
      "ds_read_b64_tr_b16 v[62:63], %[w1] offset:26368\n\t"
      "s_waitcnt lgkmcnt(10)\n\t"
      "v_mfma_f32_16x16x32_bf16 a[16:19], v[40:43], v[88:91], a[16:19]\n\t"
      "ds_read_b64_tr_b16 v[32:33], %[w0] offset:32768\n\t"
      "v_mfma_f32_16x16x32_bf16 a[20:23], v[40:43], v[92:95], a[20:23]\n\t"
      "ds_read_b64_tr_b16 v[34:35], %[w0] offset:33792\n\t"
      "s_waitcnt lgkmcnt(10)\n\t"
      "v_mfma_f32_16x16x32_bf16 a[24:27], v[44:47], v[88:91], a[24:27]\n\t"
      "ds_read_b64_tr_b16 v[36:37], %[w1] offset:32768\n\t"
      "v_mfma_f32_16x16x32_bf16 a[28:31], v[44:47], v[92:95], a[28:31]\n\t"
      "ds_read_b64_tr_b16 v[38:39], %[w1] offset:33792\n\t"
      "s_add_u32 %[t1], %[sh], 8192\n\t"
      "buffer_load_dwordx4 v[64:67], %[vh0], %[rs], %[t1] offen offset:0 sc1\n\t"
      "s_waitcnt lgkmcnt(10)\n\t"
      "v_mfma_f32_16x16x32_bf16 a[32:35], v[48:51], v[88:91], a[32:35]\n\t"
      "ds_read_b64_tr_b16 v[40:41], %[w0] offset:33024\n\t"
      "v_mfma_f32_16x16x32_bf16 a[36:39], v[48:51], v[92:95], a[36:39]\n\t"
      "ds_read_b64_tr_b16 v[42:43], %[w0] offset:34048\n\t"
      "buffer_load_dwordx4 v[68:71], %[vh1], %[rs], %[t1] offen offset:0 sc1\n\t"
      "s_waitcnt lgkmcnt(10)\n\t"
      "v_mfma_f32_16x16x32_bf16 a[40:43], v[52:55], v[88:91], a[40:43]\n\t"
      "ds_read_b64_tr_b16 v[44:45], %[w1] offset:33024\n\t"
      "v_mfma_f32_16x16x32_bf16 a[44:47], v[52:55], v[92:95], a[44:47]\n\t"
      "ds_read_b64_tr_b16 v[46:47], %[w1] offset:34048\n\t"
      "buffer_load_dwordx4 v[72:75], %[vh0], %[rs], %[t1] offen offset:1024 sc1\n\t"
      "s_waitcnt lgkmcnt(10)\n\t"
      "v_mfma_f32_16x16x32_bf16 a[48:51], v[56:59], v[88:91], a[48:51]\n\t"
      "ds_read_b64_tr_b16 v[48:49], %[w0] offset:33280\n\t"
      "v_mfma_f32_16x16x32_bf16 a[52:55], v[56:59], v[92:95], a[52:55]\n\t"
      "ds_read_b64_tr_b16 v[50:51], %[w0] offset:34304\n\t"
      "buffer_load_dwordx4 v[76:79], %[vh1], %[rs], %[t1] offen offset:1024 sc1\n\t"
      "s_waitcnt lgkmcnt(10)\n\t"
      "v_mfma_f32_16x16x32_bf16 a[56:59], v[60:63], v[88:91], a[56:59]\n\t"
      "ds_read_b64_tr_b16 v[52:53], %[w1] offset:33280\n\t"
      "v_mfma_f32_16x16x32_bf16 a[60:63], v[60:63], v[92:95], a[60:63]\n\t"
      "ds_read_b64_tr_b16 v[54:55], %[w1] offset:34304\n\t"
      "s_waitcnt vmcnt(15)\n\t"
      "s_waitcnt lgkmcnt(10)\n\t"
      "v_mfma_f32_16x16x32_bf16 a[0:3], v[32:35], v[96:99], a[0:3]\n\t"
      "ds_read_b64_tr_b16 v[56:57], %[w0] offset:33536\n\t"
      "v_mfma_f32_16x16x32_bf16 a[4:7], v[32:35], v[100:103], a[4:7]\n\t"
      "ds_read_b64_tr_b16 v[58:59], %[w0] offset:34560\n\t"
      "s_waitcnt lgkmcnt(10)\n\t"
      "v_mfma_f32_16x16x32_bf16 a[8:11], v[36:39], v[96:99], a[8:11]\n\t"
      "ds_read_b64_tr_b16 v[60:61], %[w1] offset:33536\n\t"
      "v_accvgpr_read_b32 v24, a88\n\t"
      "v_accvgpr_read_b32 v25, a89\n\t"
      "v_mfma_f32_16x16x32_bf16 a[12:15], v[36:39], v[100:103], a[12:15]\n\t"
      "ds_read_b64_tr_b16 v[62:63], %[w1] offset:34560\n\t"
      "s_waitcnt lgkmcnt(10)\n\t"
      "v_mfma_f32_16x16x32_bf16 a[16:19], v[40:43], v[96:99], a[16:19]\n\t"
      "ds_read_b64_tr_b16 v[32:33], %[w0] offset:40960\n\t"
      "v_accvgpr_read_b32 v26, a90\n\t"
      "v_accvgpr_read_b32 v27, a91\n\t"
      "v_mfma_f32_16x16x32_bf16 a[20:23], v[40:43], v[100:103], a[20:23]\n\t"
      "ds_read_b64_tr_b16 v[34:35], %[w0] offset:41984\n\t"
      "s_waitcnt lgkmcnt(10)\n\t"
      "v_mfma_f32_16x16x32_bf16 a[24:27], v[44:47], v[96:99], a[24:27]\n\t"
      "ds_read_b64_tr_b16 v[36:37], %[w1] offset:40960\n\t"
      "v_accvgpr_read_b32 v28, a92\n\t"
      "v_accvgpr_read_b32 v29, a93\n\t"
      "v_mfma_f32_16x16x32_bf16 a[28:31], v[44:47], v[100:103], a[28:31]\n\t"
      "ds_read_b64_tr_b16 v[38:39], %[w1] offset:41984\n\t"
      "s_waitcnt lgkmcnt(10)\n\t"
      "v_mfma_f32_16x16x32_bf16 a[32:35], v[48:51], v[96:99], a[32:35]\n\t"
      "ds_read_b64_tr_b16 v[40:41], %[w0] offset:41216\n\t"
      "v_accvgpr_read_b32 v30, a94\n\t"
      "v_accvgpr_read_b32 v31, a95\n\t"
      "v_mfma_f32_16x16x32_bf16 a[36:39], v[48:51], v[100:103], a[36:39]\n\t"
      "ds_read_b64_tr_b16 v[42:43], %[w0] offset:42240\n\t"
      "s_waitcnt lgkmcnt(10)\n\t"
      "v_mfma_f32_16x16x32_bf16 a[40:43], v[52:55], v[96:99], a[40:43]\n\t"
      "ds_read_b64_tr_b16 v[44:45], %[w1] offset:41216\n\t"
      "v_cvt_pk_bf16_f32 v24, v24, v25\n\t"
      "v_cvt_pk_bf16_f32 v25, v26, v27\n\t"
      "v_mfma_f32_16x16x32_bf16 a[44:47], v[52:55], v[100:103], a[44:47]\n\t"
      "ds_read_b64_tr_b16 v[46:47], %[w1] offset:42240\n\t"
      "s_waitcnt lgkmcnt(10)\n\t"
      "v_mfma_f32_16x16x32_bf16 a[48:51], v[56:59], v[96:99], a[48:51]\n\t"
      "ds_read_b64_tr_b16 v[48:49], %[w0] offset:41472\n\t"
      "v_cvt_pk_bf16_f32 v26, v28, v29\n\t"
      "v_cvt_pk_bf16_f32 v27, v30, v31\n\t"
      "s_add_u32 %[t0], %[so1], 3072\n\t"
      "buffer_store_dwordx4 v[24:27], %[vh0], %[rs], %[t0] offen sc1\n\t"
      "v_mfma_f32_16x16x32_bf16 a[52:55], v[56:59], v[100:103], a[52:55]\n\t"
      "ds_read_b64_tr_b16 v[50:51], %[w0] offset:42496\n\t"
      "s_add_u32 %[t0], %[sw], 3670016\n\t"
      "s_add_u32 m0, %[dw], 114688\n\t"
      "s_nop 0\n\t"
      "buffer_load_dwordx4 %[vw], %[rw2], %[t0] offen lds\n\t"
      "s_waitcnt lgkmcnt(10)\n\t"
      "v_mfma_f32_16x16x32_bf16 a[56:59], v[60:63], v[96:99], a[56:59]\n\t"
      "ds_read_b64_tr_b16 v[52:53], %[w1] offset:41472\n\t"
      "v_mfma_f32_16x16x32_bf16 a[60:63], v[60:63], v[100:103], a[60:63]\n\t"
      "ds_read_b64_tr_b16 v[54:55], %[w1] offset:42496\n\t"
      "s_add_u32 %[t0], %[sw], 3932160\n\t"
      "s_add_u32 m0, %[dw], 122880\n\t"
      "s_nop 0\n\t"
      "buffer_load_dwordx4 %[vw], %[rw2], %[t0] offen lds\n\t"
      "s_waitcnt vmcnt(29)\n\t"
      "s_barrier\n\t"
      "s_waitcnt lgkmcnt(10)\n\t"
      "v_mfma_f32_16x16x32_bf16 a[0:3], v[32:35], v[104:107], a[0:3]\n\t"
      "ds_read_b64_tr_b16 v[56:57], %[w0] offset:41728\n\t"
      "v_mfma_f32_16x16x32_bf16 a[4:7], v[32:35], v[108:111], a[4:7]\n\t"
      "ds_read_b64_tr_b16 v[58:59], %[w0] offset:42752\n\t"
      "s_waitcnt lgkmcnt(10)\n\t"
      "v_mfma_f32_16x16x32_bf16 a[8:11], v[36:39], v[104:107], a[8:11]\n\t"
      "ds_read_b64_tr_b16 v[60:61], %[w1] offset:41728\n\t"
      "v_accvgpr_read_b32 v16, a96\n\t"
      "v_accvgpr_read_b32 v17, a97\n\t"
      "v_mfma_f32_16x16x32_bf16 a[12:15], v[36:39], v[108:111], a[12:15]\n\t"
      "ds_read_b64_tr_b16 v[62:63], %[w1] offset:42752\n\t"
      "s_waitcnt lgkmcnt(10)\n\t"
      "v_mfma_f32_16x16x32_bf16 a[16:19], v[40:43], v[104:107], a[16:19]\n\t"
      "ds_read_b64_tr_b16 v[32:33], %[w0] offset:49152\n\t"
      "v_accvgpr_read_b32 v18, a98\n\t"
      "v_accvgpr_read_b32 v19, a99\n\t"
      "v_mfma_f32_16x16x32_bf16 a[20:23], v[40:43], v[108:111], a[20:23]\n\t"
      "ds_read_b64_tr_b16 v[34:35], %[w0] offset:50176\n\t"
      "s_waitcnt lgkmcnt(10)\n\t"
      "v_mfma_f32_16x16x32_bf16 a[24:27], v[44:47], v[104:107], a[24:27]\n\t"
      "ds_read_b64_tr_b16 v[36:37], %[w1] offset:49152\n\t"
      "v_accvgpr_read_b32 v20, a100\n\t"
      "v_accvgpr_read_b32 v21, a101\n\t"
      "v_mfma_f32_16x16x32_bf16 a[28:31], v[44:47], v[108:111], a[28:31]\n\t"
      "ds_read_b64_tr_b16 v[38:39], %[w1] offset:50176\n\t"
      "s_waitcnt lgkmcnt(10)\n\t"
      "v_mfma_f32_16x16x32_bf16 a[32:35], v[48:51], v[104:107], a[32:35]\n\t"
      "ds_read_b64_tr_b16 v[40:41], %[w0] offset:49408\n\t"
      "v_accvgpr_read_b32 v22, a102\n\t"
      "v_accvgpr_read_b32 v23, a103\n\t"
      "v_mfma_f32_16x16x32_bf16 a[36:39], v[48:51], v[108:111], a[36:39]\n\t"
      "ds_read_b64_tr_b16 v[42:43], %[w0] offset:50432\n\t"
      "s_waitcnt lgkmcnt(10)\n\t"
      "v_mfma_f32_16x16x32_bf16 a[40:43], v[52:55], v[104:107], a[40:43]\n\t"
      "ds_read_b64_tr_b16 v[44:45], %[w1] offset:49408\n\t"
      "v_cvt_pk_bf16_f32 v16, v16, v17\n\t"
      "v_cvt_pk_bf16_f32 v17, v18, v19\n\t"
      "v_mfma_f32_16x16x32_bf16 a[44:47], v[52:55], v[108:111], a[44:47]\n\t"
      "ds_read_b64_tr_b16 v[46:47], %[w1] offset:50432\n\t"
      "s_waitcnt lgkmcnt(10)\n\t"
      "v_mfma_f32_16x16x32_bf16 a[48:51], v[56:59], v[104:107], a[48:51]\n\t"
      "ds_read_b64_tr_b16 v[48:49], %[w0] offset:49664\n\t"
      "v_cvt_pk_bf16_f32 v18, v20, v21\n\t"
      "v_cvt_pk_bf16_f32 v19, v22, v23\n\t"
      "s_add_u32 %[t0], %[so1], 4096\n\t"
      "buffer_store_dwordx4 v[16:19], %[vh0], %[rs], %[t0] offen sc1\n\t"
      "v_mfma_f32_16x16x32_bf16 a[52:55], v[56:59], v[108:111], a[52:55]\n\t"
      "ds_read_b64_tr_b16 v[50:51], %[w0] offset:50688\n\t"
      "s_add_u32 %[t1], %[sh], 10240\n\t"
      "buffer_load_dwordx4 v[80:83], %[vh0], %[rs], %[t1] offen offset:0 sc1\n\t"
      "s_waitcnt lgkmcnt(10)\n\t"
      "v_mfma_f32_16x16x32_bf16 a[56:59], v[60:63], v[104:107], a[56:59]\n\t"
      "ds_read_b64_tr_b16 v[52:53], %[w1] offset:49664\n\t"
      "buffer_load_dwordx4 v[84:87], %[vh1], %[rs], %[t1] offen offset:0 sc1\n\t"
      "v_mfma_f32_16x16x32_bf16 a[60:63], v[60:63], v[108:111], a[60:63]\n\t"
      "ds_read_b64_tr_b16 v[54:55], %[w1] offset:50688\n\t"
      "buffer_load_dwordx4 v[88:91], %[vh0], %[rs], %[t1] offen offset:1024 sc1\n\t"
      "buffer_load_dwordx4 v[92:95], %[vh1], %[rs], %[t1] offen offset:1024 sc1\n\t"
      "s_waitcnt vmcnt(15)\n\t"
      "s_waitcnt lgkmcnt(10)\n\t"
      "v_mfma_f32_16x16x32_bf16 a[0:3], v[32:35], v[112:115], a[0:3]\n\t"
      "ds_read_b64_tr_b16 v[56:57], %[w0] offset:49920\n\t"
      "v_mfma_f32_16x16x32_bf16 a[4:7], v[32:35], v[116:119], a[4:7]\n\t"
      "ds_read_b64_tr_b16 v[58:59], %[w0] offset:50944\n\t"
      "s_waitcnt lgkmcnt(10)\n\t"
      "v_mfma_f32_16x16x32_bf16 a[8:11], v[36:39], v[112:115], a[8:11]\n\t"
      "ds_read_b64_tr_b16 v[60:61], %[w1] offset:49920\n\t"
      "v_accvgpr_read_b32 v24, a104\n\t"
      "v_accvgpr_read_b32 v25, a105\n\t"
      "v_mfma_f32_16x16x32_bf16 a[12:15], v[36:39], v[116:119], a[12:15]\n\t"
      "ds_read_b64_tr_b16 v[62:63], %[w1] offset:50944\n\t"
      "s_waitcnt lgkmcnt(10)\n\t"
      "v_mfma_f32_16x16x32_bf16 a[16:19], v[40:43], v[112:115], a[16:19]\n\t"
      "ds_read_b64_tr_b16 v[32:33], %[w0] offset:57344\n\t"
      "v_accvgpr_read_b32 v26, a106\n\t"
      "v_accvgpr_read_b32 v27, a107\n\t"
      "v_mfma_f32_16x16x32_bf16 a[20:23], v[40:43], v[116:119], a[20:23]\n\t"
      "ds_read_b64_tr_b16 v[34:35], %[w0] offset:58368\n\t"
      "s_waitcnt lgkmcnt(10)\n\t"
      "v_mfma_f32_16x16x32_bf16 a[24:27], v[44:47], v[112:115], a[24:27]\n\t"
      "ds_read_b64_tr_b16 v[36:37], %[w1] offset:57344\n\t"
      "v_accvgpr_read_b32 v28, a108\n\t"
      "v_accvgpr_read_b32 v29, a109\n\t"
      "v_mfma_f32_16x16x32_bf16 a[28:31], v[44:47], v[116:119], a[28:31]\n\t"
      "ds_read_b64_tr_b16 v[38:39], %[w1] offset:58368\n\t"
      "s_waitcnt lgkmcnt(10)\n\t"
      "v_mfma_f32_16x16x32_bf16 a[32:35], v[48:51], v[112:115], a[32:35]\n\t"
      "ds_read_b64_tr_b16 v[40:41], %[w0] offset:57600\n\t"
      "v_accvgpr_read_b32 v30, a110\n\t"
      "v_accvgpr_read_b32 v31, a111\n\t"
      "v_mfma_f32_16x16x32_bf16 a[36:39], v[48:51], v[116:119], a[36:39]\n\t"
      "ds_read_b64_tr_b16 v[42:43], %[w0] offset:58624\n\t"
      "s_waitcnt lgkmcnt(10)\n\t"
      "v_mfma_f32_16x16x32_bf16 a[40:43], v[52:55], v[112:115], a[40:43]\n\t"
      "ds_read_b64_tr_b16 v[44:45], %[w1] offset:57600\n\t"
      "v_cvt_pk_bf16_f32 v24, v24, v25\n\t"
      "v_cvt_pk_bf16_f32 v25, v26, v27\n\t"
      "v_mfma_f32_16x16x32_bf16 a[44:47], v[52:55], v[116:119], a[44:47]\n\t"
      "ds_read_b64_tr_b16 v[46:47], %[w1] offset:58624\n\t"
      "s_waitcnt lgkmcnt(10)\n\t"
      "v_mfma_f32_16x16x32_bf16 a[48:51], v[56:59], v[112:115], a[48:51]\n\t"
      "ds_read_b64_tr_b16 v[48:49], %[w0] offset:57856\n\t"
      "v_cvt_pk_bf16_f32 v26, v28, v29\n\t"
      "v_cvt_pk_bf16_f32 v27, v30, v31\n\t"
      "v_mfma_f32_16x16x32_bf16 a[52:55], v[56:59], v[116:119], a[52:55]\n\t"
      "ds_read_b64_tr_b16 v[50:51], %[w0] offset:58880\n\t"
      "s_add_u32 %[t0], %[so1], 5120\n\t"
      "buffer_store_dwordx4 v[24:27], %[vh0], %[rs], %[t0] offen sc1\n\t"
      "s_waitcnt lgkmcnt(10)\n\t"
      "v_mfma_f32_16x16x32_bf16 a[56:59], v[60:63], v[112:115], a[56:59]\n\t"
      "ds_read_b64_tr_b16 v[52:53], %[w1] offset:57856\n\t"
      "v_mfma_f32_16x16x32_bf16 a[60:63], v[60:63], v[116:119], a[60:63]\n\t"
      "ds_read_b64_tr_b16 v[54:55], %[w1] offset:58880\n\t"
      "s_waitcnt vmcnt(28)\n\t"
      "s_barrier\n\t"
      "s_waitcnt lgkmcnt(10)\n\t"
      "v_mfma_f32_16x16x32_bf16 a[0:3], v[32:35], v[120:123], a[0:3]\n\t"
      "ds_read_b64_tr_b16 v[56:57], %[w0] offset:58112\n\t"
      "v_mfma_f32_16x16x32_bf16 a[4:7], v[32:35], v[124:127], a[4:7]\n\t"
      "ds_read_b64_tr_b16 v[58:59], %[w0] offset:59136\n\t"
      "s_waitcnt lgkmcnt(10)\n\t"
      "v_mfma_f32_16x16x32_bf16 a[8:11], v[36:39], v[120:123], a[8:11]\n\t"
      "ds_read_b64_tr_b16 v[60:61], %[w1] offset:58112\n\t"
      "v_mfma_f32_16x16x32_bf16 a[12:15], v[36:39], v[124:127], a[12:15]\n\t"
      "ds_read_b64_tr_b16 v[62:63], %[w1] offset:59136\n\t"
      "s_waitcnt lgkmcnt(10)\n\t"
      "v_mfma_f32_16x16x32_bf16 a[16:19], v[40:43], v[120:123], a[16:19]\n\t"
      "v_xor_b32 %[w0], 0x10000, %[w0]\n\t"
      "v_xor_b32 %[w1], 0x10000, %[w1]\n\t"
      "ds_read_b64_tr_b16 v[32:33], %[w0] offset:0\n\t"
      "v_mfma_f32_16x16x32_bf16 a[20:23], v[40:43], v[124:127], a[20:23]\n\t"
      "ds_read_b64_tr_b16 v[34:35], %[w0] offset:1024\n\t"
      "s_waitcnt lgkmcnt(10)\n\t"
      "v_mfma_f32_16x16x32_bf16 a[24:27], v[44:47], v[120:123], a[24:27]\n\t"
      "ds_read_b64_tr_b16 v[36:37], %[w1] offset:0\n\t"
      "v_mfma_f32_16x16x32_bf16 a[28:31], v[44:47], v[124:127], a[28:31]\n\t"
      "ds_read_b64_tr_b16 v[38:39], %[w1] offset:1024\n\t"
      "s_add_u32 %[t1], %[sh], 12288\n\t"
      "buffer_load_dwordx4 v[96:99], %[vh0], %[rs], %[t1] offen offset:0 sc1\n\t"
      "s_waitcnt lgkmcnt(10)\n\t"
      "v_mfma_f32_16x16x32_bf16 a[32:35], v[48:51], v[120:123], a[32:35]\n\t"
      "ds_read_b64_tr_b16 v[40:41], %[w0] offset:256\n\t"
      "v_mfma_f32_16x16x32_bf16 a[36:39], v[48:51], v[124:127], a[36:39]\n\t"
      "ds_read_b64_tr_b16 v[42:43], %[w0] offset:1280\n\t"
      "buffer_load_dwordx4 v[100:103], %[vh1], %[rs], %[t1] offen offset:0 sc1\n\t"
      "s_waitcnt lgkmcnt(10)\n\t"
      "v_mfma_f32_16x16x32_bf16 a[40:43], v[52:55], v[120:123], a[40:43]\n\t"
      "ds_read_b64_tr_b16 v[44:45], %[w1] offset:256\n\t"
      "v_mfma_f32_16x16x32_bf16 a[44:47], v[52:55], v[124:127], a[44:47]\n\t"
      "ds_read_b64_tr_b16 v[46:47], %[w1] offset:1280\n\t"
      "buffer_load_dwordx4 v[104:107], %[vh0], %[rs], %[t1] offen offset:1024 sc1\n\t"
      "s_waitcnt lgkmcnt(10)\n\t"
      "v_mfma_f32_16x16x32_bf16 a[48:51], v[56:59], v[120:123], a[48:51]\n\t"
      "ds_read_b64_tr_b16 v[48:49], %[w0] offset:512\n\t"
      "v_mfma_f32_16x16x32_bf16 a[52:55], v[56:59], v[124:127], a[52:55]\n\t"
      "ds_read_b64_tr_b16 v[50:51], %[w0] offset:1536\n\t"
      "buffer_load_dwordx4 v[108:111], %[vh1], %[rs], %[t1] offen offset:1024 sc1\n\t"
      "s_waitcnt lgkmcnt(10)\n\t"
      "v_mfma_f32_16x16x32_bf16 a[56:59], v[60:63], v[120:123], a[56:59]\n\t"
      "ds_read_b64_tr_b16 v[52:53], %[w1] offset:512\n\t"
      "v_mfma_f32_16x16x32_bf16 a[60:63], v[60:63], v[124:127], a[60:63]\n\t"
      "ds_read_b64_tr_b16 v[54:55], %[w1] offset:1536\n\t"
      "s_waitcnt vmcnt(13)\n\t"
      "s_waitcnt lgkmcnt(10)\n\t"
      "v_mfma_f32_16x16x32_bf16 a[0:3], v[32:35], v[64:67], a[0:3]\n\t"
      "ds_read_b64_tr_b16 v[56:57], %[w0] offset:768\n\t"
      "v_mfma_f32_16x16x32_bf16 a[4:7], v[32:35], v[68:71], a[4:7]\n\t"
      "ds_read_b64_tr_b16 v[58:59], %[w0] offset:1792\n\t"
      "s_waitcnt lgkmcnt(10)\n\t"
      "v_mfma_f32_16x16x32_bf16 a[8:11], v[36:39], v[64:67], a[8:11]\n\t"
      "ds_read_b64_tr_b16 v[60:61], %[w1] offset:768\n\t"
      "v_accvgpr_read_b32 v16, a112\n\t"
      "v_accvgpr_read_b32 v17, a113\n\t"
      "v_mfma_f32_16x16x32_bf16 a[12:15], v[36:39], v[68:71], a[12:15]\n\t"
      "ds_read_b64_tr_b16 v[62:63], %[w1] offset:1792\n\t"
      "s_waitcnt lgkmcnt(10)\n\t"
      "v_mfma_f32_16x16x32_bf16 a[16:19], v[40:43], v[64:67], a[16:19]\n\t"
      "ds_read_b64_tr_b16 v[32:33], %[w0] offset:8192\n\t"
      "v_accvgpr_read_b32 v18, a114\n\t"
      "v_accvgpr_read_b32 v19, a115\n\t"
      "v_mfma_f32_16x16x32_bf16 a[20:23], v[40:43], v[68:71], a[20:23]\n\t"
      "ds_read_b64_tr_b16 v[34:35], %[w0] offset:9216\n\t"
      "s_waitcnt lgkmcnt(10)\n\t"
      "v_mfma_f32_16x16x32_bf16 a[24:27], v[44:47], v[64:67], a[24:27]\n\t"
      "ds_read_b64_tr_b16 v[36:37], %[w1] offset:8192\n\t"
      "v_accvgpr_read_b32 v20, a116\n\t"
      "v_accvgpr_read_b32 v21, a117\n\t"
      "v_mfma_f32_16x16x32_bf16 a[28:31], v[44:47], v[68:71], a[28:31]\n\t"
      "ds_read_b64_tr_b16 v[38:39], %[w1] offset:9216\n\t"
      "s_waitcnt lgkmcnt(10)\n\t"
      "v_mfma_f32_16x16x32_bf16 a[32:35], v[48:51], v[64:67], a[32:35]\n\t"
      "ds_read_b64_tr_b16 v[40:41], %[w0] offset:8448\n\t"
      "v_accvgpr_read_b32 v22, a118\n\t"
      "v_accvgpr_read_b32 v23, a119\n\t"
      "v_mfma_f32_16x16x32_bf16 a[36:39], v[48:51], v[68:71], a[36:39]\n\t"
      "ds_read_b64_tr_b16 v[42:43], %[w0] offset:9472\n\t"
      "s_waitcnt lgkmcnt(10)\n\t"
      "v_mfma_f32_16x16x32_bf16 a[40:43], v[52:55], v[64:67], a[40:43]\n\t"
      "ds_read_b64_tr_b16 v[44:45], %[w1] offset:8448\n\t"
      "v_cvt_pk_bf16_f32 v16, v16, v17\n\t"
      "v_cvt_pk_bf16_f32 v17, v18, v19\n\t"
      "v_mfma_f32_16x16x32_bf16 a[44:47], v[52:55], v[68:71], a[44:47]\n\t"
      "ds_read_b64_tr_b16 v[46:47], %[w1] offset:9472\n\t"
      "s_waitcnt lgkmcnt(10)\n\t"
      "v_mfma_f32_16x16x32_bf16 a[48:51], v[56:59], v[64:67], a[48:51]\n\t"
      "ds_read_b64_tr_b16 v[48:49], %[w0] offset:8704\n\t"
      "v_cvt_pk_bf16_f32 v18, v20, v21\n\t"
      "v_cvt_pk_bf16_f32 v19, v22, v23\n\t"
      "v_mfma_f32_16x16x32_bf16 a[52:55], v[56:59], v[68:71], a[52:55]\n\t"
      "ds_read_b64_tr_b16 v[50:51], %[w0] offset:9728\n\t"
      "s_add_u32 %[t0], %[so1], 6144\n\t"
      "buffer_store_dwordx4 v[16:19], %[vh0], %[rs], %[t0] offen sc1\n\t"
      "s_waitcnt lgkmcnt(10)\n\t"
      "v_mfma_f32_16x16x32_bf16 a[56:59], v[60:63], v[64:67], a[56:59]\n\t"
      "ds_read_b64_tr_b16 v[52:53], %[w1] offset:8704\n\t"
      "v_mfma_f32_16x16x32_bf16 a[60:63], v[60:63], v[68:71], a[60:63]\n\t"
      "ds_read_b64_tr_b16 v[54:55], %[w1] offset:9728\n\t"
      "s_waitcnt vmcnt(26)\n\t"
      "s_barrier\n\t"
      "s_waitcnt lgkmcnt(10)\n\t"
      "v_mfma_f32_16x16x32_bf16 a[0:3], v[32:35], v[72:75], a[0:3]\n\t"
      "ds_read_b64_tr_b16 v[56:57], %[w0] offset:8960\n\t"
      "v_mfma_f32_16x16x32_bf16 a[4:7], v[32:35], v[76:79], a[4:7]\n\t"
      "ds_read_b64_tr_b16 v[58:59], %[w0] offset:9984\n\t"
      "s_waitcnt lgkmcnt(10)\n\t"
      "v_mfma_f32_16x16x32_bf16 a[8:11], v[36:39], v[72:75], a[8:11]\n\t"
      "ds_read_b64_tr_b16 v[60:61], %[w1] offset:8960\n\t"
      "v_accvgpr_read_b32 v24, a120\n\t"
      "v_accvgpr_read_b32 v25, a121\n\t"
      "v_mfma_f32_16x16x32_bf16 a[12:15], v[36:39], v[76:79], a[12:15]\n\t"
      "ds_read_b64_tr_b16 v[62:63], %[w1] offset:9984\n\t"
      "s_waitcnt lgkmcnt(10)\n\t"
      "v_mfma_f32_16x16x32_bf16 a[16:19], v[40:43], v[72:75], a[16:19]\n\t"
      "ds_read_b64_tr_b16 v[32:33], %[w0] offset:16384\n\t"
      "v_accvgpr_read_b32 v26, a122\n\t"
      "v_accvgpr_read_b32 v27, a123\n\t"
      "v_mfma_f32_16x16x32_bf16 a[20:23], v[40:43], v[76:79], a[20:23]\n\t"
      "ds_read_b64_tr_b16 v[34:35], %[w0] offset:17408\n\t"
      "s_waitcnt lgkmcnt(10)\n\t"
      "v_mfma_f32_16x16x32_bf16 a[24:27], v[44:47], v[72:75], a[24:27]\n\t"
      "ds_read_b64_tr_b16 v[36:37], %[w1] offset:16384\n\t"
      "v_accvgpr_read_b32 v28, a124\n\t"
      "v_accvgpr_read_b32 v29, a125\n\t"
      "v_mfma_f32_16x16x32_bf16 a[28:31], v[44:47], v[76:79], a[28:31]\n\t"
      "ds_read_b64_tr_b16 v[38:39], %[w1] offset:17408\n\t"
      "s_waitcnt lgkmcnt(10)\n\t"
      "v_mfma_f32_16x16x32_bf16 a[32:35], v[48:51], v[72:75], a[32:35]\n\t"
      "ds_read_b64_tr_b16 v[40:41], %[w0] offset:16640\n\t"
      "v_accvgpr_read_b32 v30, a126\n\t"
      "v_accvgpr_read_b32 v31, a127\n\t"
      "v_mfma_f32_16x16x32_bf16 a[36:39], v[48:51], v[76:79], a[36:39]\n\t"
      "ds_read_b64_tr_b16 v[42:43], %[w0] offset:17664\n\t"
      "s_waitcnt lgkmcnt(10)\n\t"
      "v_mfma_f32_16x16x32_bf16 a[40:43], v[52:55], v[72:75], a[40:43]\n\t"
      "ds_read_b64_tr_b16 v[44:45], %[w1] offset:16640\n\t"
      "v_cvt_pk_bf16_f32 v24, v24, v25\n\t"
      "v_cvt_pk_bf16_f32 v25, v26, v27\n\t"
      "v_mfma_f32_16x16x32_bf16 a[44:47], v[52:55], v[76:79], a[44:47]\n\t"
      "ds_read_b64_tr_b16 v[46:47], %[w1] offset:17664\n\t"
      "s_waitcnt lgkmcnt(10)\n\t"
      "v_mfma_f32_16x16x32_bf16 a[48:51], v[56:59], v[72:75], a[48:51]\n\t"
      "ds_read_b64_tr_b16 v[48:49], %[w0] offset:16896\n\t"
      "v_cvt_pk_bf16_f32 v26, v28, v29\n\t"
      "v_cvt_pk_bf16_f32 v27, v30, v31\n\t"
      "s_add_u32 %[t0], %[so1], 7168\n\t"
      "buffer_store_dwordx4 v[24:27], %[vh0], %[rs], %[t0] offen sc1\n\t"
      "v_mfma_f32_16x16x32_bf16 a[52:55], v[56:59], v[76:79], a[52:55]\n\t"
      "ds_read_b64_tr_b16 v[50:51], %[w0] offset:17920\n\t"
      "s_add_u32 %[t1], %[sh], 14336\n\t"
      "buffer_load_dwordx4 v[112:115], %[vh0], %[rs], %[t1] offen offset:0 sc1\n\t"
      "s_waitcnt lgkmcnt(10)\n\t"
      "v_mfma_f32_16x16x32_bf16 a[56:59], v[60:63], v[72:75], a[56:59]\n\t"
      "ds_read_b64_tr_b16 v[52:53], %[w1] offset:16896\n\t"
      "buffer_load_dwordx4 v[116:119], %[vh1], %[rs], %[t1] offen offset:0 sc1\n\t"
      "v_mfma_f32_16x16x32_bf16 a[60:63], v[60:63], v[76:79], a[60:63]\n\t"
      "ds_read_b64_tr_b16 v[54:55], %[w1] offset:17920\n\t"
      "buffer_load_dwordx4 v[120:123], %[vh0], %[rs], %[t1] offen offset:1024 sc1\n\t"
      "buffer_load_dwordx4 v[124:127], %[vh1], %[rs], %[t1] offen offset:1024 sc1\n\t"
      "s_waitcnt vmcnt(11)\n\t"
      "s_waitcnt lgkmcnt(10)\n\t"
      "v_mfma_f32_16x16x32_bf16 a[0:3], v[32:35], v[80:83], a[0:3]\n\t"
      "ds_read_b64_tr_b16 v[56:57], %[w0] offset:17152\n\t"
      "v_mfma_f32_16x16x32_bf16 a[4:7], v[32:35], v[84:87], a[4:7]\n\t"
      "ds_read_b64_tr_b16 v[58:59], %[w0] offset:18176\n\t"
      "s_waitcnt lgkmcnt(10)\n\t"
      "v_mfma_f32_16x16x32_bf16 a[8:11], v[36:39], v[80:83], a[8:11]\n\t"
      "ds_read_b64_tr_b16 v[60:61], %[w1] offset:17152\n\t"
      "v_mfma_f32_16x16x32_bf16 a[12:15], v[36:39], v[84:87], a[12:15]\n\t"
      "ds_read_b64_tr_b16 v[62:63], %[w1] offset:18176\n\t"
      "s_waitcnt lgkmcnt(10)\n\t"
      "v_mfma_f32_16x16x32_bf16 a[16:19], v[40:43], v[80:83], a[16:19]\n\t"
      "ds_read_b64_tr_b16 v[32:33], %[w0] offset:24576\n\t"
      "v_mfma_f32_16x16x32_bf16 a[20:23], v[40:43], v[84:87], a[20:23]\n\t"
      "ds_read_b64_tr_b16 v[34:35], %[w0] offset:25600\n\t"
      "s_waitcnt lgkmcnt(10)\n\t"
      "v_mfma_f32_16x16x32_bf16 a[24:27], v[44:47], v[80:83], a[24:27]\n\t"
      "ds_read_b64_tr_b16 v[36:37], %[w1] offset:24576\n\t"
      "v_mfma_f32_16x16x32_bf16 a[28:31], v[44:47], v[84:87], a[28:31]\n\t"
      "ds_read_b64_tr_b16 v[38:39], %[w1] offset:25600\n\t"
      "s_waitcnt lgkmcnt(10)\n\t"
      "v_mfma_f32_16x16x32_bf16 a[32:35], v[48:51], v[80:83], a[32:35]\n\t"
      "ds_read_b64_tr_b16 v[40:41], %[w0] offset:24832\n\t"
      "v_mfma_f32_16x16x32_bf16 a[36:39], v[48:51], v[84:87], a[36:39]\n\t"
      "ds_read_b64_tr_b16 v[42:43], %[w0] offset:25856\n\t"
      "s_waitcnt lgkmcnt(10)\n\t"
      "v_mfma_f32_16x16x32_bf16 a[40:43], v[52:55], v[80:83], a[40:43]\n\t"
      "ds_read_b64_tr_b16 v[44:45], %[w1] offset:24832\n\t"
      "v_mfma_f32_16x16x32_bf16 a[44:47], v[52:55], v[84:87], a[44:47]\n\t"
      "ds_read_b64_tr_b16 v[46:47], %[w1] offset:25856\n\t"
      "s_waitcnt lgkmcnt(10)\n\t"
      "v_mfma_f32_16x16x32_bf16 a[48:51], v[56:59], v[80:83], a[48:51]\n\t"
      "ds_read_b64_tr_b16 v[48:49], %[w0] offset:25088\n\t"
      "v_mfma_f32_16x16x32_bf16 a[52:55], v[56:59], v[84:87], a[52:55]\n\t"
      "ds_read_b64_tr_b16 v[50:51], %[w0] offset:26112\n\t"
      "s_waitcnt lgkmcnt(10)\n\t"
      "v_mfma_f32_16x16x32_bf16 a[56:59], v[60:63], v[80:83], a[56:59]\n\t"
      "ds_read_b64_tr_b16 v[52:53], %[w1] offset:25088\n\t"
      "v_mfma_f32_16x16x32_bf16 a[60:63], v[60:63], v[84:87], a[60:63]\n\t"
      "ds_read_b64_tr_b16 v[54:55], %[w1] offset:26112\n\t"
      "s_waitcnt vmcnt(23)\n\t"
      "s_barrier\n\t"
      "s_waitcnt lgkmcnt(10)\n\t"
      "v_mfma_f32_16x16x32_bf16 a[0:3], v[32:35], v[88:91], a[0:3]\n\t"
      "ds_read_b64_tr_b16 v[56:57], %[w0] offset:25344\n\t"
      "v_mfma_f32_16x16x32_bf16 a[4:7], v[32:35], v[92:95], a[4:7]\n\t"
      "ds_read_b64_tr_b16 v[58:59], %[w0] offset:26368\n\t"
      "s_waitcnt lgkmcnt(10)\n\t"
      "v_mfma_f32_16x16x32_bf16 a[8:11], v[36:39], v[88:91], a[8:11]\n\t"
      "ds_read_b64_tr_b16 v[60:61], %[w1] offset:25344\n\t"
      "v_mfma_f32_16x16x32_bf16 a[12:15], v[36:39], v[92:95], a[12:15]\n\t"
      "ds_read_b64_tr_b16 v[62:63], %[w1] offset:26368\n\t"
      "s_waitcnt lgkmcnt(10)\n\t"
      "v_mfma_f32_16x16x32_bf16 a[16:19], v[40:43], v[88:91], a[16:19]\n\t"
      "ds_read_b64_tr_b16 v[32:33], %[w0] offset:32768\n\t"
      "v_mfma_f32_16x16x32_bf16 a[20:23], v[40:43], v[92:95], a[20:23]\n\t"
      "ds_read_b64_tr_b16 v[34:35], %[w0] offset:33792\n\t"
      "s_waitcnt lgkmcnt(10)\n\t"
      "v_mfma_f32_16x16x32_bf16 a[24:27], v[44:47], v[88:91], a[24:27]\n\t"
      "ds_read_b64_tr_b16 v[36:37], %[w1] offset:32768\n\t"
      "v_mfma_f32_16x16x32_bf16 a[28:31], v[44:47], v[92:95], a[28:31]\n\t"
      "ds_read_b64_tr_b16 v[38:39], %[w1] offset:33792\n\t"
      "buffer_load_dword %[pv], off, %[rs], %[fk] sc1\n\t"
      "s_waitcnt lgkmcnt(10)\n\t"
      "v_mfma_f32_16x16x32_bf16 a[32:35], v[48:51], v[88:91], a[32:35]\n\t"
      "ds_read_b64_tr_b16 v[40:41], %[w0] offset:33024\n\t"
      "v_mfma_f32_16x16x32_bf16 a[36:39], v[48:51], v[92:95], a[36:39]\n\t"
      "ds_read_b64_tr_b16 v[42:43], %[w0] offset:34048\n\t"
      "s_waitcnt lgkmcnt(10)\n\t"
      "v_mfma_f32_16x16x32_bf16 a[40:43], v[52:55], v[88:91], a[40:43]\n\t"
      "ds_read_b64_tr_b16 v[44:45], %[w1] offset:33024\n\t"
      "v_mfma_f32_16x16x32_bf16 a[44:47], v[52:55], v[92:95], a[44:47]\n\t"
      "ds_read_b64_tr_b16 v[46:47], %[w1] offset:34048\n\t"
      "s_waitcnt lgkmcnt(10)\n\t"
      "v_mfma_f32_16x16x32_bf16 a[48:51], v[56:59], v[88:91], a[48:51]\n\t"
      "ds_read_b64_tr_b16 v[48:49], %[w0] offset:33280\n\t"
      "v_mfma_f32_16x16x32_bf16 a[52:55], v[56:59], v[92:95], a[52:55]\n\t"
      "ds_read_b64_tr_b16 v[50:51], %[w0] offset:34304\n\t"
      "s_waitcnt lgkmcnt(10)\n\t"
      "v_mfma_f32_16x16x32_bf16 a[56:59], v[60:63], v[88:91], a[56:59]\n\t"
      "ds_read_b64_tr_b16 v[52:53], %[w1] offset:33280\n\t"
      "v_mfma_f32_16x16x32_bf16 a[60:63], v[60:63], v[92:95], a[60:63]\n\t"
      "ds_read_b64_tr_b16 v[54:55], %[w1] offset:34304\n\t"
      "s_waitcnt vmcnt(7)\n\t"
      "s_waitcnt vmcnt(0)\n\t"
      "v_readfirstlane_b32 %[t0], %[pv]\n\t"
      "s_cmp_lt_u32 %[t0], 8\n\t"
      "s_cbranch_scc1 .Lw2%=\n\t"
      ".Lr2%=:\n\t"
      "s_waitcnt lgkmcnt(10)\n\t"
      "v_mfma_f32_16x16x32_bf16 a[0:3], v[32:35], v[96:99], a[0:3]\n\t"
      "ds_read_b64_tr_b16 v[56:57], %[w0] offset:33536\n\t"
      "v_mfma_f32_16x16x32_bf16 a[4:7], v[32:35], v[100:103], a[4:7]\n\t"
      "ds_read_b64_tr_b16 v[58:59], %[w0] offset:34560\n\t"
      "s_waitcnt lgkmcnt(10)\n\t"
      "v_mfma_f32_16x16x32_bf16 a[8:11], v[36:39], v[96:99], a[8:11]\n\t"
      "ds_read_b64_tr_b16 v[60:61], %[w1] offset:33536\n\t"
      "v_mfma_f32_16x16x32_bf16 a[12:15], v[36:39], v[100:103], a[12:15]\n\t"
      "ds_read_b64_tr_b16 v[62:63], %[w1] offset:34560\n\t"
      "s_waitcnt lgkmcnt(10)\n\t"
      "v_mfma_f32_16x16x32_bf16 a[16:19], v[40:43], v[96:99], a[16:19]\n\t"
      "ds_read_b64_tr_b16 v[32:33], %[w0] offset:40960\n\t"
      "v_mfma_f32_16x16x32_bf16 a[20:23], v[40:43], v[100:103], a[20:23]\n\t"
      "ds_read_b64_tr_b16 v[34:35], %[w0] offset:41984\n\t"
      "s_waitcnt lgkmcnt(10)\n\t"
      "v_mfma_f32_16x16x32_bf16 a[24:27], v[44:47], v[96:99], a[24:27]\n\t"
      "ds_read_b64_tr_b16 v[36:37], %[w1] offset:40960\n\t"
      "v_mfma_f32_16x16x32_bf16 a[28:31], v[44:47], v[100:103], a[28:31]\n\t"
      "ds_read_b64_tr_b16 v[38:39], %[w1] offset:41984\n\t"
      "s_add_u32 %[t0], %[skv], 0\n\t"
      "s_add_u32 m0, %[dkv], 0\n\t"
      "s_nop 0\n\t"
      "buffer_load_dwordx4 %[vh0], %[rs], %[t0] offen offset:0 sc1 lds\n\t"
      "buffer_load_dwordx4 %[vh0], %[rs], %[t0] offen offset:1024 sc1 lds\n\t"
      "s_waitcnt lgkmcnt(10)\n\t"
      "v_mfma_f32_16x16x32_bf16 a[32:35], v[48:51], v[96:99], a[32:35]\n\t"
      "ds_read_b64_tr_b16 v[40:41], %[w0] offset:41216\n\t"
      "v_mfma_f32_16x16x32_bf16 a[36:39], v[48:51], v[100:103], a[36:39]\n\t"
      "ds_read_b64_tr_b16 v[42:43], %[w0] offset:42240\n\t"
      "s_add_u32 %[t0], %[skv], 16384\n\t"
      "s_add_u32 m0, %[dkv], 16384\n\t"
      "s_nop 0\n\t"
      "buffer_load_dwordx4 %[vh0], %[rs], %[t0] offen offset:0 sc1 lds\n\t"
      "buffer_load_dwordx4 %[vh0], %[rs], %[t0] offen offset:1024 sc1 lds\n\t"
      "s_waitcnt lgkmcnt(10)\n\t"
      "v_mfma_f32_16x16x32_bf16 a[40:43], v[52:55], v[96:99], a[40:43]\n\t"
      "ds_read_b64_tr_b16 v[44:45], %[w1] offset:41216\n\t"
      "v_mfma_f32_16x16x32_bf16 a[44:47], v[52:55], v[100:103], a[44:47]\n\t"
      "ds_read_b64_tr_b16 v[46:47], %[w1] offset:42240\n\t"
      "s_waitcnt lgkmcnt(10)\n\t"
      "v_mfma_f32_16x16x32_bf16 a[48:51], v[56:59], v[96:99], a[48:51]\n\t"
      "ds_read_b64_tr_b16 v[48:49], %[w0] offset:41472\n\t"
      "v_mfma_f32_16x16x32_bf16 a[52:55], v[56:59], v[100:103], a[52:55]\n\t"
      "ds_read_b64_tr_b16 v[50:51], %[w0] offset:42496\n\t"
      "s_waitcnt lgkmcnt(10)\n\t"
      "v_mfma_f32_16x16x32_bf16 a[56:59], v[60:63], v[96:99], a[56:59]\n\t"
      "ds_read_b64_tr_b16 v[52:53], %[w1] offset:41472\n\t"
      "v_mfma_f32_16x16x32_bf16 a[60:63], v[60:63], v[100:103], a[60:63]\n\t"
      "ds_read_b64_tr_b16 v[54:55], %[w1] offset:42496\n\t"
      "s_waitcnt vmcnt(9)\n\t"
      "s_barrier\n\t"
      "s_cmp_lg_u32 %[wid], 0\n\t"
      "s_cbranch_scc1 .Ls1%=\n\t"
      "s_mov_b64 exec, 1\n\t"
      "v_mov_b32 v31, 1\n\t"
      "buffer_atomic_add v31, off, %[rs], %[fkv]\n\t"
      "s_mov_b64 exec, -1\n\t"
      ".Ls1%=:\n\t"
      "s_waitcnt lgkmcnt(10)\n\t"
      "v_mfma_f32_16x16x32_bf16 a[0:3], v[32:35], v[104:107], a[0:3]\n\t"
      "ds_read_b64_tr_b16 v[56:57], %[w0] offset:41728\n\t"
      "v_mfma_f32_16x16x32_bf16 a[4:7], v[32:35], v[108:111], a[4:7]\n\t"
      "ds_read_b64_tr_b16 v[58:59], %[w0] offset:42752\n\t"
      "s_waitcnt lgkmcnt(10)\n\t"
      "v_mfma_f32_16x16x32_bf16 a[8:11], v[36:39], v[104:107], a[8:11]\n\t"
      "ds_read_b64_tr_b16 v[60:61], %[w1] offset:41728\n\t"
      "v_mfma_f32_16x16x32_bf16 a[12:15], v[36:39], v[108:111], a[12:15]\n\t"
      "ds_read_b64_tr_b16 v[62:63], %[w1] offset:42752\n\t"
      "s_waitcnt lgkmcnt(10)\n\t"
      "v_mfma_f32_16x16x32_bf16 a[16:19], v[40:43], v[104:107], a[16:19]\n\t"
      "ds_read_b64_tr_b16 v[32:33], %[w0] offset:49152\n\t"
      "v_mfma_f32_16x16x32_bf16 a[20:23], v[40:43], v[108:111], a[20:23]\n\t"
      "ds_read_b64_tr_b16 v[34:35], %[w0] offset:50176\n\t"
      "s_waitcnt lgkmcnt(10)\n\t"
      "v_mfma_f32_16x16x32_bf16 a[24:27], v[44:47], v[104:107], a[24:27]\n\t"
      "ds_read_b64_tr_b16 v[36:37], %[w1] offset:49152\n\t"
      "v_mfma_f32_16x16x32_bf16 a[28:31], v[44:47], v[108:111], a[28:31]\n\t"
      "ds_read_b64_tr_b16 v[38:39], %[w1] offset:50176\n\t"
      "s_waitcnt lgkmcnt(10)\n\t"
      "v_mfma_f32_16x16x32_bf16 a[32:35], v[48:51], v[104:107], a[32:35]\n\t"
      "ds_read_b64_tr_b16 v[40:41], %[w0] offset:49408\n\t"
      "v_mfma_f32_16x16x32_bf16 a[36:39], v[48:51], v[108:111], a[36:39]\n\t"
      "ds_read_b64_tr_b16 v[42:43], %[w0] offset:50432\n\t"
      "s_waitcnt lgkmcnt(10)\n\t"
      "v_mfma_f32_16x16x32_bf16 a[40:43], v[52:55], v[104:107], a[40:43]\n\t"
      "ds_read_b64_tr_b16 v[44:45], %[w1] offset:49408\n\t"
      "v_mfma_f32_16x16x32_bf16 a[44:47], v[52:55], v[108:111], a[44:47]\n\t"
      "ds_read_b64_tr_b16 v[46:47], %[w1] offset:50432\n\t"
      "s_waitcnt lgkmcnt(10)\n\t"
      "v_mfma_f32_16x16x32_bf16 a[48:51], v[56:59], v[104:107], a[48:51]\n\t"
      "ds_read_b64_tr_b16 v[48:49], %[w0] offset:49664\n\t"
      "v_mfma_f32_16x16x32_bf16 a[52:55], v[56:59], v[108:111], a[52:55]\n\t"
      "ds_read_b64_tr_b16 v[50:51], %[w0] offset:50688\n\t"
      "s_waitcnt lgkmcnt(10)\n\t"
      "v_mfma_f32_16x16x32_bf16 a[56:59], v[60:63], v[104:107], a[56:59]\n\t"
      "ds_read_b64_tr_b16 v[52:53], %[w1] offset:49664\n\t"
      "v_mfma_f32_16x16x32_bf16 a[60:63], v[60:63], v[108:111], a[60:63]\n\t"
      "ds_read_b64_tr_b16 v[54:55], %[w1] offset:50688\n\t"
      "s_waitcnt vmcnt(5)\n\t"
      "s_waitcnt lgkmcnt(10)\n\t"
      "v_mfma_f32_16x16x32_bf16 a[0:3], v[32:35], v[112:115], a[0:3]\n\t"
      "ds_read_b64_tr_b16 v[56:57], %[w0] offset:49920\n\t"
      "v_mfma_f32_16x16x32_bf16 a[4:7], v[32:35], v[116:119], a[4:7]\n\t"
      "ds_read_b64_tr_b16 v[58:59], %[w0] offset:50944\n\t"
      "s_waitcnt lgkmcnt(10)\n\t"
      "v_mfma_f32_16x16x32_bf16 a[8:11], v[36:39], v[112:115], a[8:11]\n\t"
      "ds_read_b64_tr_b16 v[60:61], %[w1] offset:49920\n\t"
      "v_mfma_f32_16x16x32_bf16 a[12:15], v[36:39], v[116:119], a[12:15]\n\t"
      "ds_read_b64_tr_b16 v[62:63], %[w1] offset:50944\n\t"
      "s_waitcnt lgkmcnt(10)\n\t"
      "v_mfma_f32_16x16x32_bf16 a[16:19], v[40:43], v[112:115], a[16:19]\n\t"
      "ds_read_b64_tr_b16 v[32:33], %[w0] offset:57344\n\t"
      "v_mfma_f32_16x16x32_bf16 a[20:23], v[40:43], v[116:119], a[20:23]\n\t"
      "ds_read_b64_tr_b16 v[34:35], %[w0] offset:58368\n\t"
      "s_waitcnt lgkmcnt(10)\n\t"
      "v_mfma_f32_16x16x32_bf16 a[24:27], v[44:47], v[112:115], a[24:27]\n\t"
      "ds_read_b64_tr_b16 v[36:37], %[w1] offset:57344\n\t"
      "v_mfma_f32_16x16x32_bf16 a[28:31], v[44:47], v[116:119], a[28:31]\n\t"
      "ds_read_b64_tr_b16 v[38:39], %[w1] offset:58368\n\t"
      "s_waitcnt lgkmcnt(10)\n\t"
      "v_mfma_f32_16x16x32_bf16 a[32:35], v[48:51], v[112:115], a[32:35]\n\t"
      "ds_read_b64_tr_b16 v[40:41], %[w0] offset:57600\n\t"
      "v_mfma_f32_16x16x32_bf16 a[36:39], v[48:51], v[116:119], a[36:39]\n\t"
      "ds_read_b64_tr_b16 v[42:43], %[w0] offset:58624\n\t"
      "s_waitcnt lgkmcnt(10)\n\t"
      "v_mfma_f32_16x16x32_bf16 a[40:43], v[52:55], v[112:115], a[40:43]\n\t"
      "ds_read_b64_tr_b16 v[44:45], %[w1] offset:57600\n\t"
      "v_mfma_f32_16x16x32_bf16 a[44:47], v[52:55], v[116:119], a[44:47]\n\t"
      "ds_read_b64_tr_b16 v[46:47], %[w1] offset:58624\n\t"
      "s_waitcnt lgkmcnt(10)\n\t"
      "v_mfma_f32_16x16x32_bf16 a[48:51], v[56:59], v[112:115], a[48:51]\n\t"
      "ds_read_b64_tr_b16 v[48:49], %[w0] offset:57856\n\t"
      "v_mfma_f32_16x16x32_bf16 a[52:55], v[56:59], v[116:119], a[52:55]\n\t"
      "ds_read_b64_tr_b16 v[50:51], %[w0] offset:58880\n\t"
      "s_waitcnt lgkmcnt(10)\n\t"
      "v_mfma_f32_16x16x32_bf16 a[56:59], v[60:63], v[112:115], a[56:59]\n\t"
      "ds_read_b64_tr_b16 v[52:53], %[w1] offset:57856\n\t"
      "v_mfma_f32_16x16x32_bf16 a[60:63], v[60:63], v[116:119], a[60:63]\n\t"
      "ds_read_b64_tr_b16 v[54:55], %[w1] offset:58880\n\t"
      "s_waitcnt lgkmcnt(10)\n\t"
      "v_mfma_f32_16x16x32_bf16 a[0:3], v[32:35], v[120:123], a[0:3]\n\t"
      "ds_read_b64_tr_b16 v[56:57], %[w0] offset:58112\n\t"
      "v_mfma_f32_16x16x32_bf16 a[4:7], v[32:35], v[124:127], a[4:7]\n\t"
      "ds_read_b64_tr_b16 v[58:59], %[w0] offset:59136\n\t"
      "s_waitcnt lgkmcnt(10)\n\t"
      "v_mfma_f32_16x16x32_bf16 a[8:11], v[36:39], v[120:123], a[8:11]\n\t"
      "ds_read_b64_tr_b16 v[60:61], %[w1] offset:58112\n\t"
      "v_mfma_f32_16x16x32_bf16 a[12:15], v[36:39], v[124:127], a[12:15]\n\t"
      "ds_read_b64_tr_b16 v[62:63], %[w1] offset:59136\n\t"
      "s_waitcnt lgkmcnt(10)\n\t"
      "v_mfma_f32_16x16x32_bf16 a[16:19], v[40:43], v[120:123], a[16:19]\n\t"
      "v_mfma_f32_16x16x32_bf16 a[20:23], v[40:43], v[124:127], a[20:23]\n\t"
      "s_waitcnt lgkmcnt(8)\n\t"
      "v_mfma_f32_16x16x32_bf16 a[24:27], v[44:47], v[120:123], a[24:27]\n\t"
      "v_mfma_f32_16x16x32_bf16 a[28:31], v[44:47], v[124:127], a[28:31]\n\t"
      "s_waitcnt lgkmcnt(6)\n\t"
      "v_mfma_f32_16x16x32_bf16 a[32:35], v[48:51], v[120:123], a[32:35]\n\t"
      "v_mfma_f32_16x16x32_bf16 a[36:39], v[48:51], v[124:127], a[36:39]\n\t"
      "s_waitcnt lgkmcnt(4)\n\t"
      "v_mfma_f32_16x16x32_bf16 a[40:43], v[52:55], v[120:123], a[40:43]\n\t"
      "v_mfma_f32_16x16x32_bf16 a[44:47], v[52:55], v[124:127], a[44:47]\n\t"
      "s_waitcnt lgkmcnt(2)\n\t"
      "v_mfma_f32_16x16x32_bf16 a[48:51], v[56:59], v[120:123], a[48:51]\n\t"
      "v_mfma_f32_16x16x32_bf16 a[52:55], v[56:59], v[124:127], a[52:55]\n\t"
      "s_waitcnt lgkmcnt(0)\n\t"
      "v_mfma_f32_16x16x32_bf16 a[56:59], v[60:63], v[120:123], a[56:59]\n\t"
      "v_mfma_f32_16x16x32_bf16 a[60:63], v[60:63], v[124:127], a[60:63]\n\t"
      "s_nop 7\n\t"
      "s_branch .Lx%=\n\t"
      ".Lw1%=:\n\t"
      "s_sleep 2\n\t"
      "buffer_load_dword %[pv], off, %[rs], %[ph] sc1\n\t"
      "s_waitcnt vmcnt(0)\n\t"
      "v_readfirstlane_b32 %[t0], %[pv]\n\t"
      "s_cmp_lt_u32 %[t0], 16\n\t"
      "s_cbranch_scc1 .Lw1%=\n\t"
      "s_branch .Lr1%=\n\t"
      ".Lw2%=:\n\t"
      "s_sleep 2\n\t"
      "buffer_load_dword %[pv], off, %[rs], %[fk] sc1\n\t"
      "s_waitcnt vmcnt(0)\n\t"
      "v_readfirstlane_b32 %[t0], %[pv]\n\t"
      "s_cmp_lt_u32 %[t0], 8\n\t"
      "s_cbranch_scc1 .Lw2%=\n\t"
      "s_branch .Lr2%=\n\t"
      ".Lx%=:\n\t"
      : "+{v[64:95]}"(R1), [pv] "+v"(pollv), [w0] "+v"(wB0), [w1] "+v"(wB1), [ts] "=&s"(t2), [t0] "=&s"(t0), [t1] "=&s"(t1)
      : [st] "s"((unsigned)idx * 8192u + (unsigned)xcd * 1024u + wid * 128u), [sw] "s"(sW), [dw] "s"(dW), [vw] "v"(vW), [rw0] "s"(srdWk), [sc] "s"(sC), [vh0] "v"(vL), [vh1] "v"(vL + 16384u), [rs] "s"(srdS), [rw1] "s"(srdWv), [rw2] "s"(srdWq), [sh] "s"(sH), [so0] "s"(sO0), [so1] "s"(sO1), [wid] "s"(wid), [fh] "s"(fH), [ph] "s"(pH), [fkv] "s"(fKV), [fk] "s"(fKV + (F_K - 16u) * 128u), [skv] "s"(sKV), [dkv] "s"(dKV), [px] "s"(pX), [vxn] "v"((lane >> 3) * 256u + (lane & 7u) * 16u), [sxn] "s"((unsigned)WS_XN + (unsigned)(b >> 1) * 16384u + wid * 2048u + (unsigned)(b & 1) * 128u), [vhw] "v"(lds0 + L_Q + wid * 1024u + (vL ^ (wid * 16u))), [vhr] "v"(lds0 + L_Q + (lane & 7u) * 1024u + wid * 128u + ((lane >> 3) ^ (lane & 7u)) * 16u)
      : "memory", "scc", ACLOBQ, "v16", "v17", "v18", "v19", "v20", "v21", "v22", "v23", "v24", "v25", "v26", "v27", "v28", "v29", "v30", "v31", "v32", "v33", "v34", "v35", "v36", "v37", "v38", "v39", "v40", "v41", "v42", "v43", "v44", "v45", "v46", "v47", "v48", "v49", "v50", "v51", "v52", "v53", "v54", "v55", "v56", "v57", "v58", "v59", "v60", "v61", "v62", "v63", "v96", "v97", "v98", "v99", "v100", "v101", "v102", "v103", "v104", "v105", "v106", "v107", "v108", "v109", "v110", "v111", "v112", "v113", "v114", "v115", "v116", "v117", "v118", "v119", "v120", "v121", "v122", "v123", "v124", "v125", "v126", "v127");
    RT(1, 1);
    { f32x4 t0, t1; asm volatile("v_accvgpr_read_b32 %0, a0\n\tv_accvgpr_read_b32 %1, a1\n\tv_accvgpr_read_b32 %2, a2\n\tv_accvgpr_read_b32 %3, a3" : "=v"(t0[0]), "=v"(t0[1]), "=v"(t0[2]), "=v"(t0[3])); asm volatile("v_accvgpr_read_b32 %0, a8\n\tv_accvgpr_read_b32 %1, a9\n\tv_accvgpr_read_b32 %2, a10\n\tv_accvgpr_read_b32 %3, a11" : "=v"(t1[0]), "=v"(t1[1]), "=v"(t1[2]), "=v"(t1[3])); Q[0][0] = __builtin_bit_cast(bf16x8, pack2(t0, t1)); }
    { f32x4 t0, t1; asm volatile("v_accvgpr_read_b32 %0, a4\n\tv_accvgpr_read_b32 %1, a5\n\tv_accvgpr_read_b32 %2, a6\n\tv_accvgpr_read_b32 %3, a7" : "=v"(t0[0]), "=v"(t0[1]), "=v"(t0[2]), "=v"(t0[3])); asm volatile("v_accvgpr_read_b32 %0, a12\n\tv_accvgpr_read_b32 %1, a13\n\tv_accvgpr_read_b32 %2, a14\n\tv_accvgpr_read_b32 %3, a15" : "=v"(t1[0]), "=v"(t1[1]), "=v"(t1[2]), "=v"(t1[3])); Q[0][1] = __builtin_bit_cast(bf16x8, pack2(t0, t1)); }
    { f32x4 t0, t1; asm volatile("v_accvgpr_read_b32 %0, a16\n\tv_accvgpr_read_b32 %1, a17\n\tv_accvgpr_read_b32 %2, a18\n\tv_accvgpr_read_b32 %3, a19" : "=v"(t0[0]), "=v"(t0[1]), "=v"(t0[2]), "=v"(t0[3])); asm volatile("v_accvgpr_read_b32 %0, a24\n\tv_accvgpr_read_b32 %1, a25\n\tv_accvgpr_read_b32 %2, a26\n\tv_accvgpr_read_b32 %3, a27" : "=v"(t1[0]), "=v"(t1[1]), "=v"(t1[2]), "=v"(t1[3])); Q[1][0] = __builtin_bit_cast(bf16x8, pack2(t0, t1)); }
    { f32x4 t0, t1; asm volatile("v_accvgpr_read_b32 %0, a20\n\tv_accvgpr_read_b32 %1, a21\n\tv_accvgpr_read_b32 %2, a22\n\tv_accvgpr_read_b32 %3, a23" : "=v"(t0[0]), "=v"(t0[1]), "=v"(t0[2]), "=v"(t0[3])); asm volatile("v_accvgpr_read_b32 %0, a28\n\tv_accvgpr_read_b32 %1, a29\n\tv_accvgpr_read_b32 %2, a30\n\tv_accvgpr_read_b32 %3, a31" : "=v"(t1[0]), "=v"(t1[1]), "=v"(t1[2]), "=v"(t1[3])); Q[1][1] = __builtin_bit_cast(bf16x8, pack2(t0, t1)); }
    { f32x4 t0, t1; asm volatile("v_accvgpr_read_b32 %0, a32\n\tv_accvgpr_read_b32 %1, a33\n\tv_accvgpr_read_b32 %2, a34\n\tv_accvgpr_read_b32 %3, a35" : "=v"(t0[0]), "=v"(t0[1]), "=v"(t0[2]), "=v"(t0[3])); asm volatile("v_accvgpr_read_b32 %0, a40\n\tv_accvgpr_read_b32 %1, a41\n\tv_accvgpr_read_b32 %2, a42\n\tv_accvgpr_read_b32 %3, a43" : "=v"(t1[0]), "=v"(t1[1]), "=v"(t1[2]), "=v"(t1[3])); Q[2][0] = __builtin_bit_cast(bf16x8, pack2(t0, t1)); }
    { f32x4 t0, t1; asm volatile("v_accvgpr_read_b32 %0, a36\n\tv_accvgpr_read_b32 %1, a37\n\tv_accvgpr_read_b32 %2, a38\n\tv_accvgpr_read_b32 %3, a39" : "=v"(t0[0]), "=v"(t0[1]), "=v"(t0[2]), "=v"(t0[3])); asm volatile("v_accvgpr_read_b32 %0, a44\n\tv_accvgpr_read_b32 %1, a45\n\tv_accvgpr_read_b32 %2, a46\n\tv_accvgpr_read_b32 %3, a47" : "=v"(t1[0]), "=v"(t1[1]), "=v"(t1[2]), "=v"(t1[3])); Q[2][1] = __builtin_bit_cast(bf16x8, pack2(t0, t1)); }
    { f32x4 t0, t1; asm volatile("v_accvgpr_read_b32 %0, a48\n\tv_accvgpr_read_b32 %1, a49\n\tv_accvgpr_read_b32 %2, a50\n\tv_accvgpr_read_b32 %3, a51" : "=v"(t0[0]), "=v"(t0[1]), "=v"(t0[2]), "=v"(t0[3])); asm volatile("v_accvgpr_read_b32 %0, a56\n\tv_accvgpr_read_b32 %1, a57\n\tv_accvgpr_read_b32 %2, a58\n\tv_accvgpr_read_b32 %3, a59" : "=v"(t1[0]), "=v"(t1[1]), "=v"(t1[2]), "=v"(t1[3])); Q[3][0] = __builtin_bit_cast(bf16x8, pack2(t0, t1)); }
    { f32x4 t0, t1; asm volatile("v_accvgpr_read_b32 %0, a52\n\tv_accvgpr_read_b32 %1, a53\n\tv_accvgpr_read_b32 %2, a54\n\tv_accvgpr_read_b32 %3, a55" : "=v"(t0[0]), "=v"(t0[1]), "=v"(t0[2]), "=v"(t0[3])); asm volatile("v_accvgpr_read_b32 %0, a60\n\tv_accvgpr_read_b32 %1, a61\n\tv_accvgpr_read_b32 %2, a62\n\tv_accvgpr_read_b32 %3, a63" : "=v"(t1[0]), "=v"(t1[1]), "=v"(t1[2]), "=v"(t1[3])); Q[3][1] = __builtin_bit_cast(bf16x8, pack2(t0, t1)); }
    if (grp == 0) {
#pragma unroll
      for (int ds = 0; ds < 4; ++ds)
#pragma unroll
        for (int qt = 0; qt < 2; ++qt) lds_w128(lds0 + L_Q + (w4 * 8u + ds * 2u + qt) * 1024u + vL, __builtin_bit_cast(u32x4, Q[ds][qt]));
      WAIT_V(0); WAIT_L(0); BAR();
    } else
      asm volatile("buffer_load_dword %0, off, %1, %2 sc1\n\ts_waitcnt vmcnt(1) lgkmcnt(0)\n\ts_barrier\n\ts_barrier\n\ts_waitcnt vmcnt(0)" : "=v"(pollv) : "s"(srdS), "s"(fKV) : "memory");
  }
  ST(1); RT(1, 2);
  const unsigned sK = (unsigned)WS_KF + (unsigned)head * 524288u + w4 * 4096u, sV = sK + 16777216u;
  const unsigned dK = lds0 + L_K + w4 * 4096u, dV = lds0 + L_V + w4 * 4096u;
  f32x4 S[4][2], NM[2];
  bf16x8 P[2][2];
  float m[2] = {0.f, 0.f};
  NM[0] = f32x4{0.f, 0.f, 0.f, 0.f}; NM[1] = NM[0];
  bf16x8 ones = __builtin_bit_cast(bf16x8, u32x4{0x3f803f80u, 0x3f803f80u, 0x3f803f80u, 0x3f803f80u});
  asm volatile("" : "+v"(ones), "+v"(NM[0]), "+v"(NM[1]));
  const float ninf = -__builtin_inff();
  float thr = ninf;
  unsigned zf = 1u;
  unsigned vL2 = vL;
  asm volatile("" : "+v"(vL2));
  const unsigned lane2 = vL2 >> 4;
  const int lc = 4 * (int)(lane2 >> 4) - (int)(lane2 & 15u);
  if (grp == 1) {
    while (UNLIKELY(rfl(pollv) < 8u))
      asm volatile("s_sleep 1\n\tbuffer_load_dword %0, off, %1, %2 sc1\n\ts_waitcnt vmcnt(0)" : "=v"(pollv) : "s"(srdS), "s"((unsigned)WS_FLG + 2048u + (unsigned)head * 128u) : "memory");
    dma2<0>(srdS, vL, sV, dV); dma2<2048>(srdS, vL, sV, dV);
  }
  unsigned srcK = sK + (unsigned)(grp ? 2 : 18 - nB) * 16384u, srcV = sV + (unsigned)(grp ? 1 : 17 - nB) * 16384u;
  unsigned dstK = dK + (grp ? 0u : 49152u), dstV = dV + (grp ? 16384u : 32768u);
  const unsigned mK = grp ? 0x7fffu : (0xffffu & ~((1u << (nB - 1)) - 1u));
  const unsigned mV = grp ? 0xffffu : (0x1ffffu & ~((1u << nB) - 1u));
  const unsigned mS = grp ? ((15u << (nB - 2)) | 0x18000u) : (pi == 7 ? 0x10000u : 0u);
  unsigned vb = lds0 + L_V + vL, kb = lds0 + L_K + vL;
  typedef const __attribute__((address_space(4))) char* kargp;
  unsigned mG = grp == 0 && pi >= 4 ? (((2u << 8) - 1u) << 1) | (0x10000u << (1 + 8 + 3)) : 0u;
  asm volatile("" : "+s"(mG));
  const unsigned fgw = xcd < 7 ? (unsigned)xcd : 6u, n16 = ((unsigned)idx >> 3) * 4u + ((unsigned)idx & 3u), hi = 0u, wv = w4 + 4u * 0u;
  const char* pG = (const char*)((n16 & 1u) ? wu : wg) + ((n16 >> 1) * 229376u + hi * 114688u + fgw * 512u + wv * 64u);
  const char* pD = (const char*)wd + ((8u * fgw + (n16 >> 1)) * 32768u + (n16 & 1u) * 512u + hi * 256u + wv * 32u);
  const char* pO = (const char*)wo + (((unsigned)xcd * 16u + n16) * 32768u + w4 * 8192u + (2u * hi + 0u) * 2048u + 4096u);
  const unsigned sGo = (unsigned)WS_WGU + (((16u * fgw + 2u * wv) * 16u + (n16 >> 1) * 2u + hi) * 2u + (n16 & 1u)) * 1024u;
  const unsigned sDo = (unsigned)WS_WDF + ((8u * fgw + (n16 >> 1)) * 32u + 16u * (n16 & 1u) + 8u * hi + wv) * 1024u;
  unsigned tS;
  asm volatile(
    "s_waitcnt lgkmcnt(0)\n\t"
    "ds_read_b128 a[72:75], %[kb] offset:0\n\t"
    "ds_read_b128 a[76:79], %[kb] offset:1024\n\t"
    "ds_read_b128 a[80:83], %[kb] offset:2048\n\t"
    "ds_read_b128 a[84:87], %[kb] offset:3072\n\t"
    "ds_read_b128 a[88:91], %[kb] offset:4096\n\t"
    "ds_read_b128 a[92:95], %[kb] offset:5120\n\t"
    "s_waitcnt lgkmcnt(5)\n\t"
    "v_mfma_f32_16x16x32_bf16 %[s00], a[72:75], %[q00], %[n0]\n\t"
    "v_mfma_f32_16x16x32_bf16 %[s01], a[72:75], %[q01], %[n1]\n\t"
    "ds_read_b128 a[96:99], %[kb] offset:6144\n\t"
    "s_waitcnt lgkmcnt(5)\n\t"
    "v_mfma_f32_16x16x32_bf16 %[s00], a[76:79], %[q10], %[s00]\n\t"
    "v_mfma_f32_16x16x32_bf16 %[s01], a[76:79], %[q11], %[s01]\n\t"
    "ds_read_b128 a[100:103], %[kb] offset:7168\n\t"
    "s_waitcnt lgkmcnt(5)\n\t"
    "v_mfma_f32_16x16x32_bf16 %[s00], a[80:83], %[q20], %[s00]\n\t"
    "v_mfma_f32_16x16x32_bf16 %[s01], a[80:83], %[q21], %[s01]\n\t"
    "ds_read_b128 a[72:75], %[kb] offset:8192\n\t"
    "s_waitcnt lgkmcnt(5)\n\t"
    "v_mfma_f32_16x16x32_bf16 %[s00], a[84:87], %[q30], %[s00]\n\t"
    "v_mfma_f32_16x16x32_bf16 %[s01], a[84:87], %[q31], %[s01]\n\t"
    "ds_read_b128 a[76:79], %[kb] offset:9216\n\t"
    "s_waitcnt lgkmcnt(5)\n\t"
    "v_mfma_f32_16x16x32_bf16 %[s10], a[88:91], %[q00], %[n0]\n\t"
    "v_mfma_f32_16x16x32_bf16 %[s11], a[88:91], %[q01], %[n1]\n\t"
    "ds_read_b128 a[80:83], %[kb] offset:10240\n\t"
    "s_waitcnt lgkmcnt(5)\n\t"
    "v_mfma_f32_16x16x32_bf16 %[s10], a[92:95], %[q10], %[s10]\n\t"
    "v_mfma_f32_16x16x32_bf16 %[s11], a[92:95], %[q11], %[s11]\n\t"
    "ds_read_b128 a[84:87], %[kb] offset:11264\n\t"
    "s_waitcnt lgkmcnt(5)\n\t"
    "v_mfma_f32_16x16x32_bf16 %[s10], a[96:99], %[q20], %[s10]\n\t"
    "v_mfma_f32_16x16x32_bf16 %[s11], a[96:99], %[q21], %[s11]\n\t"
    "ds_read_b128 a[88:91], %[kb] offset:12288\n\t"
    "s_waitcnt lgkmcnt(5)\n\t"
    "v_mfma_f32_16x16x32_bf16 %[s10], a[100:103], %[q30], %[s10]\n\t"
    "v_mfma_f32_16x16x32_bf16 %[s11], a[100:103], %[q31], %[s11]\n\t"
    "ds_read_b128 a[92:95], %[kb] offset:13312\n\t"
    "s_waitcnt lgkmcnt(5)\n\t"
    "v_mfma_f32_16x16x32_bf16 %[s20], a[72:75], %[q00], %[n0]\n\t"
    "v_mfma_f32_16x16x32_bf16 %[s21], a[72:75], %[q01], %[n1]\n\t"
    "ds_read_b128 a[96:99], %[kb] offset:14336\n\t"
    "s_waitcnt lgkmcnt(5)\n\t"
    "v_mfma_f32_16x16x32_bf16 %[s20], a[76:79], %[q10], %[s20]\n\t"
    "v_mfma_f32_16x16x32_bf16 %[s21], a[76:79], %[q11], %[s21]\n\t"
    "ds_read_b128 a[100:103], %[kb] offset:15360\n\t"
    "s_waitcnt lgkmcnt(5)\n\t"
    "v_mfma_f32_16x16x32_bf16 %[s20], a[80:83], %[q20], %[s20]\n\t"
    "v_mfma_f32_16x16x32_bf16 %[s21], a[80:83], %[q21], %[s21]\n\t"
    "s_waitcnt lgkmcnt(4)\n\t"
    "v_mfma_f32_16x16x32_bf16 %[s20], a[84:87], %[q30], %[s20]\n\t"
    "v_mfma_f32_16x16x32_bf16 %[s21], a[84:87], %[q31], %[s21]\n\t"
    "s_waitcnt lgkmcnt(3)\n\t"
    "v_mfma_f32_16x16x32_bf16 %[s30], a[88:91], %[q00], %[n0]\n\t"
    "v_mfma_f32_16x16x32_bf16 %[s31], a[88:91], %[q01], %[n1]\n\t"
    "s_waitcnt lgkmcnt(2)\n\t"
    "v_mfma_f32_16x16x32_bf16 %[s30], a[92:95], %[q10], %[s30]\n\t"
    "v_mfma_f32_16x16x32_bf16 %[s31], a[92:95], %[q11], %[s31]\n\t"
    "s_waitcnt lgkmcnt(1)\n\t"
    "s_waitcnt vmcnt(0) lgkmcnt(0)\n\t"
    "s_barrier\n\t"
    "v_mfma_f32_16x16x32_bf16 %[s30], a[96:99], %[q20], %[s30]\n\t"
    "v_mfma_f32_16x16x32_bf16 %[s31], a[96:99], %[q21], %[s31]\n\t"
    "s_waitcnt lgkmcnt(0)\n\t"
    "v_mfma_f32_16x16x32_bf16 %[s30], a[100:103], %[q30], %[s30]\n\t"
    "v_mfma_f32_16x16x32_bf16 %[s31], a[100:103], %[q31], %[s31]\n\t"
    "s_nop 7\n\t"
    : [s00] "=&v"(S[0][0]), [s01] "=&v"(S[0][1]), [s10] "=&v"(S[1][0]), [s11] "=&v"(S[1][1]), [s20] "=&v"(S[2][0]), [s21] "=&v"(S[2][1]), [s30] "=&v"(S[3][0]), [s31] "=&v"(S[3][1])
    : [q00] "v"(Q[0][0]), [q01] "v"(Q[0][1]), [q10] "v"(Q[1][0]), [q11] "v"(Q[1][1]), [q20] "v"(Q[2][0]), [q21] "v"(Q[2][1]), [q30] "v"(Q[3][0]), [q31] "v"(Q[3][1]), [n0] "v"(NM[0]), [n1] "v"(NM[1]), [kb] "v"(kb)
    : "memory", "scc", ACLOB);

  TM_DECL;
  for (int j = 0;; ++j) {
    SBAR();
    TM(0);
    const bool doK = (mK >> j) & 1u, doV = (mV >> j) & 1u;
    if (LIKELY(doK)) dma2<0>(srdS, vL, srcK, dstK);
    if (UNLIKELY((mG >> j) & 0x10000u)) count_up(srdS, F_WT + (unsigned)xcd, wid == 0u);
    if (UNLIKELY((mS >> j) & 1u)) {
      const bool own = grp == 1 && j >= nB;
      if (own && j == nB) {
        const unsigned so = (unsigned)WS_CT + ((unsigned)(qbB * 8) + w4 * 2u) * 131072u + (unsigned)head * 4096u;
      { float l0; asm volatile("v_accvgpr_read_b32 %0, a64" : "=v"(l0)); const float il = 1.f / l0;
        { f32x4 t0, t1; asm volatile("v_accvgpr_read_b32 %0, a0\n\tv_accvgpr_read_b32 %1, a1\n\tv_accvgpr_read_b32 %2, a2\n\tv_accvgpr_read_b32 %3, a3" : "=v"(t0[0]), "=v"(t0[1]), "=v"(t0[2]), "=v"(t0[3])); asm volatile("v_accvgpr_read_b32 %0, a8\n\tv_accvgpr_read_b32 %1, a9\n\tv_accvgpr_read_b32 %2, a10\n\tv_accvgpr_read_b32 %3, a11" : "=v"(t1[0]), "=v"(t1[1]), "=v"(t1[2]), "=v"(t1[3])); sstore16(pack2(t0 * il, t1 * il), srdS, vL, so + 0u); }
        { f32x4 t0, t1; asm volatile("v_accvgpr_read_b32 %0, a16\n\tv_accvgpr_read_b32 %1, a17\n\tv_accvgpr_read_b32 %2, a18\n\tv_accvgpr_read_b32 %3, a19" : "=v"(t0[0]), "=v"(t0[1]), "=v"(t0[2]), "=v"(t0[3])); asm volatile("v_accvgpr_read_b32 %0, a24\n\tv_accvgpr_read_b32 %1, a25\n\tv_accvgpr_read_b32 %2, a26\n\tv_accvgpr_read_b32 %3, a27" : "=v"(t1[0]), "=v"(t1[1]), "=v"(t1[2]), "=v"(t1[3])); sstore16(pack2(t0 * il, t1 * il), srdS, vL, so + 1024u); }
        { f32x4 t0, t1; asm volatile("v_accvgpr_read_b32 %0, a32\n\tv_accvgpr_read_b32 %1, a33\n\tv_accvgpr_read_b32 %2, a34\n\tv_accvgpr_read_b32 %3, a35" : "=v"(t0[0]), "=v"(t0[1]), "=v"(t0[2]), "=v"(t0[3])); asm volatile("v_accvgpr_read_b32 %0, a40\n\tv_accvgpr_read_b32 %1, a41\n\tv_accvgpr_read_b32 %2, a42\n\tv_accvgpr_read_b32 %3, a43" : "=v"(t1[0]), "=v"(t1[1]), "=v"(t1[2]), "=v"(t1[3])); sstore16(pack2(t0 * il, t1 * il), srdS, vL, so + 2048u); }
        { f32x4 t0, t1; asm volatile("v_accvgpr_read_b32 %0, a48\n\tv_accvgpr_read_b32 %1, a49\n\tv_accvgpr_read_b32 %2, a50\n\tv_accvgpr_read_b32 %3, a51" : "=v"(t0[0]), "=v"(t0[1]), "=v"(t0[2]), "=v"(t0[3])); asm volatile("v_accvgpr_read_b32 %0, a56\n\tv_accvgpr_read_b32 %1, a57\n\tv_accvgpr_read_b32 %2, a58\n\tv_accvgpr_read_b32 %3, a59" : "=v"(t1[0]), "=v"(t1[1]), "=v"(t1[2]), "=v"(t1[3])); sstore16(pack2(t0 * il, t1 * il), srdS, vL, so + 3072u); }
      }
      { float l0; asm volatile("v_accvgpr_read_b32 %0, a68" : "=v"(l0)); const float il = 1.f / l0;
        { f32x4 t0, t1; asm volatile("v_accvgpr_read_b32 %0, a4\n\tv_accvgpr_read_b32 %1, a5\n\tv_accvgpr_read_b32 %2, a6\n\tv_accvgpr_read_b32 %3, a7" : "=v"(t0[0]), "=v"(t0[1]), "=v"(t0[2]), "=v"(t0[3])); asm volatile("v_accvgpr_read_b32 %0, a12\n\tv_accvgpr_read_b32 %1, a13\n\tv_accvgpr_read_b32 %2, a14\n\tv_accvgpr_read_b32 %3, a15" : "=v"(t1[0]), "=v"(t1[1]), "=v"(t1[2]), "=v"(t1[3])); sstore16(pack2(t0 * il, t1 * il), srdS, vL, so + 131072u); }
        { f32x4 t0, t1; asm volatile("v_accvgpr_read_b32 %0, a20\n\tv_accvgpr_read_b32 %1, a21\n\tv_accvgpr_read_b32 %2, a22\n\tv_accvgpr_read_b32 %3, a23" : "=v"(t0[0]), "=v"(t0[1]), "=v"(t0[2]), "=v"(t0[3])); asm volatile("v_accvgpr_read_b32 %0, a28\n\tv_accvgpr_read_b32 %1, a29\n\tv_accvgpr_read_b32 %2, a30\n\tv_accvgpr_read_b32 %3, a31" : "=v"(t1[0]), "=v"(t1[1]), "=v"(t1[2]), "=v"(t1[3])); sstore16(pack2(t0 * il, t1 * il), srdS, vL, so + 132096u); }
        { f32x4 t0, t1; asm volatile("v_accvgpr_read_b32 %0, a36\n\tv_accvgpr_read_b32 %1, a37\n\tv_accvgpr_read_b32 %2, a38\n\tv_accvgpr_read_b32 %3, a39" : "=v"(t0[0]), "=v"(t0[1]), "=v"(t0[2]), "=v"(t0[3])); asm volatile("v_accvgpr_read_b32 %0, a44\n\tv_accvgpr_read_b32 %1, a45\n\tv_accvgpr_read_b32 %2, a46\n\tv_accvgpr_read_b32 %3, a47" : "=v"(t1[0]), "=v"(t1[1]), "=v"(t1[2]), "=v"(t1[3])); sstore16(pack2(t0 * il, t1 * il), srdS, vL, so + 133120u); }
        { f32x4 t0, t1; asm volatile("v_accvgpr_read_b32 %0, a52\n\tv_accvgpr_read_b32 %1, a53\n\tv_accvgpr_read_b32 %2, a54\n\tv_accvgpr_read_b32 %3, a55" : "=v"(t0[0]), "=v"(t0[1]), "=v"(t0[2]), "=v"(t0[3])); asm volatile("v_accvgpr_read_b32 %0, a60\n\tv_accvgpr_read_b32 %1, a61\n\tv_accvgpr_read_b32 %2, a62\n\tv_accvgpr_read_b32 %3, a63" : "=v"(t1[0]), "=v"(t1[1]), "=v"(t1[2]), "=v"(t1[3])); sstore16(pack2(t0 * il, t1 * il), srdS, vL, so + 134144u); }
      }
        zf = 1u;
      }
      if (own && j == nB + 1) count_up(srdS, F_CB + (unsigned)(xcd * 8 + pi), wid == 4u);
      const int d0 = ((grp == 1 && !own) ? qbB : qbA) * 128 + 32 * (int)w4 - 64 * (own ? 17 + j - nB : j);
      if (d0 < 63) {
        asm volatile(
          "v_cmp_lt_i32 vcc, 16, %32\n\t"
          "v_cndmask_b32 %4, %4, %33, vcc\n\t"
          "v_cmp_lt_i32 vcc, 15, %32\n\t"
          "v_cndmask_b32 %5, %5, %33, vcc\n\t"
          "v_cmp_lt_i32 vcc, 14, %32\n\t"
          "v_cndmask_b32 %6, %6, %33, vcc\n\t"
          "v_cmp_lt_i32 vcc, 13, %32\n\t"
          "v_cndmask_b32 %7, %7, %33, vcc\n\t"
          "v_cmp_lt_i32 vcc, 0, %32\n\t"
          "v_cndmask_b32 %0, %0, %33, vcc\n\t"
          "v_cndmask_b32 %12, %12, %33, vcc\n\t"
          "v_cmp_lt_i32 vcc, -1, %32\n\t"
          "v_cndmask_b32 %1, %1, %33, vcc\n\t"
          "v_cndmask_b32 %13, %13, %33, vcc\n\t"
          "v_cmp_lt_i32 vcc, -2, %32\n\t"
          "v_cndmask_b32 %2, %2, %33, vcc\n\t"
          "v_cndmask_b32 %14, %14, %33, vcc\n\t"
          "v_cmp_lt_i32 vcc, -3, %32\n\t"
          "v_cndmask_b32 %3, %3, %33, vcc\n\t"
          "v_cndmask_b32 %15, %15, %33, vcc\n\t"
          "v_cmp_lt_i32 vcc, -16, %32\n\t"
          "v_cndmask_b32 %8, %8, %33, vcc\n\t"
          "v_cndmask_b32 %20, %20, %33, vcc\n\t"
          "v_cmp_lt_i32 vcc, -17, %32\n\t"
          "v_cndmask_b32 %9, %9, %33, vcc\n\t"
          "v_cndmask_b32 %21, %21, %33, vcc\n\t"
          "v_cmp_lt_i32 vcc, -18, %32\n\t"
          "v_cndmask_b32 %10, %10, %33, vcc\n\t"
          "v_cndmask_b32 %22, %22, %33, vcc\n\t"
          "v_cmp_lt_i32 vcc, -19, %32\n\t"
          "v_cndmask_b32 %11, %11, %33, vcc\n\t"
          "v_cndmask_b32 %23, %23, %33, vcc\n\t"
          "v_cmp_lt_i32 vcc, -32, %32\n\t"
          "v_cndmask_b32 %16, %16, %33, vcc\n\t"
          "v_cndmask_b32 %28, %28, %33, vcc\n\t"
          "v_cmp_lt_i32 vcc, -33, %32\n\t"
          "v_cndmask_b32 %17, %17, %33, vcc\n\t"
          "v_cndmask_b32 %29, %29, %33, vcc\n\t"
          "v_cmp_lt_i32 vcc, -34, %32\n\t"
          "v_cndmask_b32 %18, %18, %33, vcc\n\t"
          "v_cndmask_b32 %30, %30, %33, vcc\n\t"
          "v_cmp_lt_i32 vcc, -35, %32\n\t"
          "v_cndmask_b32 %19, %19, %33, vcc\n\t"
          "v_cndmask_b32 %31, %31, %33, vcc\n\t"
          "v_cmp_lt_i32 vcc, -48, %32\n\t"
          "v_cndmask_b32 %24, %24, %33, vcc\n\t"
          "v_cmp_lt_i32 vcc, -49, %32\n\t"
          "v_cndmask_b32 %25, %25, %33, vcc\n\t"
          "v_cmp_lt_i32 vcc, -50, %32\n\t"
          "v_cndmask_b32 %26, %26, %33, vcc\n\t"
          "v_cmp_lt_i32 vcc, -51, %32\n\t"
          "v_cndmask_b32 %27, %27, %33, vcc\n\t"
          : "+v"(S[0][0][0]), "+v"(S[0][0][1]), "+v"(S[0][0][2]), "+v"(S[0][0][3]), "+v"(S[0][1][0]), "+v"(S[0][1][1]), "+v"(S[0][1][2]), "+v"(S[0][1][3]), "+v"(S[1][0][0]), "+v"(S[1][0][1]), "+v"(S[1][0][2]), "+v"(S[1][0][3]), "+v"(S[1][1][0]), "+v"(S[1][1][1]), "+v"(S[1][1][2]), "+v"(S[1][1][3]), "+v"(S[2][0][0]), "+v"(S[2][0][1]), "+v"(S[2][0][2]), "+v"(S[2][0][3]), "+v"(S[2][1][0]), "+v"(S[2][1][1]), "+v"(S[2][1][2]), "+v"(S[2][1][3]), "+v"(S[3][0][0]), "+v"(S[3][0][1]), "+v"(S[3][0][2]), "+v"(S[3][0][3]), "+v"(S[3][1][0]), "+v"(S[3][1][1]), "+v"(S[3][1][2]), "+v"(S[3][1][3]) : "v"(lc - d0), "v"(ninf) : "vcc");
      }
    }
    {
      float mq[2];
      asm(
        "v_max3_f32 %0, %2, %3, %4\n\t"
        "v_max3_f32 %1, %18, %19, %20\n\t"
        "v_max3_f32 %0, %0, %5, %6\n\t"
        "v_max3_f32 %1, %1, %21, %22\n\t"
        "v_max3_f32 %0, %0, %7, %8\n\t"
        "v_max3_f32 %1, %1, %23, %24\n\t"
        "v_max3_f32 %0, %0, %9, %10\n\t"
        "v_max3_f32 %1, %1, %25, %26\n\t"
        "v_max3_f32 %0, %0, %11, %12\n\t"
        "v_max3_f32 %1, %1, %27, %28\n\t"
        "v_max3_f32 %0, %0, %13, %14\n\t"
        "v_max3_f32 %1, %1, %29, %30\n\t"
        "v_max3_f32 %0, %0, %15, %16\n\t"
        "v_max3_f32 %1, %1, %31, %32\n\t"
        "v_max_f32 %0, %0, %17\n\t"
        "v_max_f32 %1, %1, %33\n\t"
        : "=&v"(mq[0]), "=&v"(mq[1]) : "v"(S[0][0][0]), "v"(S[0][0][1]), "v"(S[0][0][2]), "v"(S[0][0][3]), "v"(S[1][0][0]), "v"(S[1][0][1]), "v"(S[1][0][2]), "v"(S[1][0][3]), "v"(S[2][0][0]), "v"(S[2][0][1]), "v"(S[2][0][2]), "v"(S[2][0][3]), "v"(S[3][0][0]), "v"(S[3][0][1]), "v"(S[3][0][2]), "v"(S[3][0][3]), "v"(S[0][1][0]), "v"(S[0][1][1]), "v"(S[0][1][2]), "v"(S[0][1][3]), "v"(S[1][1][0]), "v"(S[1][1][1]), "v"(S[1][1][2]), "v"(S[1][1][3]), "v"(S[2][1][0]), "v"(S[2][1][1]), "v"(S[2][1][2]), "v"(S[2][1][3]), "v"(S[3][1][0]), "v"(S[3][1][1]), "v"(S[3][1][2]), "v"(S[3][1][3]));
      if (UNLIKELY(__builtin_amdgcn_ballot_w64(mq[0] > thr || mq[1] > thr) != 0ull)) {
        const bool first = thr < 0.f;
        float al[2];
#pragma unroll
        for (int qt = 0; qt < 2; ++qt) {
          float a = mq[qt];
          a = __builtin_fmaxf(a, __int_as_float(__builtin_amdgcn_ds_bpermute((int)((vL2 >> 2) ^ 64u), __float_as_int(a))));
          a = __builtin_fmaxf(a, __int_as_float(__builtin_amdgcn_ds_bpermute((int)((vL2 >> 2) ^ 128u), __float_as_int(a))));
          const float dl = first ? a : __builtin_fmaxf(a, 0.f);
          al[qt] = __builtin_amdgcn_exp2f(-dl);
          m[qt] = first ? dl : m[qt] + dl;
#pragma unroll
          for (int kt = 0; kt < 4; ++kt) S[kt][qt] -= dl;
          NM[qt] = f32x4{-m[qt], -m[qt], -m[qt], -m[qt]};
          asm volatile("" : "+v"(NM[qt]));
        }
        if (!first) {
          { float t0, t1; asm volatile(
            "v_accvgpr_read_b32 %0, a0\n\t"
            "v_accvgpr_read_b32 %1, a1\n\t"
            "v_mul_f32 %0, %2, %0\n\t"
            "v_mul_f32 %1, %2, %1\n\t"
            "v_accvgpr_write_b32 a0, %0\n\t"
            "v_accvgpr_write_b32 a1, %1\n\t"
            "v_accvgpr_read_b32 %0, a2\n\t"
            "v_accvgpr_read_b32 %1, a3\n\t"
            "v_mul_f32 %0, %2, %0\n\t"
            "v_mul_f32 %1, %2, %1\n\t"
            "v_accvgpr_write_b32 a2, %0\n\t"
            "v_accvgpr_write_b32 a3, %1\n\t"
            "v_accvgpr_read_b32 %0, a4\n\t"
            "v_accvgpr_read_b32 %1, a5\n\t"
            "v_mul_f32 %0, %3, %0\n\t"
            "v_mul_f32 %1, %3, %1\n\t"
            "v_accvgpr_write_b32 a4, %0\n\t"
            "v_accvgpr_write_b32 a5, %1\n\t"
            "v_accvgpr_read_b32 %0, a6\n\t"
            "v_accvgpr_read_b32 %1, a7\n\t"
            "v_mul_f32 %0, %3, %0\n\t"
            "v_mul_f32 %1, %3, %1\n\t"
            "v_accvgpr_write_b32 a6, %0\n\t"
            "v_accvgpr_write_b32 a7, %1\n\t"
            "v_accvgpr_read_b32 %0, a8\n\t"
            "v_accvgpr_read_b32 %1, a9\n\t"
            "v_mul_f32 %0, %2, %0\n\t"
            "v_mul_f32 %1, %2, %1\n\t"
            "v_accvgpr_write_b32 a8, %0\n\t"
            "v_accvgpr_write_b32 a9, %1\n\t"
            "v_accvgpr_read_b32 %0, a10\n\t"
            "v_accvgpr_read_b32 %1, a11\n\t"
            "v_mul_f32 %0, %2, %0\n\t"
            "v_mul_f32 %1, %2, %1\n\t"
            "v_accvgpr_write_b32 a10, %0\n\t"
            "v_accvgpr_write_b32 a11, %1\n\t"
            "v_accvgpr_read_b32 %0, a12\n\t"
            "v_accvgpr_read_b32 %1, a13\n\t"
            "v_mul_f32 %0, %3, %0\n\t"
            "v_mul_f32 %1, %3, %1\n\t"
            "v_accvgpr_write_b32 a12, %0\n\t"
            "v_accvgpr_write_b32 a13, %1\n\t"
            "v_accvgpr_read_b32 %0, a14\n\t"
            "v_accvgpr_read_b32 %1, a15\n\t"
            "v_mul_f32 %0, %3, %0\n\t"
            "v_mul_f32 %1, %3, %1\n\t"
            "v_accvgpr_write_b32 a14, %0\n\t"
            "v_accvgpr_write_b32 a15, %1\n\t"
            "v_accvgpr_read_b32 %0, a16\n\t"
            "v_accvgpr_read_b32 %1, a17\n\t"
            "v_mul_f32 %0, %2, %0\n\t"
            "v_mul_f32 %1, %2, %1\n\t"
            "v_accvgpr_write_b32 a16, %0\n\t"
            "v_accvgpr_write_b32 a17, %1\n\t"
            "v_accvgpr_read_b32 %0, a18\n\t"
            "v_accvgpr_read_b32 %1, a19\n\t"
            "v_mul_f32 %0, %2, %0\n\t"
            "v_mul_f32 %1, %2, %1\n\t"
            "v_accvgpr_write_b32 a18, %0\n\t"
            "v_accvgpr_write_b32 a19, %1\n\t"
            "v_accvgpr_read_b32 %0, a20\n\t"
            "v_accvgpr_read_b32 %1, a21\n\t"
            "v_mul_f32 %0, %3, %0\n\t"
            "v_mul_f32 %1, %3, %1\n\t"
            "v_accvgpr_write_b32 a20, %0\n\t"
            "v_accvgpr_write_b32 a21, %1\n\t"
            "v_accvgpr_read_b32 %0, a22\n\t"
            "v_accvgpr_read_b32 %1, a23\n\t"
            "v_mul_f32 %0, %3, %0\n\t"
            "v_mul_f32 %1, %3, %1\n\t"
            "v_accvgpr_write_b32 a22, %0\n\t"
            "v_accvgpr_write_b32 a23, %1\n\t"
            "v_accvgpr_read_b32 %0, a24\n\t"
            "v_accvgpr_read_b32 %1, a25\n\t"
            "v_mul_f32 %0, %2, %0\n\t"
            "v_mul_f32 %1, %2, %1\n\t"
            "v_accvgpr_write_b32 a24, %0\n\t"
            "v_accvgpr_write_b32 a25, %1\n\t"
            "v_accvgpr_read_b32 %0, a26\n\t"
            "v_accvgpr_read_b32 %1, a27\n\t"
            "v_mul_f32 %0, %2, %0\n\t"
            "v_mul_f32 %1, %2, %1\n\t"
            "v_accvgpr_write_b32 a26, %0\n\t"
            "v_accvgpr_write_b32 a27, %1\n\t"
            "v_accvgpr_read_b32 %0, a28\n\t"
            "v_accvgpr_read_b32 %1, a29\n\t"
            "v_mul_f32 %0, %3, %0\n\t"
            "v_mul_f32 %1, %3, %1\n\t"
            "v_accvgpr_write_b32 a28, %0\n\t"
            "v_accvgpr_write_b32 a29, %1\n\t"
            "v_accvgpr_read_b32 %0, a30\n\t"
            "v_accvgpr_read_b32 %1, a31\n\t"
            "v_mul_f32 %0, %3, %0\n\t"
            "v_mul_f32 %1, %3, %1\n\t"
            "v_accvgpr_write_b32 a30, %0\n\t"
            "v_accvgpr_write_b32 a31, %1\n\t"
            "v_accvgpr_read_b32 %0, a32\n\t"
            "v_accvgpr_read_b32 %1, a33\n\t"
            "v_mul_f32 %0, %2, %0\n\t"
            "v_mul_f32 %1, %2, %1\n\t"
            "v_accvgpr_write_b32 a32, %0\n\t"
            "v_accvgpr_write_b32 a33, %1\n\t"
            "v_accvgpr_read_b32 %0, a34\n\t"
            "v_accvgpr_read_b32 %1, a35\n\t"
            "v_mul_f32 %0, %2, %0\n\t"
            "v_mul_f32 %1, %2, %1\n\t"
            "v_accvgpr_write_b32 a34, %0\n\t"
            "v_accvgpr_write_b32 a35, %1\n\t"
            "v_accvgpr_read_b32 %0, a36\n\t"
            "v_accvgpr_read_b32 %1, a37\n\t"
            "v_mul_f32 %0, %3, %0\n\t"
            "v_mul_f32 %1, %3, %1\n\t"
            "v_accvgpr_write_b32 a36, %0\n\t"
            "v_accvgpr_write_b32 a37, %1\n\t"
            "v_accvgpr_read_b32 %0, a38\n\t"
            "v_accvgpr_read_b32 %1, a39\n\t"
            "v_mul_f32 %0, %3, %0\n\t"
            "v_mul_f32 %1, %3, %1\n\t"
            "v_accvgpr_write_b32 a38, %0\n\t"
            "v_accvgpr_write_b32 a39, %1\n\t"
            "v_accvgpr_read_b32 %0, a40\n\t"
            "v_accvgpr_read_b32 %1, a41\n\t"
            "v_mul_f32 %0, %2, %0\n\t"
            "v_mul_f32 %1, %2, %1\n\t"
            "v_accvgpr_write_b32 a40, %0\n\t"
            "v_accvgpr_write_b32 a41, %1\n\t"
            "v_accvgpr_read_b32 %0, a42\n\t"
            "v_accvgpr_read_b32 %1, a43\n\t"
            "v_mul_f32 %0, %2, %0\n\t"
            "v_mul_f32 %1, %2, %1\n\t"
            "v_accvgpr_write_b32 a42, %0\n\t"
            "v_accvgpr_write_b32 a43, %1\n\t"
            "v_accvgpr_read_b32 %0, a44\n\t"
            "v_accvgpr_read_b32 %1, a45\n\t"
            "v_mul_f32 %0, %3, %0\n\t"
            "v_mul_f32 %1, %3, %1\n\t"
            "v_accvgpr_write_b32 a44, %0\n\t"
            "v_accvgpr_write_b32 a45, %1\n\t"
            "v_accvgpr_read_b32 %0, a46\n\t"
            "v_accvgpr_read_b32 %1, a47\n\t"
            "v_mul_f32 %0, %3, %0\n\t"
            "v_mul_f32 %1, %3, %1\n\t"
            "v_accvgpr_write_b32 a46, %0\n\t"
            "v_accvgpr_write_b32 a47, %1\n\t"
            "v_accvgpr_read_b32 %0, a48\n\t"
            "v_accvgpr_read_b32 %1, a49\n\t"
            "v_mul_f32 %0, %2, %0\n\t"
            "v_mul_f32 %1, %2, %1\n\t"
            "v_accvgpr_write_b32 a48, %0\n\t"
            "v_accvgpr_write_b32 a49, %1\n\t"
            "v_accvgpr_read_b32 %0, a50\n\t"
            "v_accvgpr_read_b32 %1, a51\n\t"
            "v_mul_f32 %0, %2, %0\n\t"
            "v_mul_f32 %1, %2, %1\n\t"
            "v_accvgpr_write_b32 a50, %0\n\t"
            "v_accvgpr_write_b32 a51, %1\n\t"
            "v_accvgpr_read_b32 %0, a52\n\t"
            "v_accvgpr_read_b32 %1, a53\n\t"
            "v_mul_f32 %0, %3, %0\n\t"
            "v_mul_f32 %1, %3, %1\n\t"
            "v_accvgpr_write_b32 a52, %0\n\t"
            "v_accvgpr_write_b32 a53, %1\n\t"
            "v_accvgpr_read_b32 %0, a54\n\t"
            "v_accvgpr_read_b32 %1, a55\n\t"
            "v_mul_f32 %0, %3, %0\n\t"
            "v_mul_f32 %1, %3, %1\n\t"
            "v_accvgpr_write_b32 a54, %0\n\t"
            "v_accvgpr_write_b32 a55, %1\n\t"
            "v_accvgpr_read_b32 %0, a56\n\t"
            "v_accvgpr_read_b32 %1, a57\n\t"
            "v_mul_f32 %0, %2, %0\n\t"
            "v_mul_f32 %1, %2, %1\n\t"
            "v_accvgpr_write_b32 a56, %0\n\t"
            "v_accvgpr_write_b32 a57, %1\n\t"
            "v_accvgpr_read_b32 %0, a58\n\t"
            "v_accvgpr_read_b32 %1, a59\n\t"
            "v_mul_f32 %0, %2, %0\n\t"
            "v_mul_f32 %1, %2, %1\n\t"
            "v_accvgpr_write_b32 a58, %0\n\t"
            "v_accvgpr_write_b32 a59, %1\n\t"
            "v_accvgpr_read_b32 %0, a60\n\t"
            "v_accvgpr_read_b32 %1, a61\n\t"
            "v_mul_f32 %0, %3, %0\n\t"
            "v_mul_f32 %1, %3, %1\n\t"
            "v_accvgpr_write_b32 a60, %0\n\t"
            "v_accvgpr_write_b32 a61, %1\n\t"
            "v_accvgpr_read_b32 %0, a62\n\t"
            "v_accvgpr_read_b32 %1, a63\n\t"
            "v_mul_f32 %0, %3, %0\n\t"
            "v_mul_f32 %1, %3, %1\n\t"
            "v_accvgpr_write_b32 a62, %0\n\t"
            "v_accvgpr_write_b32 a63, %1\n\t"
            "v_accvgpr_read_b32 %0, a64\n\t"
            "v_accvgpr_read_b32 %1, a65\n\t"
            "v_mul_f32 %0, %2, %0\n\t"
            "v_mul_f32 %1, %2, %1\n\t"
            "v_accvgpr_write_b32 a64, %0\n\t"
            "v_accvgpr_write_b32 a65, %1\n\t"
            "v_accvgpr_read_b32 %0, a66\n\t"
            "v_accvgpr_read_b32 %1, a67\n\t"
            "v_mul_f32 %0, %2, %0\n\t"
            "v_mul_f32 %1, %2, %1\n\t"
            "v_accvgpr_write_b32 a66, %0\n\t"
            "v_accvgpr_write_b32 a67, %1\n\t"
            "v_accvgpr_read_b32 %0, a68\n\t"
            "v_accvgpr_read_b32 %1, a69\n\t"
            "v_mul_f32 %0, %3, %0\n\t"
            "v_mul_f32 %1, %3, %1\n\t"
            "v_accvgpr_write_b32 a68, %0\n\t"
            "v_accvgpr_write_b32 a69, %1\n\t"
            "v_accvgpr_read_b32 %0, a70\n\t"
            "v_accvgpr_read_b32 %1, a71\n\t"
            "v_mul_f32 %0, %3, %0\n\t"
            "v_mul_f32 %1, %3, %1\n\t"
            "v_accvgpr_write_b32 a70, %0\n\t"
            "v_accvgpr_write_b32 a71, %1\n\t"
            : "=&v"(t0), "=&v"(t1) : "v"(al[0]), "v"(al[1]) : ACLOB); }
        }
        thr = 16.f;
      }
    }
    SBAR();
    if (LIKELY(doK)) dma2<2048>(srdS, vL, srcK, dstK);
    P[0][0] = exp_pack(S[0][0], S[1][0]); P[0][1] = exp_pack(S[0][1], S[1][1]);
    asm volatile("" : "+v"(P[0][0]), "+v"(P[0][1]));
    SBAR();
    if (LIKELY(doV)) dma2<0>(srdS, vL, srcV, dstV);
    P[1][0] = exp_pack(S[2][0], S[3][0]);
    asm volatile("" : "+v"(P[1][0]));
    SBAR();
    if (LIKELY(doV)) dma2<2048>(srdS, vL, srcV, dstV);
    P[1][1] = exp_pack(S[2][1], S[3][1]);
    srcK += 16384u; srcV += 16384u; dstK ^= 16384u; dstV ^= 16384u;
    kb ^= 16384u;
    if (UNLIKELY(grp == 1 && j == nB - 1)) {
#pragma unroll
      for (int ds = 0; ds < 4; ++ds)
#pragma unroll
        for (int qt = 0; qt < 2; ++qt) Q[ds][qt] = lds_b128(lds0 + L_Q + (w4 * 8u + ds * 2u + qt) * 1024u + vL);
      NM[0] = f32x4{0.f, 0.f, 0.f, 0.f}; NM[1] = NM[0];
      asm volatile("" : "+v"(NM[0]), "+v"(NM[1]));
      thr = ninf;
      kb += 32768u;
    }
    SBAR();
    TM(1);
    BAR();
    TM(2);
    if (j == 16) break;
  asm volatile(
    "s_waitcnt lgkmcnt(0)\n\t"
    "ds_read_b128 a[72:75], %[vb] offset:0\n\t"
    "ds_read_b128 a[76:79], %[vb] offset:1024\n\t"
    "ds_read_b128 a[80:83], %[vb] offset:2048\n\t"
    "ds_read_b128 a[84:87], %[vb] offset:3072\n\t"
    "ds_read_b128 a[88:91], %[vb] offset:4096\n\t"
    "ds_read_b128 a[92:95], %[vb] offset:5120\n\t"
    "s_cmp_lg_u32 %[zf], 0\n\t"
    "s_cbranch_scc1 .Lz%=\n\t"
    "v_mfma_f32_16x16x32_bf16 a[64:67], %[one], %[p00], a[64:67]\n\t"
    "v_mfma_f32_16x16x32_bf16 a[68:71], %[one], %[p01], a[68:71]\n\t"
    "v_mfma_f32_16x16x32_bf16 a[64:67], %[one], %[p10], a[64:67]\n\t"
    "v_mfma_f32_16x16x32_bf16 a[68:71], %[one], %[p11], a[68:71]\n\t"
    "s_waitcnt lgkmcnt(5)\n\t"
    "v_mfma_f32_16x16x32_bf16 a[0:3], a[72:75], %[p00], a[0:3]\n\t"
    "v_mfma_f32_16x16x32_bf16 a[4:7], a[72:75], %[p01], a[4:7]\n\t"
    "ds_read_b128 a[96:99], %[vb] offset:6144\n\t"
    "s_waitcnt lgkmcnt(5)\n\t"
    "v_mfma_f32_16x16x32_bf16 a[8:11], a[76:79], %[p00], a[8:11]\n\t"
    "v_mfma_f32_16x16x32_bf16 a[12:15], a[76:79], %[p01], a[12:15]\n\t"
    "ds_read_b128 a[100:103], %[vb] offset:7168\n\t"
    "s_waitcnt lgkmcnt(5)\n\t"
    "v_mfma_f32_16x16x32_bf16 a[16:19], a[80:83], %[p00], a[16:19]\n\t"
    "v_mfma_f32_16x16x32_bf16 a[20:23], a[80:83], %[p01], a[20:23]\n\t"
    "ds_read_b128 a[72:75], %[vb] offset:8192\n\t"
    "s_waitcnt lgkmcnt(5)\n\t"
    "v_mfma_f32_16x16x32_bf16 a[24:27], a[84:87], %[p00], a[24:27]\n\t"
    "v_mfma_f32_16x16x32_bf16 a[28:31], a[84:87], %[p01], a[28:31]\n\t"
    "ds_read_b128 a[76:79], %[vb] offset:9216\n\t"
    "s_waitcnt lgkmcnt(5)\n\t"
    "v_mfma_f32_16x16x32_bf16 a[32:35], a[88:91], %[p00], a[32:35]\n\t"
    "v_mfma_f32_16x16x32_bf16 a[36:39], a[88:91], %[p01], a[36:39]\n\t"
    "ds_read_b128 a[80:83], %[vb] offset:10240\n\t"
    "s_waitcnt lgkmcnt(5)\n\t"
    "v_mfma_f32_16x16x32_bf16 a[40:43], a[92:95], %[p00], a[40:43]\n\t"
    "v_mfma_f32_16x16x32_bf16 a[44:47], a[92:95], %[p01], a[44:47]\n\t"
    "ds_read_b128 a[84:87], %[vb] offset:11264\n\t"
    "s_waitcnt lgkmcnt(5)\n\t"
    "v_mfma_f32_16x16x32_bf16 a[48:51], a[96:99], %[p00], a[48:51]\n\t"
    "v_mfma_f32_16x16x32_bf16 a[52:55], a[96:99], %[p01], a[52:55]\n\t"
    "ds_read_b128 a[88:91], %[vb] offset:12288\n\t"
    "s_waitcnt lgkmcnt(5)\n\t"
    "v_mfma_f32_16x16x32_bf16 a[56:59], a[100:103], %[p00], a[56:59]\n\t"
    "v_mfma_f32_16x16x32_bf16 a[60:63], a[100:103], %[p01], a[60:63]\n\t"
    "ds_read_b128 a[92:95], %[vb] offset:13312\n\t"
    ".Lj%=:\n\t"
    "s_waitcnt lgkmcnt(5)\n\t"
    "v_mfma_f32_16x16x32_bf16 a[0:3], a[72:75], %[p10], a[0:3]\n\t"
    "v_mfma_f32_16x16x32_bf16 a[4:7], a[72:75], %[p11], a[4:7]\n\t"
    "ds_read_b128 a[96:99], %[vb] offset:14336\n\t"
    "s_waitcnt lgkmcnt(5)\n\t"
    "v_mfma_f32_16x16x32_bf16 a[8:11], a[76:79], %[p10], a[8:11]\n\t"
    "v_mfma_f32_16x16x32_bf16 a[12:15], a[76:79], %[p11], a[12:15]\n\t"
    "ds_read_b128 a[100:103], %[vb] offset:15360\n\t"
    "s_waitcnt lgkmcnt(5)\n\t"
    "v_mfma_f32_16x16x32_bf16 a[16:19], a[80:83], %[p10], a[16:19]\n\t"
    "v_mfma_f32_16x16x32_bf16 a[20:23], a[80:83], %[p11], a[20:23]\n\t"
    "ds_read_b128 a[72:75], %[kb] offset:0\n\t"
    "s_waitcnt lgkmcnt(5)\n\t"
    "v_mfma_f32_16x16x32_bf16 a[24:27], a[84:87], %[p10], a[24:27]\n\t"
    "v_mfma_f32_16x16x32_bf16 a[28:31], a[84:87], %[p11], a[28:31]\n\t"
    "ds_read_b128 a[76:79], %[kb] offset:1024\n\t"
    "s_waitcnt lgkmcnt(5)\n\t"
    "v_mfma_f32_16x16x32_bf16 a[32:35], a[88:91], %[p10], a[32:35]\n\t"
    "v_mfma_f32_16x16x32_bf16 a[36:39], a[88:91], %[p11], a[36:39]\n\t"
    "ds_read_b128 a[80:83], %[kb] offset:2048\n\t"
    "s_waitcnt lgkmcnt(5)\n\t"
    "v_mfma_f32_16x16x32_bf16 a[40:43], a[92:95], %[p10], a[40:43]\n\t"
    "v_mfma_f32_16x16x32_bf16 a[44:47], a[92:95], %[p11], a[44:47]\n\t"
    "ds_read_b128 a[84:87], %[kb] offset:3072\n\t"
    "s_waitcnt lgkmcnt(5)\n\t"
    "v_mfma_f32_16x16x32_bf16 a[48:51], a[96:99], %[p10], a[48:51]\n\t"
    "v_mfma_f32_16x16x32_bf16 a[52:55], a[96:99], %[p11], a[52:55]\n\t"
    "ds_read_b128 a[88:91], %[kb] offset:4096\n\t"
    "s_waitcnt lgkmcnt(5)\n\t"
    "v_mfma_f32_16x16x32_bf16 a[56:59], a[100:103], %[p10], a[56:59]\n\t"
    "v_mfma_f32_16x16x32_bf16 a[60:63], a[100:103], %[p11], a[60:63]\n\t"
    "ds_read_b128 a[92:95], %[kb] offset:5120\n\t"
    "s_waitcnt lgkmcnt(5)\n\t"
    "v_mfma_f32_16x16x32_bf16 %[s00], a[72:75], %[q00], %[n0]\n\t"
    "v_mfma_f32_16x16x32_bf16 %[s01], a[72:75], %[q01], %[n1]\n\t"
    "ds_read_b128 a[96:99], %[kb] offset:6144\n\t"
    "s_waitcnt lgkmcnt(5)\n\t"
    "v_mfma_f32_16x16x32_bf16 %[s00], a[76:79], %[q10], %[s00]\n\t"
    "v_mfma_f32_16x16x32_bf16 %[s01], a[76:79], %[q11], %[s01]\n\t"
    "ds_read_b128 a[100:103], %[kb] offset:7168\n\t"
    "s_waitcnt lgkmcnt(5)\n\t"
    "v_mfma_f32_16x16x32_bf16 %[s00], a[80:83], %[q20], %[s00]\n\t"
    "v_mfma_f32_16x16x32_bf16 %[s01], a[80:83], %[q21], %[s01]\n\t"
    "ds_read_b128 a[72:75], %[kb] offset:8192\n\t"
    "s_waitcnt lgkmcnt(5)\n\t"
    "v_mfma_f32_16x16x32_bf16 %[s00], a[84:87], %[q30], %[s00]\n\t"
    "v_mfma_f32_16x16x32_bf16 %[s01], a[84:87], %[q31], %[s01]\n\t"
    "ds_read_b128 a[76:79], %[kb] offset:9216\n\t"
    "s_waitcnt lgkmcnt(5)\n\t"
    "v_mfma_f32_16x16x32_bf16 %[s10], a[88:91], %[q00], %[n0]\n\t"
    "v_mfma_f32_16x16x32_bf16 %[s11], a[88:91], %[q01], %[n1]\n\t"
    "ds_read_b128 a[80:83], %[kb] offset:10240\n\t"
    "s_waitcnt lgkmcnt(5)\n\t"
    "v_mfma_f32_16x16x32_bf16 %[s10], a[92:95], %[q10], %[s10]\n\t"
    "v_mfma_f32_16x16x32_bf16 %[s11], a[92:95], %[q11], %[s11]\n\t"
    "ds_read_b128 a[84:87], %[kb] offset:11264\n\t"
    "s_waitcnt lgkmcnt(5)\n\t"
    "v_mfma_f32_16x16x32_bf16 %[s10], a[96:99], %[q20], %[s10]\n\t"
    "v_mfma_f32_16x16x32_bf16 %[s11], a[96:99], %[q21], %[s11]\n\t"
    "ds_read_b128 a[88:91], %[kb] offset:12288\n\t"
    "s_waitcnt lgkmcnt(5)\n\t"
    "v_mfma_f32_16x16x32_bf16 %[s10], a[100:103], %[q30], %[s10]\n\t"
    "v_mfma_f32_16x16x32_bf16 %[s11], a[100:103], %[q31], %[s11]\n\t"
    "ds_read_b128 a[92:95], %[kb] offset:13312\n\t"
    "s_waitcnt lgkmcnt(5)\n\t"
    "v_mfma_f32_16x16x32_bf16 %[s20], a[72:75], %[q00], %[n0]\n\t"
    "v_mfma_f32_16x16x32_bf16 %[s21], a[72:75], %[q01], %[n1]\n\t"
    "ds_read_b128 a[96:99], %[kb] offset:14336\n\t"
    "s_waitcnt lgkmcnt(5)\n\t"
    "v_mfma_f32_16x16x32_bf16 %[s20], a[76:79], %[q10], %[s20]\n\t"
    "v_mfma_f32_16x16x32_bf16 %[s21], a[76:79], %[q11], %[s21]\n\t"
    "ds_read_b128 a[100:103], %[kb] offset:15360\n\t"
    "s_waitcnt lgkmcnt(5)\n\t"
    "v_mfma_f32_16x16x32_bf16 %[s20], a[80:83], %[q20], %[s20]\n\t"
    "v_mfma_f32_16x16x32_bf16 %[s21], a[80:83], %[q21], %[s21]\n\t"
    "s_waitcnt lgkmcnt(4)\n\t"
    "v_mfma_f32_16x16x32_bf16 %[s20], a[84:87], %[q30], %[s20]\n\t"
    "v_mfma_f32_16x16x32_bf16 %[s21], a[84:87], %[q31], %[s21]\n\t"
    "s_waitcnt lgkmcnt(3)\n\t"
    "v_mfma_f32_16x16x32_bf16 %[s30], a[88:91], %[q00], %[n0]\n\t"
    "v_mfma_f32_16x16x32_bf16 %[s31], a[88:91], %[q01], %[n1]\n\t"
    "s_waitcnt lgkmcnt(2)\n\t"
    "v_mfma_f32_16x16x32_bf16 %[s30], a[92:95], %[q10], %[s30]\n\t"
    "v_mfma_f32_16x16x32_bf16 %[s31], a[92:95], %[q11], %[s31]\n\t"
    "s_waitcnt lgkmcnt(1)\n\t"
    "s_bitcmp1_b32 %[jt], %[j]\n\t"
    "s_cbranch_scc1 .Ls%=\n\t"
    "s_waitcnt vmcnt(0)\n\t"
    ".Lx%=:\n\t"
    "s_waitcnt lgkmcnt(0)\n\t"
    "s_barrier\n\t"
    "v_mfma_f32_16x16x32_bf16 %[s30], a[96:99], %[q20], %[s30]\n\t"
    "v_mfma_f32_16x16x32_bf16 %[s31], a[96:99], %[q21], %[s31]\n\t"
    "s_waitcnt lgkmcnt(0)\n\t"
    "v_mfma_f32_16x16x32_bf16 %[s30], a[100:103], %[q30], %[s30]\n\t"
    "v_mfma_f32_16x16x32_bf16 %[s31], a[100:103], %[q31], %[s31]\n\t"
    "s_branch .Le%=\n\t"
    ".Lz%=:\n\t"
    "v_mfma_f32_16x16x32_bf16 a[64:67], %[one], %[p00], 0\n\t"
    "v_mfma_f32_16x16x32_bf16 a[68:71], %[one], %[p01], 0\n\t"
    "v_mfma_f32_16x16x32_bf16 a[64:67], %[one], %[p10], a[64:67]\n\t"
    "v_mfma_f32_16x16x32_bf16 a[68:71], %[one], %[p11], a[68:71]\n\t"
    "s_waitcnt lgkmcnt(5)\n\t"
    "v_mfma_f32_16x16x32_bf16 a[0:3], a[72:75], %[p00], 0\n\t"
    "v_mfma_f32_16x16x32_bf16 a[4:7], a[72:75], %[p01], 0\n\t"
    "ds_read_b128 a[96:99], %[vb] offset:6144\n\t"
    "s_waitcnt lgkmcnt(5)\n\t"
    "v_mfma_f32_16x16x32_bf16 a[8:11], a[76:79], %[p00], 0\n\t"
    "v_mfma_f32_16x16x32_bf16 a[12:15], a[76:79], %[p01], 0\n\t"
    "ds_read_b128 a[100:103], %[vb] offset:7168\n\t"
    "s_waitcnt lgkmcnt(5)\n\t"
    "v_mfma_f32_16x16x32_bf16 a[16:19], a[80:83], %[p00], 0\n\t"
    "v_mfma_f32_16x16x32_bf16 a[20:23], a[80:83], %[p01], 0\n\t"
    "ds_read_b128 a[72:75], %[vb] offset:8192\n\t"
    "s_waitcnt lgkmcnt(5)\n\t"
    "v_mfma_f32_16x16x32_bf16 a[24:27], a[84:87], %[p00], 0\n\t"
    "v_mfma_f32_16x16x32_bf16 a[28:31], a[84:87], %[p01], 0\n\t"
    "ds_read_b128 a[76:79], %[vb] offset:9216\n\t"
    "s_waitcnt lgkmcnt(5)\n\t"
    "v_mfma_f32_16x16x32_bf16 a[32:35], a[88:91], %[p00], 0\n\t"
    "v_mfma_f32_16x16x32_bf16 a[36:39], a[88:91], %[p01], 0\n\t"
    "ds_read_b128 a[80:83], %[vb] offset:10240\n\t"
    "s_waitcnt lgkmcnt(5)\n\t"
    "v_mfma_f32_16x16x32_bf16 a[40:43], a[92:95], %[p00], 0\n\t"
    "v_mfma_f32_16x16x32_bf16 a[44:47], a[92:95], %[p01], 0\n\t"
    "ds_read_b128 a[84:87], %[vb] offset:11264\n\t"
    "s_waitcnt lgkmcnt(5)\n\t"
    "v_mfma_f32_16x16x32_bf16 a[48:51], a[96:99], %[p00], 0\n\t"
    "v_mfma_f32_16x16x32_bf16 a[52:55], a[96:99], %[p01], 0\n\t"
    "ds_read_b128 a[88:91], %[vb] offset:12288\n\t"
    "s_waitcnt lgkmcnt(5)\n\t"
    "v_mfma_f32_16x16x32_bf16 a[56:59], a[100:103], %[p00], 0\n\t"
    "v_mfma_f32_16x16x32_bf16 a[60:63], a[100:103], %[p01], 0\n\t"
    "ds_read_b128 a[92:95], %[vb] offset:13312\n\t"
    "s_branch .Lj%=\n\t"
    ".Ls%=:\n\t"
    "s_bitcmp0_b32 %[j], 0\n\t"
    "s_cbranch_scc1 .Lx%=\n\t"
    "s_waitcnt vmcnt(0)\n\t"
    "s_cmp_eq_u32 %[j], 3\n\t"
    "s_cbranch_scc1 .Ls1%=\n\t"
    "s_cmp_eq_u32 %[j], 5\n\t"
    "s_cbranch_scc1 .Ls2%=\n\t"
    "s_cmp_eq_u32 %[j], 7\n\t"
    "s_cbranch_scc1 .Ls3%=\n\t"
    "s_cmp_eq_u32 %[j], 9\n\t"
    "s_cbranch_scc1 .Ls4%=\n\t"
    ".Ls0%=:\n\t"
    "v_lshrrev_b32 v112, 8, %[vl]\n\t"
    "v_mul_u32_u24 v113, 14336, v112\n\t"
    "v_lshrrev_b32 v114, 2, %[vl]\n\t"
    "v_and_or_b32 v113, v114, 60, v113\n\t"
    "v_add_u32 v116, 0, v113\n\t"
    "v_add_u32 v117, 7168, v113\n\t"
    "v_add_u32 v118, 57344, v113\n\t"
    "v_add_u32 v119, 64512, v113\n\t"
    "global_load_dword a104, v116, %[pg]\n\t"
    "global_load_dword a105, v116, %[pg] offset:3584\n\t"
    "global_load_dword a106, v117, %[pg]\n\t"
    "global_load_dword a107, v117, %[pg] offset:3584\n\t"
    "global_load_dword a108, v118, %[pg]\n\t"
    "global_load_dword a109, v118, %[pg] offset:3584\n\t"
    "global_load_dword a110, v119, %[pg]\n\t"
    "global_load_dword a111, v119, %[pg] offset:3584\n\t"
    "v_lshrrev_b32 v114, 3, %[vl]\n\t"
    "v_and_b32 v114, 30, v114\n\t"
    "v_lshl_or_b32 v113, v112, 12, v114\n\t"
    "v_add_u32 v114, 0, v113\n\t"
    "v_add_u32 v115, 16384, v113\n\t"
    "global_load_ushort a112, v114, %[pd] offset:0\n\t"
    "global_load_ushort a113, v114, %[pd] offset:1024\n\t"
    "global_load_ushort a114, v114, %[pd] offset:2048\n\t"
    "global_load_ushort a115, v114, %[pd] offset:3072\n\t"
    "global_load_ushort a116, v115, %[pd] offset:0\n\t"
    "global_load_ushort a117, v115, %[pd] offset:1024\n\t"
    "global_load_ushort a118, v115, %[pd] offset:2048\n\t"
    "global_load_ushort a119, v115, %[pd] offset:3072\n\t"
    "v_lshlrev_b32 v112, 3, %[vl]\n\t"
    "s_mov_b64 exec, 0xffff\n\t"
    "global_load_dword a127, v112, %[po] offset:-4096\n\t"
    "s_mov_b64 exec, -1\n\t"
    "s_branch .Lx%=\n\t"
    ".Ls1%=:\n\t"
    "v_accvgpr_read_b32 v112, a104\n\t"
    "v_accvgpr_read_b32 v113, a105\n\t"
    "v_accvgpr_read_b32 v114, a106\n\t"
    "v_accvgpr_read_b32 v115, a107\n\t"
    "v_accvgpr_read_b32 v116, a108\n\t"
    "v_accvgpr_read_b32 v117, a109\n\t"
    "v_accvgpr_read_b32 v118, a110\n\t"
    "v_accvgpr_read_b32 v119, a111\n\t"
    "s_mov_b32 %[t0], 0x05040100\n\t"
    "v_perm_b32 v120, v113, v112, %[t0]\n\t"
    "v_perm_b32 v121, v115, v114, %[t0]\n\t"
    "v_perm_b32 v122, v117, v116, %[t0]\n\t"
    "v_perm_b32 v123, v119, v118, %[t0]\n\t"
    "s_mov_b32 %[t0], 0x07060302\n\t"
    "v_perm_b32 v124, v113, v112, %[t0]\n\t"
    "v_perm_b32 v125, v115, v114, %[t0]\n\t"
    "v_perm_b32 v126, v117, v116, %[t0]\n\t"
    "v_perm_b32 v127, v119, v118, %[t0]\n\t"
    "v_mov_b32 v112, v120\n\t"
    "v_mov_b32 v113, v121\n\t"
    "v_mov_b32 v114, v122\n\t"
    "v_mov_b32 v115, v123\n\t"
    "v_and_b32 v117, 0x70, %[vl]\n\t"
    "v_and_b32 v116, 0x300, %[vl]\n\t"
    "v_lshl_add_u32 v116, v117, 1, v116\n\t"
    "v_lshrrev_b32 v117, 3, %[vl]\n\t"
    "v_and_or_b32 v116, v117, 16, v116\n\t"
    "v_mov_b32_dpp v120, v124 row_ror:8 row_mask:0xf bank_mask:0xc\n\t"
    "v_mov_b32_dpp v121, v125 row_ror:8 row_mask:0xf bank_mask:0xc\n\t"
    "v_mov_b32_dpp v122, v126 row_ror:8 row_mask:0xf bank_mask:0xc\n\t"
    "v_mov_b32_dpp v123, v127 row_ror:8 row_mask:0xf bank_mask:0xc\n\t"
    "v_mov_b32_dpp v124, v112 row_ror:8 row_mask:0xf bank_mask:0x3\n\t"
    "v_mov_b32_dpp v125, v113 row_ror:8 row_mask:0xf bank_mask:0x3\n\t"
    "v_mov_b32_dpp v126, v114 row_ror:8 row_mask:0xf bank_mask:0x3\n\t"
    "v_mov_b32_dpp v127, v115 row_ror:8 row_mask:0xf bank_mask:0x3\n\t"
    "s_add_u32 %[t0], %[sg], 0\n\t"
    "buffer_store_dwordx4 v[120:123], v116, %[rs], %[t0] offen sc1\n\t"
    "s_add_u32 %[t0], %[t0], 32768\n\t"
    "buffer_store_dwordx4 v[124:127], v116, %[rs], %[t0] offen sc1\n\t"
    "s_nop 1\n\t"
    "v_accvgpr_read_b32 v112, a112\n\t"
    "v_accvgpr_read_b32 v113, a113\n\t"
    "v_accvgpr_read_b32 v114, a114\n\t"
    "v_accvgpr_read_b32 v115, a115\n\t"
    "v_accvgpr_read_b32 v116, a116\n\t"
    "v_accvgpr_read_b32 v117, a117\n\t"
    "v_accvgpr_read_b32 v118, a118\n\t"
    "v_accvgpr_read_b32 v119, a119\n\t"
    "v_lshl_or_b32 v120, v113, 16, v112\n\t"
    "v_lshl_or_b32 v121, v115, 16, v114\n\t"
    "v_lshl_or_b32 v122, v117, 16, v116\n\t"
    "v_lshl_or_b32 v123, v119, 16, v118\n\t"
    "s_add_u32 %[t0], %[sd], 0\n\t"
    "buffer_store_dwordx4 v[120:123], %[vl], %[rs], %[t0] offen sc1\n\t"
    "v_lshrrev_b32 v112, 8, %[vl]\n\t"
    "v_mul_u32_u24 v113, 14336, v112\n\t"
    "v_lshrrev_b32 v114, 2, %[vl]\n\t"
    "v_and_or_b32 v113, v114, 60, v113\n\t"
    "v_add_u32 v116, 256, v113\n\t"
    "v_add_u32 v117, 7424, v113\n\t"
    "v_add_u32 v118, 57600, v113\n\t"
    "v_add_u32 v119, 64768, v113\n\t"
    "global_load_dword a104, v116, %[pg]\n\t"
    "global_load_dword a105, v116, %[pg] offset:3584\n\t"
    "global_load_dword a106, v117, %[pg]\n\t"
    "global_load_dword a107, v117, %[pg] offset:3584\n\t"
    "global_load_dword a108, v118, %[pg]\n\t"
    "global_load_dword a109, v118, %[pg] offset:3584\n\t"
    "global_load_dword a110, v119, %[pg]\n\t"
    "global_load_dword a111, v119, %[pg] offset:3584\n\t"
    "v_lshrrev_b32 v114, 3, %[vl]\n\t"
    "v_and_b32 v114, 30, v114\n\t"
    "v_lshl_or_b32 v113, v112, 12, v114\n\t"
    "v_add_u32 v114, 128, v113\n\t"
    "v_add_u32 v115, 16512, v113\n\t"
    "global_load_ushort a112, v114, %[pd] offset:0\n\t"
    "global_load_ushort a113, v114, %[pd] offset:1024\n\t"
    "global_load_ushort a114, v114, %[pd] offset:2048\n\t"
    "global_load_ushort a115, v114, %[pd] offset:3072\n\t"
    "global_load_ushort a116, v115, %[pd] offset:0\n\t"
    "global_load_ushort a117, v115, %[pd] offset:1024\n\t"
    "global_load_ushort a118, v115, %[pd] offset:2048\n\t"
    "global_load_ushort a119, v115, %[pd] offset:3072\n\t"
    "v_lshlrev_b32 v112, 3, %[vl]\n\t"
    "s_mov_b64 exec, 0xffff\n\t"
    "global_load_dword a127, v112, %[po] offset:-2048\n\t"
    "s_mov_b64 exec, -1\n\t"
    "s_branch .Lx%=\n\t"
    ".Ls2%=:\n\t"
    "v_accvgpr_read_b32 v112, a104\n\t"
    "v_accvgpr_read_b32 v113, a105\n\t"
    "v_accvgpr_read_b32 v114, a106\n\t"
    "v_accvgpr_read_b32 v115, a107\n\t"
    "v_accvgpr_read_b32 v116, a108\n\t"
    "v_accvgpr_read_b32 v117, a109\n\t"
    "v_accvgpr_read_b32 v118, a110\n\t"
    "v_accvgpr_read_b32 v119, a111\n\t"
    "s_mov_b32 %[t0], 0x05040100\n\t"
    "v_perm_b32 v120, v113, v112, %[t0]\n\t"
    "v_perm_b32 v121, v115, v114, %[t0]\n\t"
    "v_perm_b32 v122, v117, v116, %[t0]\n\t"
    "v_perm_b32 v123, v119, v118, %[t0]\n\t"
    "s_mov_b32 %[t0], 0x07060302\n\t"
    "v_perm_b32 v124, v113, v112, %[t0]\n\t"
    "v_perm_b32 v125, v115, v114, %[t0]\n\t"
    "v_perm_b32 v126, v117, v116, %[t0]\n\t"
    "v_perm_b32 v127, v119, v118, %[t0]\n\t"
    "v_mov_b32 v112, v120\n\t"
    "v_mov_b32 v113, v121\n\t"
    "v_mov_b32 v114, v122\n\t"
    "v_mov_b32 v115, v123\n\t"
    "v_and_b32 v117, 0x70, %[vl]\n\t"
    "v_and_b32 v116, 0x300, %[vl]\n\t"
    "v_lshl_add_u32 v116, v117, 1, v116\n\t"
    "v_lshrrev_b32 v117, 3, %[vl]\n\t"
    "v_and_or_b32 v116, v117, 16, v116\n\t"
    "v_mov_b32_dpp v120, v124 row_ror:8 row_mask:0xf bank_mask:0xc\n\t"
    "v_mov_b32_dpp v121, v125 row_ror:8 row_mask:0xf bank_mask:0xc\n\t"
    "v_mov_b32_dpp v122, v126 row_ror:8 row_mask:0xf bank_mask:0xc\n\t"
    "v_mov_b32_dpp v123, v127 row_ror:8 row_mask:0xf bank_mask:0xc\n\t"
    "v_mov_b32_dpp v124, v112 row_ror:8 row_mask:0xf bank_mask:0x3\n\t"
    "v_mov_b32_dpp v125, v113 row_ror:8 row_mask:0xf bank_mask:0x3\n\t"
    "v_mov_b32_dpp v126, v114 row_ror:8 row_mask:0xf bank_mask:0x3\n\t"
    "v_mov_b32_dpp v127, v115 row_ror:8 row_mask:0xf bank_mask:0x3\n\t"
    "s_add_u32 %[t0], %[sg], 262144\n\t"
    "buffer_store_dwordx4 v[120:123], v116, %[rs], %[t0] offen sc1\n\t"
    "s_add_u32 %[t0], %[t0], 32768\n\t"
    "buffer_store_dwordx4 v[124:127], v116, %[rs], %[t0] offen sc1\n\t"
    "s_nop 1\n\t"
    "v_accvgpr_read_b32 v112, a112\n\t"
    "v_accvgpr_read_b32 v113, a113\n\t"
    "v_accvgpr_read_b32 v114, a114\n\t"
    "v_accvgpr_read_b32 v115, a115\n\t"
    "v_accvgpr_read_b32 v116, a116\n\t"
    "v_accvgpr_read_b32 v117, a117\n\t"
    "v_accvgpr_read_b32 v118, a118\n\t"
    "v_accvgpr_read_b32 v119, a119\n\t"
    "v_lshl_or_b32 v120, v113, 16, v112\n\t"
    "v_lshl_or_b32 v121, v115, 16, v114\n\t"
    "v_lshl_or_b32 v122, v117, 16, v116\n\t"
    "v_lshl_or_b32 v123, v119, 16, v118\n\t"
    "s_add_u32 %[t0], %[sd], 4096\n\t"
    "buffer_store_dwordx4 v[120:123], %[vl], %[rs], %[t0] offen sc1\n\t"
    "v_lshrrev_b32 v112, 8, %[vl]\n\t"
    "v_mul_u32_u24 v113, 14336, v112\n\t"
    "v_lshrrev_b32 v114, 2, %[vl]\n\t"
    "v_and_or_b32 v113, v114, 60, v113\n\t"
    "v_add_u32 v116, 114688, v113\n\t"
    "v_add_u32 v117, 121856, v113\n\t"
    "v_add_u32 v118, 172032, v113\n\t"
    "v_add_u32 v119, 179200, v113\n\t"
    "global_load_dword a104, v116, %[pg]\n\t"
    "global_load_dword a105, v116, %[pg] offset:3584\n\t"
    "global_load_dword a106, v117, %[pg]\n\t"
    "global_load_dword a107, v117, %[pg] offset:3584\n\t"
    "global_load_dword a108, v118, %[pg]\n\t"
    "global_load_dword a109, v118, %[pg] offset:3584\n\t"
    "global_load_dword a110, v119, %[pg]\n\t"
    "global_load_dword a111, v119, %[pg] offset:3584\n\t"
    "v_lshrrev_b32 v114, 3, %[vl]\n\t"
    "v_and_b32 v114, 30, v114\n\t"
    "v_lshl_or_b32 v113, v112, 12, v114\n\t"
    "v_add_u32 v114, 256, v113\n\t"
    "v_add_u32 v115, 16640, v113\n\t"
    "global_load_ushort a112, v114, %[pd] offset:0\n\t"
    "global_load_ushort a113, v114, %[pd] offset:1024\n\t"
    "global_load_ushort a114, v114, %[pd] offset:2048\n\t"
    "global_load_ushort a115, v114, %[pd] offset:3072\n\t"
    "global_load_ushort a116, v115, %[pd] offset:0\n\t"
    "global_load_ushort a117, v115, %[pd] offset:1024\n\t"
    "global_load_ushort a118, v115, %[pd] offset:2048\n\t"
    "global_load_ushort a119, v115, %[pd] offset:3072\n\t"
    "v_lshlrev_b32 v112, 3, %[vl]\n\t"
    "s_mov_b64 exec, 0xffff\n\t"
    "global_load_dword a127, v112, %[po] offset:0\n\t"
    "s_mov_b64 exec, -1\n\t"
    "s_branch .Lx%=\n\t"
    ".Ls3%=:\n\t"
    "v_accvgpr_read_b32 v112, a104\n\t"
    "v_accvgpr_read_b32 v113, a105\n\t"
    "v_accvgpr_read_b32 v114, a106\n\t"
    "v_accvgpr_read_b32 v115, a107\n\t"
    "v_accvgpr_read_b32 v116, a108\n\t"
    "v_accvgpr_read_b32 v117, a109\n\t"
    "v_accvgpr_read_b32 v118, a110\n\t"
    "v_accvgpr_read_b32 v119, a111\n\t"
    "s_mov_b32 %[t0], 0x05040100\n\t"
    "v_perm_b32 v120, v113, v112, %[t0]\n\t"
    "v_perm_b32 v121, v115, v114, %[t0]\n\t"
    "v_perm_b32 v122, v117, v116, %[t0]\n\t"
    "v_perm_b32 v123, v119, v118, %[t0]\n\t"
    "s_mov_b32 %[t0], 0x07060302\n\t"
    "v_perm_b32 v124, v113, v112, %[t0]\n\t"
    "v_perm_b32 v125, v115, v114, %[t0]\n\t"
    "v_perm_b32 v126, v117, v116, %[t0]\n\t"
    "v_perm_b32 v127, v119, v118, %[t0]\n\t"
    "v_mov_b32 v112, v120\n\t"
    "v_mov_b32 v113, v121\n\t"
    "v_mov_b32 v114, v122\n\t"
    "v_mov_b32 v115, v123\n\t"
    "v_and_b32 v117, 0x70, %[vl]\n\t"
    "v_and_b32 v116, 0x300, %[vl]\n\t"
    "v_lshl_add_u32 v116, v117, 1, v116\n\t"
    "v_lshrrev_b32 v117, 3, %[vl]\n\t"
    "v_and_or_b32 v116, v117, 16, v116\n\t"
    "v_mov_b32_dpp v120, v124 row_ror:8 row_mask:0xf bank_mask:0xc\n\t"
    "v_mov_b32_dpp v121, v125 row_ror:8 row_mask:0xf bank_mask:0xc\n\t"
    "v_mov_b32_dpp v122, v126 row_ror:8 row_mask:0xf bank_mask:0xc\n\t"
    "v_mov_b32_dpp v123, v127 row_ror:8 row_mask:0xf bank_mask:0xc\n\t"
    "v_mov_b32_dpp v124, v112 row_ror:8 row_mask:0xf bank_mask:0x3\n\t"
    "v_mov_b32_dpp v125, v113 row_ror:8 row_mask:0xf bank_mask:0x3\n\t"
    "v_mov_b32_dpp v126, v114 row_ror:8 row_mask:0xf bank_mask:0x3\n\t"
    "v_mov_b32_dpp v127, v115 row_ror:8 row_mask:0xf bank_mask:0x3\n\t"
    "s_add_u32 %[t0], %[sg], 2048\n\t"
    "buffer_store_dwordx4 v[120:123], v116, %[rs], %[t0] offen sc1\n\t"
    "s_add_u32 %[t0], %[t0], 32768\n\t"
    "buffer_store_dwordx4 v[124:127], v116, %[rs], %[t0] offen sc1\n\t"
    "s_nop 1\n\t"
    "v_accvgpr_read_b32 v112, a112\n\t"
    "v_accvgpr_read_b32 v113, a113\n\t"
    "v_accvgpr_read_b32 v114, a114\n\t"
    "v_accvgpr_read_b32 v115, a115\n\t"
    "v_accvgpr_read_b32 v116, a116\n\t"
    "v_accvgpr_read_b32 v117, a117\n\t"
    "v_accvgpr_read_b32 v118, a118\n\t"
    "v_accvgpr_read_b32 v119, a119\n\t"
    "v_lshl_or_b32 v120, v113, 16, v112\n\t"
    "v_lshl_or_b32 v121, v115, 16, v114\n\t"
    "v_lshl_or_b32 v122, v117, 16, v116\n\t"
    "v_lshl_or_b32 v123, v119, 16, v118\n\t"
    "s_add_u32 %[t0], %[sd], 8192\n\t"
    "buffer_store_dwordx4 v[120:123], %[vl], %[rs], %[t0] offen sc1\n\t"
    "v_lshrrev_b32 v112, 8, %[vl]\n\t"
    "v_mul_u32_u24 v113, 14336, v112\n\t"
    "v_lshrrev_b32 v114, 2, %[vl]\n\t"
    "v_and_or_b32 v113, v114, 60, v113\n\t"
    "v_add_u32 v116, 114944, v113\n\t"
    "v_add_u32 v117, 122112, v113\n\t"
    "v_add_u32 v118, 172288, v113\n\t"
    "v_add_u32 v119, 179456, v113\n\t"
    "global_load_dword a104, v116, %[pg]\n\t"
    "global_load_dword a105, v116, %[pg] offset:3584\n\t"
    "global_load_dword a106, v117, %[pg]\n\t"
    "global_load_dword a107, v117, %[pg] offset:3584\n\t"
    "global_load_dword a108, v118, %[pg]\n\t"
    "global_load_dword a109, v118, %[pg] offset:3584\n\t"
    "global_load_dword a110, v119, %[pg]\n\t"
    "global_load_dword a111, v119, %[pg] offset:3584\n\t"
    "v_lshrrev_b32 v114, 3, %[vl]\n\t"
    "v_and_b32 v114, 30, v114\n\t"
    "v_lshl_or_b32 v113, v112, 12, v114\n\t"
    "v_add_u32 v114, 384, v113\n\t"
    "v_add_u32 v115, 16768, v113\n\t"
    "global_load_ushort a112, v114, %[pd] offset:0\n\t"
    "global_load_ushort a113, v114, %[pd] offset:1024\n\t"
    "global_load_ushort a114, v114, %[pd] offset:2048\n\t"
    "global_load_ushort a115, v114, %[pd] offset:3072\n\t"
    "global_load_ushort a116, v115, %[pd] offset:0\n\t"
    "global_load_ushort a117, v115, %[pd] offset:1024\n\t"
    "global_load_ushort a118, v115, %[pd] offset:2048\n\t"
    "global_load_ushort a119, v115, %[pd] offset:3072\n\t"
    "v_lshlrev_b32 v112, 3, %[vl]\n\t"
    "s_mov_b64 exec, 0xffff\n\t"
    "global_load_dword a127, v112, %[po] offset:2048\n\t"
    "s_mov_b64 exec, -1\n\t"
    "s_branch .Lx%=\n\t"
    ".Ls4%=:\n\t"
    "v_accvgpr_read_b32 v112, a104\n\t"
    "v_accvgpr_read_b32 v113, a105\n\t"
    "v_accvgpr_read_b32 v114, a106\n\t"
    "v_accvgpr_read_b32 v115, a107\n\t"
    "v_accvgpr_read_b32 v116, a108\n\t"
    "v_accvgpr_read_b32 v117, a109\n\t"
    "v_accvgpr_read_b32 v118, a110\n\t"
    "v_accvgpr_read_b32 v119, a111\n\t"
    "s_mov_b32 %[t0], 0x05040100\n\t"
    "v_perm_b32 v120, v113, v112, %[t0]\n\t"
    "v_perm_b32 v121, v115, v114, %[t0]\n\t"
    "v_perm_b32 v122, v117, v116, %[t0]\n\t"
    "v_perm_b32 v123, v119, v118, %[t0]\n\t"
    "s_mov_b32 %[t0], 0x07060302\n\t"
    "v_perm_b32 v124, v113, v112, %[t0]\n\t"
    "v_perm_b32 v125, v115, v114, %[t0]\n\t"
    "v_perm_b32 v126, v117, v116, %[t0]\n\t"
    "v_perm_b32 v127, v119, v118, %[t0]\n\t"
    "v_mov_b32 v112, v120\n\t"
    "v_mov_b32 v113, v121\n\t"
    "v_mov_b32 v114, v122\n\t"
    "v_mov_b32 v115, v123\n\t"
    "v_and_b32 v117, 0x70, %[vl]\n\t"
    "v_and_b32 v116, 0x300, %[vl]\n\t"
    "v_lshl_add_u32 v116, v117, 1, v116\n\t"
    "v_lshrrev_b32 v117, 3, %[vl]\n\t"
    "v_and_or_b32 v116, v117, 16, v116\n\t"
    "v_mov_b32_dpp v120, v124 row_ror:8 row_mask:0xf bank_mask:0xc\n\t"
    "v_mov_b32_dpp v121, v125 row_ror:8 row_mask:0xf bank_mask:0xc\n\t"
    "v_mov_b32_dpp v122, v126 row_ror:8 row_mask:0xf bank_mask:0xc\n\t"
    "v_mov_b32_dpp v123, v127 row_ror:8 row_mask:0xf bank_mask:0xc\n\t"
    "v_mov_b32_dpp v124, v112 row_ror:8 row_mask:0xf bank_mask:0x3\n\t"
    "v_mov_b32_dpp v125, v113 row_ror:8 row_mask:0xf bank_mask:0x3\n\t"
    "v_mov_b32_dpp v126, v114 row_ror:8 row_mask:0xf bank_mask:0x3\n\t"
    "v_mov_b32_dpp v127, v115 row_ror:8 row_mask:0xf bank_mask:0x3\n\t"
    "s_add_u32 %[t0], %[sg], 264192\n\t"
    "buffer_store_dwordx4 v[120:123], v116, %[rs], %[t0] offen sc1\n\t"
    "s_add_u32 %[t0], %[t0], 32768\n\t"
    "buffer_store_dwordx4 v[124:127], v116, %[rs], %[t0] offen sc1\n\t"
    "s_nop 1\n\t"
    "v_accvgpr_read_b32 v112, a112\n\t"
    "v_accvgpr_read_b32 v113, a113\n\t"
    "v_accvgpr_read_b32 v114, a114\n\t"
    "v_accvgpr_read_b32 v115, a115\n\t"
    "v_accvgpr_read_b32 v116, a116\n\t"
    "v_accvgpr_read_b32 v117, a117\n\t"
    "v_accvgpr_read_b32 v118, a118\n\t"
    "v_accvgpr_read_b32 v119, a119\n\t"
    "v_lshl_or_b32 v120, v113, 16, v112\n\t"
    "v_lshl_or_b32 v121, v115, 16, v114\n\t"
    "v_lshl_or_b32 v122, v117, 16, v116\n\t"
    "v_lshl_or_b32 v123, v119, 16, v118\n\t"
    "s_add_u32 %[t0], %[sd], 12288\n\t"
    "buffer_store_dwordx4 v[120:123], %[vl], %[rs], %[t0] offen sc1\n\t"
    "s_branch .Lx%=\n\t"
    ".Le%=:\n\t"
    "s_nop 7\n\t"
    : [s00] "=&v"(S[0][0]), [s01] "=&v"(S[0][1]), [s10] "=&v"(S[1][0]), [s11] "=&v"(S[1][1]), [s20] "=&v"(S[2][0]), [s21] "=&v"(S[2][1]), [s30] "=&v"(S[3][0]), [s31] "=&v"(S[3][1]), [p00] "+{v[112:115]}"(P[0][0]), [p01] "+{v[116:119]}"(P[0][1]), [p10] "+{v[120:123]}"(P[1][0]), [p11] "+{v[124:127]}"(P[1][1]), [t0] "=&s"(tS)
    : [q00] "v"(Q[0][0]), [q01] "v"(Q[0][1]), [q10] "v"(Q[1][0]), [q11] "v"(Q[1][1]), [q20] "v"(Q[2][0]), [q21] "v"(Q[2][1]), [q30] "v"(Q[3][0]), [q31] "v"(Q[3][1]), [n0] "v"(NM[0]), [n1] "v"(NM[1]), [kb] "v"(kb), [one] "v"(ones), [vb] "v"(vb), [zf] "s"(zf), [j] "s"(j), [jt] "s"(mG), [vl] "v"(vL), [rs] "s"(srdS), [pg] "s"(pG), [pd] "s"(pD), [po] "s"(pO), [sg] "s"(sGo), [sd] "s"(sDo)
    : "memory", "scc", ACLOB, "a104", "a105", "a106", "a107", "a108", "a109", "a110", "a111", "a112", "a113", "a114", "a115", "a116", "a117", "a118", "a119", "a127");

    zf = 0u;
    vb = kb + L_V;
  }
  ST(2); RT(1, 3);
  TM_OUT;
  kargp ka = (kargp)__builtin_amdgcn_kernarg_segment_ptr();
  unsigned b2 = blockIdx.x;
  asm volatile("" : "+s"(ka), "+s"(b2));
  const u16* x2 = *(const u16* const __attribute__((address_space(4)))*)(ka + 0);
  const u16* wo2 = *(const u16* const __attribute__((address_space(4)))*)(ka + 32);
  u16* out2 = *(u16* const __attribute__((address_space(4)))*)(ka + 64);
  const unsigned xcd2 = b2 & 7u, idx2 = b2 >> 3, pi2 = idx2 & 7u, head2 = 4u * xcd2 + (idx2 >> 3), qbA2 = 15u - pi2, qbB2 = pi2;
  __amdgpu_buffer_rsrc_t srdW = mk_rsrc(wo2, HD * DM * 2);
  asm volatile("" : "+v"(vL2));
  const unsigned vW3 = ((vL2 >> 6) & 3u) * 1024u + (vL2 >> 8) * 64u + ((((vL2 >> 4) & 3u) ^ ((wid & 1u) << 1)) * 16u);
  const unsigned sW3 = (512u * xcd2 + 4u * wid) * 1024u + (unsigned)(idx2 >> 3) * 256u;
  const unsigned sA0 = (unsigned)WS_CT + ((8u * qbB2 + wid) * 128u + 16u * xcd2) * 1024u, sA1 = (unsigned)WS_CT + ((8u * qbA2 + wid) * 128u + 16u * xcd2) * 1024u;
  const unsigned dA3 = lds0 + wid * 1024u;
  const unsigned fCB = (unsigned)WS_FLG + (F_CB + (unsigned)(xcd2 * 8 + pi2)) * 128u;
  unsigned pollB, t0;
  asm volatile("buffer_load_dword %0, off, %1, %2 sc1" : "=v"(pollB) : "s"(srdS), "s"(fCB) : "memory");
  if (grp == 0) {
    unsigned sWx = sW3, dWx = dA3;
    asm volatile(
      "s_add_u32 %[t0], %[sw], 0\n\t"
      "s_add_u32 m0, %[da], 0\n\t"
      "s_nop 0\n\t"
      "buffer_load_dwordx4 %[vw], %[rw], %[t0] offen lds\n\t"
      "s_add_u32 %[t0], %[sw], 32768\n\t"
      "s_add_u32 m0, %[da], 8192\n\t"
      "s_nop 0\n\t"
      "buffer_load_dwordx4 %[vw], %[rw], %[t0] offen lds\n\t"
      "s_add_u32 %[t0], %[sw], 65536\n\t"
      "s_add_u32 m0, %[da], 16384\n\t"
      "s_nop 0\n\t"
      "buffer_load_dwordx4 %[vw], %[rw], %[t0] offen lds\n\t"
      "s_add_u32 %[t0], %[sw], 98304\n\t"
      "s_add_u32 m0, %[da], 24576\n\t"
      "s_nop 0\n\t"
      "buffer_load_dwordx4 %[vw], %[rw], %[t0] offen lds\n\t"
      "s_add_u32 %[t0], %[sw], 131072\n\t"
      "s_add_u32 m0, %[da], 32768\n\t"
      "s_nop 0\n\t"
      "buffer_load_dwordx4 %[vw], %[rw], %[t0] offen lds\n\t"
      "s_add_u32 %[t0], %[sw], 163840\n\t"
      "s_add_u32 m0, %[da], 40960\n\t"
      "s_nop 0\n\t"
      "buffer_load_dwordx4 %[vw], %[rw], %[t0] offen lds\n\t"
      "s_add_u32 %[t0], %[sw], 196608\n\t"
      "s_add_u32 m0, %[da], 49152\n\t"
      "s_nop 0\n\t"
      "buffer_load_dwordx4 %[vw], %[rw], %[t0] offen lds\n\t"
      : [t0] "=&s"(t0)
      : [sw] "s"(sWx), [vw] "v"(vW3), [rw] "s"(srdW), [da] "s"(dWx)
      : "memory", "scc");
  }
  RT(1, 5);
  asm volatile(
    "s_waitcnt lgkmcnt(0)\n\t"
    "ds_read_b128 a[72:75], %[vb] offset:0\n\t"
    "ds_read_b128 a[76:79], %[vb] offset:1024\n\t"
    "ds_read_b128 a[80:83], %[vb] offset:2048\n\t"
    "ds_read_b128 a[84:87], %[vb] offset:3072\n\t"
    "ds_read_b128 a[88:91], %[vb] offset:4096\n\t"
    "ds_read_b128 a[92:95], %[vb] offset:5120\n\t"
    "s_cmp_lg_u32 %[zf], 0\n\t"
    "s_cbranch_scc1 .Lz%=\n\t"
    "v_mfma_f32_16x16x32_bf16 a[64:67], %[one], %[p00], a[64:67]\n\t"
    "v_mfma_f32_16x16x32_bf16 a[68:71], %[one], %[p01], a[68:71]\n\t"
    "v_mfma_f32_16x16x32_bf16 a[64:67], %[one], %[p10], a[64:67]\n\t"
    "v_mfma_f32_16x16x32_bf16 a[68:71], %[one], %[p11], a[68:71]\n\t"
    "s_waitcnt lgkmcnt(5)\n\t"
    "v_mfma_f32_16x16x32_bf16 a[0:3], a[72:75], %[p00], a[0:3]\n\t"
    "v_mfma_f32_16x16x32_bf16 a[4:7], a[72:75], %[p01], a[4:7]\n\t"
    "ds_read_b128 a[96:99], %[vb] offset:6144\n\t"
    "s_waitcnt lgkmcnt(5)\n\t"
    "v_mfma_f32_16x16x32_bf16 a[8:11], a[76:79], %[p00], a[8:11]\n\t"
    "v_mfma_f32_16x16x32_bf16 a[12:15], a[76:79], %[p01], a[12:15]\n\t"
    "ds_read_b128 a[100:103], %[vb] offset:7168\n\t"
    "s_waitcnt lgkmcnt(5)\n\t"
    "v_mfma_f32_16x16x32_bf16 a[16:19], a[80:83], %[p00], a[16:19]\n\t"
    "v_mfma_f32_16x16x32_bf16 a[20:23], a[80:83], %[p01], a[20:23]\n\t"
    "ds_read_b128 a[72:75], %[vb] offset:8192\n\t"
    "s_waitcnt lgkmcnt(5)\n\t"
    "v_mfma_f32_16x16x32_bf16 a[24:27], a[84:87], %[p00], a[24:27]\n\t"
    "v_mfma_f32_16x16x32_bf16 a[28:31], a[84:87], %[p01], a[28:31]\n\t"
    "ds_read_b128 a[76:79], %[vb] offset:9216\n\t"
    "s_waitcnt lgkmcnt(5)\n\t"
    "v_mfma_f32_16x16x32_bf16 a[32:35], a[88:91], %[p00], a[32:35]\n\t"
    "v_mfma_f32_16x16x32_bf16 a[36:39], a[88:91], %[p01], a[36:39]\n\t"
    "ds_read_b128 a[80:83], %[vb] offset:10240\n\t"
    "s_waitcnt lgkmcnt(5)\n\t"
    "v_mfma_f32_16x16x32_bf16 a[40:43], a[92:95], %[p00], a[40:43]\n\t"
    "v_mfma_f32_16x16x32_bf16 a[44:47], a[92:95], %[p01], a[44:47]\n\t"
    "ds_read_b128 a[84:87], %[vb] offset:11264\n\t"
    "s_waitcnt lgkmcnt(5)\n\t"
    "v_mfma_f32_16x16x32_bf16 a[48:51], a[96:99], %[p00], a[48:51]\n\t"
    "v_mfma_f32_16x16x32_bf16 a[52:55], a[96:99], %[p01], a[52:55]\n\t"
    "ds_read_b128 a[88:91], %[vb] offset:12288\n\t"
    "s_waitcnt lgkmcnt(5)\n\t"
    "v_mfma_f32_16x16x32_bf16 a[56:59], a[100:103], %[p00], a[56:59]\n\t"
    "v_mfma_f32_16x16x32_bf16 a[60:63], a[100:103], %[p01], a[60:63]\n\t"
    "ds_read_b128 a[92:95], %[vb] offset:13312\n\t"
    ".Lj%=:\n\t"
    "s_waitcnt lgkmcnt(5)\n\t"
    "v_mfma_f32_16x16x32_bf16 a[0:3], a[72:75], %[p10], a[0:3]\n\t"
    "v_mfma_f32_16x16x32_bf16 a[4:7], a[72:75], %[p11], a[4:7]\n\t"
    "ds_read_b128 a[96:99], %[vb] offset:14336\n\t"
    "s_waitcnt lgkmcnt(5)\n\t"
    "v_mfma_f32_16x16x32_bf16 a[8:11], a[76:79], %[p10], a[8:11]\n\t"
    "v_mfma_f32_16x16x32_bf16 a[12:15], a[76:79], %[p11], a[12:15]\n\t"
    "ds_read_b128 a[100:103], %[vb] offset:15360\n\t"
    "s_waitcnt lgkmcnt(5)\n\t"
    "v_mfma_f32_16x16x32_bf16 a[16:19], a[80:83], %[p10], a[16:19]\n\t"
    "v_mfma_f32_16x16x32_bf16 a[20:23], a[80:83], %[p11], a[20:23]\n\t"
    "s_waitcnt lgkmcnt(4)\n\t"
    "v_mfma_f32_16x16x32_bf16 a[24:27], a[84:87], %[p10], a[24:27]\n\t"
    "v_mfma_f32_16x16x32_bf16 a[28:31], a[84:87], %[p11], a[28:31]\n\t"
    "s_waitcnt lgkmcnt(3)\n\t"
    "v_mfma_f32_16x16x32_bf16 a[32:35], a[88:91], %[p10], a[32:35]\n\t"
    "v_mfma_f32_16x16x32_bf16 a[36:39], a[88:91], %[p11], a[36:39]\n\t"
    "s_waitcnt lgkmcnt(2)\n\t"
    "v_mfma_f32_16x16x32_bf16 a[40:43], a[92:95], %[p10], a[40:43]\n\t"
    "v_mfma_f32_16x16x32_bf16 a[44:47], a[92:95], %[p11], a[44:47]\n\t"
    "s_waitcnt lgkmcnt(1)\n\t"
    "v_mfma_f32_16x16x32_bf16 a[48:51], a[96:99], %[p10], a[48:51]\n\t"
    "v_mfma_f32_16x16x32_bf16 a[52:55], a[96:99], %[p11], a[52:55]\n\t"
    "s_waitcnt lgkmcnt(0)\n\t"
    "v_mfma_f32_16x16x32_bf16 a[56:59], a[100:103], %[p10], a[56:59]\n\t"
    "v_mfma_f32_16x16x32_bf16 a[60:63], a[100:103], %[p11], a[60:63]\n\t"
    "s_branch .Le%=\n\t"
    ".Lz%=:\n\t"
    "v_mfma_f32_16x16x32_bf16 a[64:67], %[one], %[p00], 0\n\t"
    "v_mfma_f32_16x16x32_bf16 a[68:71], %[one], %[p01], 0\n\t"
    "v_mfma_f32_16x16x32_bf16 a[64:67], %[one], %[p10], a[64:67]\n\t"
    "v_mfma_f32_16x16x32_bf16 a[68:71], %[one], %[p11], a[68:71]\n\t"
    "s_waitcnt lgkmcnt(5)\n\t"
    "v_mfma_f32_16x16x32_bf16 a[0:3], a[72:75], %[p00], 0\n\t"
    "v_mfma_f32_16x16x32_bf16 a[4:7], a[72:75], %[p01], 0\n\t"
    "ds_read_b128 a[96:99], %[vb] offset:6144\n\t"
    "s_waitcnt lgkmcnt(5)\n\t"
    "v_mfma_f32_16x16x32_bf16 a[8:11], a[76:79], %[p00], 0\n\t"
    "v_mfma_f32_16x16x32_bf16 a[12:15], a[76:79], %[p01], 0\n\t"
    "ds_read_b128 a[100:103], %[vb] offset:7168\n\t"
    "s_waitcnt lgkmcnt(5)\n\t"
    "v_mfma_f32_16x16x32_bf16 a[16:19], a[80:83], %[p00], 0\n\t"
    "v_mfma_f32_16x16x32_bf16 a[20:23], a[80:83], %[p01], 0\n\t"
    "ds_read_b128 a[72:75], %[vb] offset:8192\n\t"
    "s_waitcnt lgkmcnt(5)\n\t"
    "v_mfma_f32_16x16x32_bf16 a[24:27], a[84:87], %[p00], 0\n\t"
    "v_mfma_f32_16x16x32_bf16 a[28:31], a[84:87], %[p01], 0\n\t"
    "ds_read_b128 a[76:79], %[vb] offset:9216\n\t"
    "s_waitcnt lgkmcnt(5)\n\t"
    "v_mfma_f32_16x16x32_bf16 a[32:35], a[88:91], %[p00], 0\n\t"
    "v_mfma_f32_16x16x32_bf16 a[36:39], a[88:91], %[p01], 0\n\t"
    "ds_read_b128 a[80:83], %[vb] offset:10240\n\t"
    "s_waitcnt lgkmcnt(5)\n\t"
    "v_mfma_f32_16x16x32_bf16 a[40:43], a[92:95], %[p00], 0\n\t"
    "v_mfma_f32_16x16x32_bf16 a[44:47], a[92:95], %[p01], 0\n\t"
    "ds_read_b128 a[84:87], %[vb] offset:11264\n\t"
    "s_waitcnt lgkmcnt(5)\n\t"
    "v_mfma_f32_16x16x32_bf16 a[48:51], a[96:99], %[p00], 0\n\t"
    "v_mfma_f32_16x16x32_bf16 a[52:55], a[96:99], %[p01], 0\n\t"
    "ds_read_b128 a[88:91], %[vb] offset:12288\n\t"
    "s_waitcnt lgkmcnt(5)\n\t"
    "v_mfma_f32_16x16x32_bf16 a[56:59], a[100:103], %[p00], 0\n\t"
    "v_mfma_f32_16x16x32_bf16 a[60:63], a[100:103], %[p01], 0\n\t"
    "ds_read_b128 a[92:95], %[vb] offset:13312\n\t"
    "s_branch .Lj%=\n\t"
    ".Le%=:\n\t"
    "s_nop 7\n\t"
    :
    : [p00] "v"(P[0][0]), [p01] "v"(P[0][1]), [p10] "v"(P[1][0]), [p11] "v"(P[1][1]), [one] "v"(ones), [vb] "v"(vb), [zf] "s"(zf)
    : "memory", "scc", ACLOB);

  RT(1, 6);
  if (grp == 0) {
    WAIT_V(7); BAR();
    unsigned sWx = sW3 + 16384u, dWx = dA3 + 4096u;
    asm volatile(
      "s_add_u32 %[t0], %[sw], 0\n\t"
      "s_add_u32 m0, %[da], 0\n\t"
      "s_nop 0\n\t"
      "buffer_load_dwordx4 %[vw], %[rw], %[t0] offen lds\n\t"
      "s_add_u32 %[t0], %[sw], 32768\n\t"
      "s_add_u32 m0, %[da], 8192\n\t"
      "s_nop 0\n\t"
      "buffer_load_dwordx4 %[vw], %[rw], %[t0] offen lds\n\t"
      "s_add_u32 %[t0], %[sw], 65536\n\t"
      "s_add_u32 m0, %[da], 16384\n\t"
      "s_nop 0\n\t"
      "buffer_load_dwordx4 %[vw], %[rw], %[t0] offen lds\n\t"
      "s_add_u32 %[t0], %[sw], 98304\n\t"
      "s_add_u32 m0, %[da], 24576\n\t"
      "s_nop 0\n\t"
      "buffer_load_dwordx4 %[vw], %[rw], %[t0] offen lds\n\t"
      "s_add_u32 %[t0], %[sw], 131072\n\t"
      "s_add_u32 m0, %[da], 32768\n\t"
      "s_nop 0\n\t"
      "buffer_load_dwordx4 %[vw], %[rw], %[t0] offen lds\n\t"
      "s_add_u32 %[t0], %[sw], 163840\n\t"
      "s_add_u32 m0, %[da], 40960\n\t"
      "s_nop 0\n\t"
      "buffer_load_dwordx4 %[vw], %[rw], %[t0] offen lds\n\t"
      "s_add_u32 %[t0], %[sw], 196608\n\t"
      "s_add_u32 m0, %[da], 49152\n\t"
      "s_nop 0\n\t"
      "buffer_load_dwordx4 %[vw], %[rw], %[t0] offen lds\n\t"
      : [t0] "=&s"(t0)
      : [sw] "s"(sWx), [vw] "v"(vW3), [rw] "s"(srdW), [da] "s"(dWx)
      : "memory", "scc");
  }
  {
    const unsigned mine = lds0 + 65536u + grp * 65536u + w4 * 8192u + vL2, other = mine ^ 196608u;
    const unsigned mlm = lds0 + 57344u + wid * 1024u + vL2;
    const bool unset = thr < 0.f;
    const float m0 = unset ? ninf : m[0], m1 = unset ? ninf : m[1];
    float l[2];
    asm volatile("v_accvgpr_read_b32 %0, a64\n\tv_accvgpr_read_b32 %1, a68" : "=v"(l[0]), "=v"(l[1]));
    f32x4 o[4][2], ob[4][2];
    if (grp == 0) {
      asm volatile(
        "ds_write_b128 %0, a[32:35] offset:0\n\t"
        "ds_write_b128 %0, a[36:39] offset:1024\n\t"
        "ds_write_b128 %0, a[40:43] offset:2048\n\t"
        "ds_write_b128 %0, a[44:47] offset:3072\n\t"
        "ds_write_b128 %0, a[48:51] offset:4096\n\t"
        "ds_write_b128 %0, a[52:55] offset:5120\n\t"
        "ds_write_b128 %0, a[56:59] offset:6144\n\t"
        "ds_write_b128 %0, a[60:63] offset:7168\n\t"
        :: "v"(mine) : "memory");
      asm volatile("v_accvgpr_read_b32 %0, a0\n\tv_accvgpr_read_b32 %1, a1\n\tv_accvgpr_read_b32 %2, a2\n\tv_accvgpr_read_b32 %3, a3" : "=v"(o[0][0][0]), "=v"(o[0][0][1]), "=v"(o[0][0][2]), "=v"(o[0][0][3])); asm volatile("v_accvgpr_read_b32 %0, a4\n\tv_accvgpr_read_b32 %1, a5\n\tv_accvgpr_read_b32 %2, a6\n\tv_accvgpr_read_b32 %3, a7" : "=v"(o[0][1][0]), "=v"(o[0][1][1]), "=v"(o[0][1][2]), "=v"(o[0][1][3])); asm volatile("v_accvgpr_read_b32 %0, a8\n\tv_accvgpr_read_b32 %1, a9\n\tv_accvgpr_read_b32 %2, a10\n\tv_accvgpr_read_b32 %3, a11" : "=v"(o[1][0][0]), "=v"(o[1][0][1]), "=v"(o[1][0][2]), "=v"(o[1][0][3])); asm volatile("v_accvgpr_read_b32 %0, a12\n\tv_accvgpr_read_b32 %1, a13\n\tv_accvgpr_read_b32 %2, a14\n\tv_accvgpr_read_b32 %3, a15" : "=v"(o[1][1][0]), "=v"(o[1][1][1]), "=v"(o[1][1][2]), "=v"(o[1][1][3])); asm volatile("v_accvgpr_read_b32 %0, a16\n\tv_accvgpr_read_b32 %1, a17\n\tv_accvgpr_read_b32 %2, a18\n\tv_accvgpr_read_b32 %3, a19" : "=v"(o[2][0][0]), "=v"(o[2][0][1]), "=v"(o[2][0][2]), "=v"(o[2][0][3])); asm volatile("v_accvgpr_read_b32 %0, a20\n\tv_accvgpr_read_b32 %1, a21\n\tv_accvgpr_read_b32 %2, a22\n\tv_accvgpr_read_b32 %3, a23" : "=v"(o[2][1][0]), "=v"(o[2][1][1]), "=v"(o[2][1][2]), "=v"(o[2][1][3])); asm volatile("v_accvgpr_read_b32 %0, a24\n\tv_accvgpr_read_b32 %1, a25\n\tv_accvgpr_read_b32 %2, a26\n\tv_accvgpr_read_b32 %3, a27" : "=v"(o[3][0][0]), "=v"(o[3][0][1]), "=v"(o[3][0][2]), "=v"(o[3][0][3])); asm volatile("v_accvgpr_read_b32 %0, a28\n\tv_accvgpr_read_b32 %1, a29\n\tv_accvgpr_read_b32 %2, a30\n\tv_accvgpr_read_b32 %3, a31" : "=v"(o[3][1][0]), "=v"(o[3][1][1]), "=v"(o[3][1][2]), "=v"(o[3][1][3]));
      asm volatile("" : "+v"(pollB));
    } else {
      asm volatile(
        "ds_write_b128 %0, a[0:3] offset:0\n\t"
        "ds_write_b128 %0, a[4:7] offset:1024\n\t"
        "ds_write_b128 %0, a[8:11] offset:2048\n\t"
        "ds_write_b128 %0, a[12:15] offset:3072\n\t"
        "ds_write_b128 %0, a[16:19] offset:4096\n\t"
        "ds_write_b128 %0, a[20:23] offset:5120\n\t"
        "ds_write_b128 %0, a[24:27] offset:6144\n\t"
        "ds_write_b128 %0, a[28:31] offset:7168\n\t"
        :: "v"(mine) : "memory");
      asm volatile("v_accvgpr_read_b32 %0, a32\n\tv_accvgpr_read_b32 %1, a33\n\tv_accvgpr_read_b32 %2, a34\n\tv_accvgpr_read_b32 %3, a35" : "=v"(o[0][0][0]), "=v"(o[0][0][1]), "=v"(o[0][0][2]), "=v"(o[0][0][3])); asm volatile("v_accvgpr_read_b32 %0, a36\n\tv_accvgpr_read_b32 %1, a37\n\tv_accvgpr_read_b32 %2, a38\n\tv_accvgpr_read_b32 %3, a39" : "=v"(o[0][1][0]), "=v"(o[0][1][1]), "=v"(o[0][1][2]), "=v"(o[0][1][3])); asm volatile("v_accvgpr_read_b32 %0, a40\n\tv_accvgpr_read_b32 %1, a41\n\tv_accvgpr_read_b32 %2, a42\n\tv_accvgpr_read_b32 %3, a43" : "=v"(o[1][0][0]), "=v"(o[1][0][1]), "=v"(o[1][0][2]), "=v"(o[1][0][3])); asm volatile("v_accvgpr_read_b32 %0, a44\n\tv_accvgpr_read_b32 %1, a45\n\tv_accvgpr_read_b32 %2, a46\n\tv_accvgpr_read_b32 %3, a47" : "=v"(o[1][1][0]), "=v"(o[1][1][1]), "=v"(o[1][1][2]), "=v"(o[1][1][3])); asm volatile("v_accvgpr_read_b32 %0, a48\n\tv_accvgpr_read_b32 %1, a49\n\tv_accvgpr_read_b32 %2, a50\n\tv_accvgpr_read_b32 %3, a51" : "=v"(o[2][0][0]), "=v"(o[2][0][1]), "=v"(o[2][0][2]), "=v"(o[2][0][3])); asm volatile("v_accvgpr_read_b32 %0, a52\n\tv_accvgpr_read_b32 %1, a53\n\tv_accvgpr_read_b32 %2, a54\n\tv_accvgpr_read_b32 %3, a55" : "=v"(o[2][1][0]), "=v"(o[2][1][1]), "=v"(o[2][1][2]), "=v"(o[2][1][3])); asm volatile("v_accvgpr_read_b32 %0, a56\n\tv_accvgpr_read_b32 %1, a57\n\tv_accvgpr_read_b32 %2, a58\n\tv_accvgpr_read_b32 %3, a59" : "=v"(o[3][0][0]), "=v"(o[3][0][1]), "=v"(o[3][0][2]), "=v"(o[3][0][3])); asm volatile("v_accvgpr_read_b32 %0, a60\n\tv_accvgpr_read_b32 %1, a61\n\tv_accvgpr_read_b32 %2, a62\n\tv_accvgpr_read_b32 %3, a63" : "=v"(o[3][1][0]), "=v"(o[3][1][1]), "=v"(o[3][1][2]), "=v"(o[3][1][3]));
      asm volatile("s_waitcnt vmcnt(0)" : "+v"(pollB) :: "memory");
    }
    lds_w128(mlm, __builtin_bit_cast(u32x4, f32x4{m0, l[0], m1, l[1]}));
    WAIT_L(0);
    BAR();
    RT(1, 7);
    if (pi2 == 7) count_up(srdS, F_CB + (unsigned)(xcd2 * 8 + pi2), wid == 4u);
    const bool ready = rfl(pollB) >= 4u;
    if (LIKELY(ready)) {
    asm volatile(
      "s_add_u32 %[t0], %[sa0], 0\n\t"
      "s_add_u32 m0, %[da], 98304\n\t"
      "s_nop 0\n\t"
      "buffer_load_dwordx4 %[vl], %[rs], %[t0] offen sc1 lds\n\t"
      "s_add_u32 %[t0], %[sa0], 1024\n\t"
      "s_add_u32 m0, %[da], 106496\n\t"
      "s_nop 0\n\t"
      "buffer_load_dwordx4 %[vl], %[rs], %[t0] offen sc1 lds\n\t"
      "s_add_u32 %[t0], %[sa0], 2048\n\t"
      "s_add_u32 m0, %[da], 114688\n\t"
      "s_nop 0\n\t"
      "buffer_load_dwordx4 %[vl], %[rs], %[t0] offen sc1 lds\n\t"
      "s_add_u32 %[t0], %[sa0], 3072\n\t"
      "s_add_u32 m0, %[da], 122880\n\t"
      "s_nop 0\n\t"
      "buffer_load_dwordx4 %[vl], %[rs], %[t0] offen sc1 lds\n\t"
      : [t0] "=&s"(t0)
      : [vl] "v"(vL2), [rs] "s"(srdS), [sa0] "s"(sA0), [da] "s"(dA3)
      : "memory", "scc");
    }
    f32x4 ot = lds_f4(mlm ^ 4096u);
#pragma unroll
    for (int i = 0; i < 4; ++i)
#pragma unroll
      for (int qt = 0; qt < 2; ++qt) ob[i][qt] = lds_f4(other + (i * 2 + qt) * 1024u);
    asm volatile("s_waitcnt lgkmcnt(0)" : "+v"(ot), "+v"(ob[0][0]), "+v"(ob[0][1]), "+v"(ob[1][0]), "+v"(ob[1][1]), "+v"(ob[2][0]), "+v"(ob[2][1]), "+v"(ob[3][0]), "+v"(ob[3][1]) :: "memory");
    BAR();
    RT(1, 8);
    if (LIKELY(ready)) {
    asm volatile(
      "s_add_u32 %[t0], %[sa0], 4096\n\t"
      "s_add_u32 m0, %[da], 131072\n\t"
      "s_nop 0\n\t"
      "buffer_load_dwordx4 %[vl], %[rs], %[t0] offen sc1 lds\n\t"
      "s_add_u32 %[t0], %[sa0], 5120\n\t"
      "s_add_u32 m0, %[da], 139264\n\t"
      "s_nop 0\n\t"
      "buffer_load_dwordx4 %[vl], %[rs], %[t0] offen sc1 lds\n\t"
      "s_add_u32 %[t0], %[sa0], 6144\n\t"
      "s_add_u32 m0, %[da], 147456\n\t"
      "s_nop 0\n\t"
      "buffer_load_dwordx4 %[vl], %[rs], %[t0] offen sc1 lds\n\t"
      "s_add_u32 %[t0], %[sa0], 7168\n\t"
      "s_add_u32 m0, %[da], 155648\n\t"
      "s_nop 0\n\t"
      "buffer_load_dwordx4 %[vl], %[rs], %[t0] offen sc1 lds\n\t"
      "s_add_u32 %[t0], %[sa0], 8192\n\t"
      "s_add_u32 m0, %[da], 81920\n\t"
      "s_nop 0\n\t"
      "buffer_load_dwordx4 %[vl], %[rs], %[t0] offen sc1 lds\n\t"
      "s_add_u32 %[t0], %[sa0], 9216\n\t"
      "s_add_u32 m0, %[da], 90112\n\t"
      "s_nop 0\n\t"
      "buffer_load_dwordx4 %[vl], %[rs], %[t0] offen sc1 lds\n\t"
      : [t0] "=&s"(t0)
      : [vl] "v"(vL2), [rs] "s"(srdS), [sa0] "s"(sA0), [da] "s"(dA3)
      : "memory", "scc");
    }
    asm volatile(
      "s_add_u32 %[t0], %[sw], 229376\n\t"
      "s_add_u32 m0, %[da], 57344\n\t"
      "s_nop 0\n\t"
      "buffer_load_dwordx4 %[vw], %[rw], %[t0] offen lds\n\t"
      "s_add_u32 %[t0], %[sw], 262144\n\t"
      "s_add_u32 m0, %[da], 65536\n\t"
      "s_nop 0\n\t"
      "buffer_load_dwordx4 %[vw], %[rw], %[t0] offen lds\n\t"
      "s_add_u32 %[t0], %[sw], 294912\n\t"
      "s_add_u32 m0, %[da], 73728\n\t"
      "s_nop 0\n\t"
      "buffer_load_dwordx4 %[vw], %[rw], %[t0] offen lds\n\t"
      : [t0] "=&s"(t0)
      : [sw] "s"(sW3), [vw] "v"(vW3), [rw] "s"(srdW), [da] "s"(dA3)
      : "memory", "scc");
    RT(1, 9);
    const unsigned so = (unsigned)WS_CT + ((unsigned)(qbA2 * 8) + w4 * 2u) * 131072u + (unsigned)head2 * 4096u + grp * 2048u;
#pragma unroll
    for (int qt = 0; qt < 2; ++qt) {
      const float ma = qt ? m1 : m0, mb = ot[2 * qt], M = __builtin_fmaxf(ma, mb);
      const float wa = __builtin_amdgcn_exp2f(ma - M), wb = __builtin_amdgcn_exp2f(mb - M);
      const float il = 1.f / (wa * l[qt] + wb * ot[2 * qt + 1]);
      const float fa = wa * il, fb = wb * il;
      f32x4 f[4];
#pragma unroll
      for (int i = 0; i < 4; ++i) f[i] = o[i][qt] * fa + ob[i][qt] * fb;
#pragma unroll
      for (int u = 0; u < 2; ++u) sstore16(pack2(f[2 * u], f[2 * u + 1]), srdS, vL2, so + qt * 131072u + u * 1024u);
    }
    if (UNLIKELY(!ready)) {
      do {
        __builtin_amdgcn_s_sleep(2);
        asm volatile("buffer_load_dword %0, off, %1, %2 sc1\n\ts_waitcnt vmcnt(0)" : "=v"(pollB) : "s"(srdS), "s"(fCB) : "memory");
      } while (rfl(pollB) < 4u);
    asm volatile(
      "s_add_u32 %[t0], %[sa0], 0\n\t"
      "s_add_u32 m0, %[da], 98304\n\t"
      "s_nop 0\n\t"
      "buffer_load_dwordx4 %[vl], %[rs], %[t0] offen sc1 lds\n\t"
      "s_add_u32 %[t0], %[sa0], 1024\n\t"
      "s_add_u32 m0, %[da], 106496\n\t"
      "s_nop 0\n\t"
      "buffer_load_dwordx4 %[vl], %[rs], %[t0] offen sc1 lds\n\t"
      "s_add_u32 %[t0], %[sa0], 2048\n\t"
      "s_add_u32 m0, %[da], 114688\n\t"
      "s_nop 0\n\t"
      "buffer_load_dwordx4 %[vl], %[rs], %[t0] offen sc1 lds\n\t"
      "s_add_u32 %[t0], %[sa0], 3072\n\t"
      "s_add_u32 m0, %[da], 122880\n\t"
      "s_nop 0\n\t"
      "buffer_load_dwordx4 %[vl], %[rs], %[t0] offen sc1 lds\n\t"
      : [t0] "=&s"(t0)
      : [vl] "v"(vL2), [rs] "s"(srdS), [sa0] "s"(sA0), [da] "s"(dA3)
      : "memory", "scc");
    asm volatile(
      "s_add_u32 %[t0], %[sa0], 4096\n\t"
      "s_add_u32 m0, %[da], 131072\n\t"
      "s_nop 0\n\t"
      "buffer_load_dwordx4 %[vl], %[rs], %[t0] offen sc1 lds\n\t"
      "s_add_u32 %[t0], %[sa0], 5120\n\t"
      "s_add_u32 m0, %[da], 139264\n\t"
      "s_nop 0\n\t"
      "buffer_load_dwordx4 %[vl], %[rs], %[t0] offen sc1 lds\n\t"
      "s_add_u32 %[t0], %[sa0], 6144\n\t"
      "s_add_u32 m0, %[da], 147456\n\t"
      "s_nop 0\n\t"
      "buffer_load_dwordx4 %[vl], %[rs], %[t0] offen sc1 lds\n\t"
      "s_add_u32 %[t0], %[sa0], 7168\n\t"
      "s_add_u32 m0, %[da], 155648\n\t"
      "s_nop 0\n\t"
      "buffer_load_dwordx4 %[vl], %[rs], %[t0] offen sc1 lds\n\t"
      "s_add_u32 %[t0], %[sa0], 8192\n\t"
      "s_add_u32 m0, %[da], 81920\n\t"
      "s_nop 0\n\t"
      "buffer_load_dwordx4 %[vl], %[rs], %[t0] offen sc1 lds\n\t"
      "s_add_u32 %[t0], %[sa0], 9216\n\t"
      "s_add_u32 m0, %[da], 90112\n\t"
      "s_nop 0\n\t"
      "buffer_load_dwordx4 %[vl], %[rs], %[t0] offen sc1 lds\n\t"
      : [t0] "=&s"(t0)
      : [vl] "v"(vL2), [rs] "s"(srdS), [sa0] "s"(sA0), [da] "s"(dA3)
      : "memory", "scc");
      WAIT_V(0);
    }
  }
  ST(3); RT(1, 4);
  {
  unsigned vL = vL2;
#define LANES asm volatile("" : "+v"(vL)); const unsigned lane = vL >> 4
  const unsigned sp = (unsigned)xcd2, bi = (unsigned)idx2 >> 2, bn = (unsigned)idx2 & 3u, par = (unsigned)idx2 & 1u;
  const float* mod = *(const float* const __attribute__((address_space(4)))*)(ka + 72);
  u32x32 WV;
  TS(0);
  const unsigned fg = sp < 7u ? sp : 6u, tb = 16u * par + ((unsigned)idx2 >> 1);
  const unsigned sG = (unsigned)WS_WGU + (16u * fg + wid) * 32768u;
  {
    LANES;
    const unsigned g = lane >> 4, wr = wid >> 1, wc = wid & 1u;
    const unsigned aB = lds0 + wr * 2048u + vL, b0 = lds0 + g * 1024u + wc * 512u + ((lane >> 2) & 3u) * 64u + (g & 1u) * 32u + (lane & 3u) * 8u;
    const unsigned sO0 = (unsigned)WS_PART + sp * 2097152u + ((8u * qbB2 + 2u * wr) * 16u + 4u * ((unsigned)idx2 >> 3) + 2u * wc) * 1024u, sO1 = sO0 + (unsigned)(qbA2 - qbB2) * 131072u;
    const unsigned f0 = (unsigned)WS_FLG + (F_S3 + qbB2) * 128u, fCA = fCB + (F_CA - F_CB) * 128u, fW = (unsigned)WS_FLG + (F_WT + sp) * 128u;
    asm volatile(
      "s_waitcnt vmcnt(16)\n\t"
      "s_barrier\n\t"
      "ds_read_b64_tr_b16 v[16:17], %[b00] offset:0\n\t"
      "ds_read_b64_tr_b16 v[18:19], %[b00] offset:4096\n\t"
      "ds_read_b128 v[32:35], %[a1] offset:32768\n\t"
      "ds_read_b64_tr_b16 v[20:21], %[b10] offset:0\n\t"
      "ds_read_b64_tr_b16 v[22:23], %[b10] offset:4096\n\t"
      "ds_read_b128 v[36:39], %[a1] offset:33792\n\t"
      "ds_read_b64_tr_b16 v[24:25], %[b00] offset:256\n\t"
      "ds_read_b64_tr_b16 v[26:27], %[b00] offset:4352\n\t"
      "ds_read_b64_tr_b16 v[28:29], %[b10] offset:256\n\t"
      "ds_read_b64_tr_b16 v[30:31], %[b10] offset:4352\n\t"
      "s_waitcnt lgkmcnt(7)\n\t"
      "v_mfma_f32_16x16x32_bf16 v[64:67], v[16:19], v[32:35], 0\n\t"
      "s_waitcnt lgkmcnt(5)\n\t"
      "v_mfma_f32_16x16x32_bf16 v[68:71], v[20:23], v[32:35], 0\n\t"
      "s_waitcnt vmcnt(14) lgkmcnt(0)\n\t"
      "s_barrier\n\t"
      "ds_read_b64_tr_b16 v[40:41], %[b00] offset:8192\n\t"
      "ds_read_b64_tr_b16 v[42:43], %[b00] offset:12288\n\t"
      "s_add_u32 %[t0], %[sa0], 10240\n\t"
      "s_add_u32 m0, %[da], 98304\n\t"
      "s_nop 0\n\t"
      "buffer_load_dwordx4 %[vl], %[rs], %[t0] offen sc1 lds\n\t"
      "v_mfma_f32_16x16x32_bf16 v[80:83], v[16:19], v[36:39], 0\n\t"
      "ds_read_b128 v[56:59], %[a1] offset:40960\n\t"
      "ds_read_b64_tr_b16 v[44:45], %[b10] offset:8192\n\t"
      "s_add_u32 %[t0], %[sw], 327680\n\t"
      "s_add_u32 m0, %[da], 0\n\t"
      "s_nop 0\n\t"
      "buffer_load_dwordx4 %[vw], %[rw], %[t0] offen lds\n\t"
      "v_mfma_f32_16x16x32_bf16 v[84:87], v[20:23], v[36:39], 0\n\t"
      "ds_read_b64_tr_b16 v[46:47], %[b10] offset:12288\n\t"
      "ds_read_b128 v[60:63], %[a1] offset:41984\n\t"
      "v_mfma_f32_16x16x32_bf16 v[72:75], v[24:27], v[32:35], 0\n\t"
      "ds_read_b64_tr_b16 v[48:49], %[b00] offset:8448\n\t"
      "ds_read_b64_tr_b16 v[50:51], %[b00] offset:12544\n\t"
      "v_mfma_f32_16x16x32_bf16 v[88:91], v[24:27], v[36:39], 0\n\t"
      "ds_read_b64_tr_b16 v[52:53], %[b10] offset:8448\n\t"
      "v_mfma_f32_16x16x32_bf16 v[76:79], v[28:31], v[32:35], 0\n\t"
      "ds_read_b64_tr_b16 v[54:55], %[b10] offset:12544\n\t"
      "v_mfma_f32_16x16x32_bf16 v[92:95], v[28:31], v[36:39], 0\n\t"
      "s_waitcnt lgkmcnt(7)\n\t"
      "v_mfma_f32_16x16x32_bf16 v[64:67], v[40:43], v[56:59], v[64:67]\n\t"
      "ds_read_b64_tr_b16 v[16:17], %[b00] offset:16384\n\t"
      "ds_read_b64_tr_b16 v[18:19], %[b00] offset:20480\n\t"
      "s_waitcnt lgkmcnt(7)\n\t"
      "v_mfma_f32_16x16x32_bf16 v[68:71], v[44:47], v[56:59], v[68:71]\n\t"
      "ds_read_b128 v[32:35], %[a1] offset:49152\n\t"
      "s_waitcnt lgkmcnt(7)\n\t"
      "v_mfma_f32_16x16x32_bf16 v[80:83], v[40:43], v[60:63], v[80:83]\n\t"
      "ds_read_b64_tr_b16 v[20:21], %[b10] offset:16384\n\t"
      "v_mfma_f32_16x16x32_bf16 v[84:87], v[44:47], v[60:63], v[84:87]\n\t"
      "ds_read_b64_tr_b16 v[22:23], %[b10] offset:20480\n\t"
      "ds_read_b128 v[36:39], %[a1] offset:50176\n\t"
      "s_waitcnt lgkmcnt(8)\n\t"
      "v_mfma_f32_16x16x32_bf16 v[72:75], v[48:51], v[56:59], v[72:75]\n\t"
      "ds_read_b64_tr_b16 v[24:25], %[b00] offset:16640\n\t"
      "v_mfma_f32_16x16x32_bf16 v[88:91], v[48:51], v[60:63], v[88:91]\n\t"
      "ds_read_b64_tr_b16 v[26:27], %[b00] offset:20736\n\t"
      "s_waitcnt lgkmcnt(8)\n\t"
      "v_mfma_f32_16x16x32_bf16 v[76:79], v[52:55], v[56:59], v[76:79]\n\t"
      "ds_read_b64_tr_b16 v[28:29], %[b10] offset:16640\n\t"
      "ds_read_b64_tr_b16 v[30:31], %[b10] offset:20736\n\t"
      "v_mfma_f32_16x16x32_bf16 v[92:95], v[52:55], v[60:63], v[92:95]\n\t"
      "s_waitcnt lgkmcnt(7)\n\t"
      "v_mfma_f32_16x16x32_bf16 v[64:67], v[16:19], v[32:35], v[64:67]\n\t"
      "s_waitcnt lgkmcnt(5)\n\t"
      "v_mfma_f32_16x16x32_bf16 v[68:71], v[20:23], v[32:35], v[68:71]\n\t"
      "s_waitcnt vmcnt(14) lgkmcnt(0)\n\t"
      "s_barrier\n\t"
      "ds_read_b64_tr_b16 v[40:41], %[b00] offset:24576\n\t"
      "ds_read_b64_tr_b16 v[42:43], %[b00] offset:28672\n\t"
      "v_mfma_f32_16x16x32_bf16 v[80:83], v[16:19], v[36:39], v[80:83]\n\t"
      "ds_read_b128 v[56:59], %[a1] offset:57344\n\t"
      "ds_read_b64_tr_b16 v[44:45], %[b10] offset:24576\n\t"
      "v_mfma_f32_16x16x32_bf16 v[84:87], v[20:23], v[36:39], v[84:87]\n\t"
      "ds_read_b64_tr_b16 v[46:47], %[b10] offset:28672\n\t"
      "ds_read_b128 v[60:63], %[a1] offset:58368\n\t"
      "v_mfma_f32_16x16x32_bf16 v[72:75], v[24:27], v[32:35], v[72:75]\n\t"
      "ds_read_b64_tr_b16 v[48:49], %[b00] offset:24832\n\t"
      "ds_read_b64_tr_b16 v[50:51], %[b00] offset:28928\n\t"
      "v_mfma_f32_16x16x32_bf16 v[88:91], v[24:27], v[36:39], v[88:91]\n\t"
      "ds_read_b64_tr_b16 v[52:53], %[b10] offset:24832\n\t"
      "s_add_u32 %[t0], %[sa0], 11264\n\t"
      "s_add_u32 m0, %[da], 106496\n\t"
      "s_nop 0\n\t"
      "buffer_load_dwordx4 %[vl], %[rs], %[t0] offen sc1 lds\n\t"
      "v_mfma_f32_16x16x32_bf16 v[76:79], v[28:31], v[32:35], v[76:79]\n\t"
      "ds_read_b64_tr_b16 v[54:55], %[b10] offset:28928\n\t"
      "s_add_u32 %[t0], %[sa0], 12288\n\t"
      "s_add_u32 m0, %[da], 114688\n\t"
      "s_nop 0\n\t"
      "buffer_load_dwordx4 %[vl], %[rs], %[t0] offen sc1 lds\n\t"
      "v_mfma_f32_16x16x32_bf16 v[92:95], v[28:31], v[36:39], v[92:95]\n\t"
      "s_add_u32 %[t0], %[sw], 360448\n\t"
      "s_add_u32 m0, %[da], 8192\n\t"
      "s_nop 0\n\t"
      "buffer_load_dwordx4 %[vw], %[rw], %[t0] offen lds\n\t"
      "s_waitcnt lgkmcnt(7)\n\t"
      "v_mfma_f32_16x16x32_bf16 v[64:67], v[40:43], v[56:59], v[64:67]\n\t"
      "ds_read_b64_tr_b16 v[16:17], %[b00] offset:32768\n\t"
      "ds_read_b64_tr_b16 v[18:19], %[b00] offset:36864\n\t"
      "s_add_u32 %[t0], %[sw], 393216\n\t"
      "s_add_u32 m0, %[da], 16384\n\t"
      "s_nop 0\n\t"
      "buffer_load_dwordx4 %[vw], %[rw], %[t0] offen lds\n\t"
      "s_waitcnt lgkmcnt(7)\n\t"
      "v_mfma_f32_16x16x32_bf16 v[68:71], v[44:47], v[56:59], v[68:71]\n\t"
      "ds_read_b128 v[32:35], %[a2] offset:0\n\t"
      "s_waitcnt lgkmcnt(7)\n\t"
      "v_mfma_f32_16x16x32_bf16 v[80:83], v[40:43], v[60:63], v[80:83]\n\t"
      "ds_read_b64_tr_b16 v[20:21], %[b10] offset:32768\n\t"
      "v_mfma_f32_16x16x32_bf16 v[84:87], v[44:47], v[60:63], v[84:87]\n\t"
      "ds_read_b64_tr_b16 v[22:23], %[b10] offset:36864\n\t"
      "ds_read_b128 v[36:39], %[a2] offset:1024\n\t"
      "s_waitcnt lgkmcnt(8)\n\t"
      "v_mfma_f32_16x16x32_bf16 v[72:75], v[48:51], v[56:59], v[72:75]\n\t"
      "ds_read_b64_tr_b16 v[24:25], %[b00] offset:33024\n\t"
      "v_mfma_f32_16x16x32_bf16 v[88:91], v[48:51], v[60:63], v[88:91]\n\t"
      "ds_read_b64_tr_b16 v[26:27], %[b00] offset:37120\n\t"
      "s_waitcnt lgkmcnt(8)\n\t"
      "v_mfma_f32_16x16x32_bf16 v[76:79], v[52:55], v[56:59], v[76:79]\n\t"
      "ds_read_b64_tr_b16 v[28:29], %[b10] offset:33024\n\t"
      "ds_read_b64_tr_b16 v[30:31], %[b10] offset:37120\n\t"
      "v_mfma_f32_16x16x32_bf16 v[92:95], v[52:55], v[60:63], v[92:95]\n\t"
      "s_waitcnt lgkmcnt(7)\n\t"
      "v_mfma_f32_16x16x32_bf16 v[64:67], v[16:19], v[32:35], v[64:67]\n\t"
      "s_waitcnt lgkmcnt(5)\n\t"
      "v_mfma_f32_16x16x32_bf16 v[68:71], v[20:23], v[32:35], v[68:71]\n\t"
      "s_waitcnt vmcnt(6) lgkmcnt(0)\n\t"
      "s_barrier\n\t"
      "s_cmp_lg_u32 %[wid], 0\n\t"
      "s_cbranch_scc1 .Ls1%=\n\t"
      "s_mov_b64 exec, 1\n\t"
      "v_mov_b32 v100, 1\n\t"
      "buffer_atomic_add v100, off, %[rs], %[fa]\n\t"
      "s_mov_b64 exec, -1\n\t"
      ".Ls1%=:\n\t"
      "ds_read_b64_tr_b16 v[40:41], %[b00] offset:40960\n\t"
      "ds_read_b64_tr_b16 v[42:43], %[b00] offset:45056\n\t"
      "v_mfma_f32_16x16x32_bf16 v[80:83], v[16:19], v[36:39], v[80:83]\n\t"
      "ds_read_b128 v[56:59], %[a2] offset:8192\n\t"
      "ds_read_b64_tr_b16 v[44:45], %[b10] offset:40960\n\t"
      "v_mfma_f32_16x16x32_bf16 v[84:87], v[20:23], v[36:39], v[84:87]\n\t"
      "ds_read_b64_tr_b16 v[46:47], %[b10] offset:45056\n\t"
      "ds_read_b128 v[60:63], %[a2] offset:9216\n\t"
      "v_mfma_f32_16x16x32_bf16 v[72:75], v[24:27], v[32:35], v[72:75]\n\t"
      "ds_read_b64_tr_b16 v[48:49], %[b00] offset:41216\n\t"
      "ds_read_b64_tr_b16 v[50:51], %[b00] offset:45312\n\t"
      "v_mfma_f32_16x16x32_bf16 v[88:91], v[24:27], v[36:39], v[88:91]\n\t"
      "ds_read_b64_tr_b16 v[52:53], %[b10] offset:41216\n\t"
      "s_add_u32 %[t0], %[sa0], 13312\n\t"
      "s_add_u32 m0, %[da], 122880\n\t"
      "s_nop 0\n\t"
      "buffer_load_dwordx4 %[vl], %[rs], %[t0] offen sc1 lds\n\t"
      "v_mfma_f32_16x16x32_bf16 v[76:79], v[28:31], v[32:35], v[76:79]\n\t"
      "ds_read_b64_tr_b16 v[54:55], %[b10] offset:45312\n\t"
      "s_add_u32 %[t0], %[sa0], 14336\n\t"
      "s_add_u32 m0, %[da], 131072\n\t"
      "s_nop 0\n\t"
      "buffer_load_dwordx4 %[vl], %[rs], %[t0] offen sc1 lds\n\t"
      "v_mfma_f32_16x16x32_bf16 v[92:95], v[28:31], v[36:39], v[92:95]\n\t"
      "s_add_u32 %[t0], %[sw], 425984\n\t"
      "s_add_u32 m0, %[da], 24576\n\t"
      "s_nop 0\n\t"
      "buffer_load_dwordx4 %[vw], %[rw], %[t0] offen lds\n\t"
      "s_waitcnt lgkmcnt(7)\n\t"
      "v_mfma_f32_16x16x32_bf16 v[64:67], v[40:43], v[56:59], v[64:67]\n\t"
      "ds_read_b64_tr_b16 v[16:17], %[b00] offset:49152\n\t"
      "ds_read_b64_tr_b16 v[18:19], %[b00] offset:53248\n\t"
      "s_add_u32 %[t0], %[sw], 458752\n\t"
      "s_add_u32 m0, %[da], 32768\n\t"
      "s_nop 0\n\t"
      "buffer_load_dwordx4 %[vw], %[rw], %[t0] offen lds\n\t"
      "s_waitcnt lgkmcnt(7)\n\t"
      "v_mfma_f32_16x16x32_bf16 v[68:71], v[44:47], v[56:59], v[68:71]\n\t"
      "ds_read_b128 v[32:35], %[a2] offset:16384\n\t"
      "s_waitcnt lgkmcnt(7)\n\t"
      "v_mfma_f32_16x16x32_bf16 v[80:83], v[40:43], v[60:63], v[80:83]\n\t"
      "ds_read_b64_tr_b16 v[20:21], %[b10] offset:49152\n\t"
      "v_mfma_f32_16x16x32_bf16 v[84:87], v[44:47], v[60:63], v[84:87]\n\t"
      "ds_read_b64_tr_b16 v[22:23], %[b10] offset:53248\n\t"
      "ds_read_b128 v[36:39], %[a2] offset:17408\n\t"
      "s_waitcnt lgkmcnt(8)\n\t"
      "v_mfma_f32_16x16x32_bf16 v[72:75], v[48:51], v[56:59], v[72:75]\n\t"
      "ds_read_b64_tr_b16 v[24:25], %[b00] offset:49408\n\t"
      "v_mfma_f32_16x16x32_bf16 v[88:91], v[48:51], v[60:63], v[88:91]\n\t"
      "ds_read_b64_tr_b16 v[26:27], %[b00] offset:53504\n\t"
      "s_waitcnt lgkmcnt(8)\n\t"
      "v_mfma_f32_16x16x32_bf16 v[76:79], v[52:55], v[56:59], v[76:79]\n\t"
      "ds_read_b64_tr_b16 v[28:29], %[b10] offset:49408\n\t"
      "ds_read_b64_tr_b16 v[30:31], %[b10] offset:53504\n\t"
      "v_mfma_f32_16x16x32_bf16 v[92:95], v[52:55], v[60:63], v[92:95]\n\t"
      "s_waitcnt lgkmcnt(7)\n\t"
      "v_mfma_f32_16x16x32_bf16 v[64:67], v[16:19], v[32:35], v[64:67]\n\t"
      "s_waitcnt lgkmcnt(5)\n\t"
      "v_mfma_f32_16x16x32_bf16 v[68:71], v[20:23], v[32:35], v[68:71]\n\t"
      "s_waitcnt vmcnt(15) lgkmcnt(0)\n\t"
      "s_barrier\n\t"
      "ds_read_b64_tr_b16 v[40:41], %[b00] offset:57344\n\t"
      "ds_read_b64_tr_b16 v[42:43], %[b00] offset:61440\n\t"
      "v_mfma_f32_16x16x32_bf16 v[80:83], v[16:19], v[36:39], v[80:83]\n\t"
      "ds_read_b128 v[56:59], %[a2] offset:24576\n\t"
      "ds_read_b64_tr_b16 v[44:45], %[b10] offset:57344\n\t"
      "v_mfma_f32_16x16x32_bf16 v[84:87], v[20:23], v[36:39], v[84:87]\n\t"
      "ds_read_b64_tr_b16 v[46:47], %[b10] offset:61440\n\t"
      "ds_read_b128 v[60:63], %[a2] offset:25600\n\t"
      "v_mfma_f32_16x16x32_bf16 v[72:75], v[24:27], v[32:35], v[72:75]\n\t"
      "ds_read_b64_tr_b16 v[48:49], %[b00] offset:57600\n\t"
      "ds_read_b64_tr_b16 v[50:51], %[b00] offset:61696\n\t"
      "v_mfma_f32_16x16x32_bf16 v[88:91], v[24:27], v[36:39], v[88:91]\n\t"
      "ds_read_b64_tr_b16 v[52:53], %[b10] offset:57600\n\t"
      "s_add_u32 %[t0], %[sa0], 15360\n\t"
      "s_add_u32 m0, %[da], 139264\n\t"
      "s_nop 0\n\t"
      "buffer_load_dwordx4 %[vl], %[rs], %[t0] offen sc1 lds\n\t"
      "v_mfma_f32_16x16x32_bf16 v[76:79], v[28:31], v[32:35], v[76:79]\n\t"
      "ds_read_b64_tr_b16 v[54:55], %[b10] offset:61696\n\t"
      "s_add_u32 %[t0], %[sw], 491520\n\t"
      "s_add_u32 m0, %[da], 40960\n\t"
      "s_nop 0\n\t"
      "buffer_load_dwordx4 %[vw], %[rw], %[t0] offen lds\n\t"
      "v_mfma_f32_16x16x32_bf16 v[92:95], v[28:31], v[36:39], v[92:95]\n\t"
      "s_waitcnt lgkmcnt(7)\n\t"
      "v_mfma_f32_16x16x32_bf16 v[64:67], v[40:43], v[56:59], v[64:67]\n\t"
      "ds_read_b64_tr_b16 v[16:17], %[b01] offset:0\n\t"
      "ds_read_b64_tr_b16 v[18:19], %[b01] offset:4096\n\t"
      "s_waitcnt lgkmcnt(7)\n\t"
      "v_mfma_f32_16x16x32_bf16 v[68:71], v[44:47], v[56:59], v[68:71]\n\t"
      "ds_read_b128 v[32:35], %[a1] offset:16384\n\t"
      "s_waitcnt lgkmcnt(7)\n\t"
      "v_mfma_f32_16x16x32_bf16 v[80:83], v[40:43], v[60:63], v[80:83]\n\t"
      "ds_read_b64_tr_b16 v[20:21], %[b11] offset:0\n\t"
      "v_mfma_f32_16x16x32_bf16 v[84:87], v[44:47], v[60:63], v[84:87]\n\t"
      "ds_read_b64_tr_b16 v[22:23], %[b11] offset:4096\n\t"
      "ds_read_b128 v[36:39], %[a1] offset:17408\n\t"
      "s_waitcnt lgkmcnt(8)\n\t"
      "v_mfma_f32_16x16x32_bf16 v[72:75], v[48:51], v[56:59], v[72:75]\n\t"
      "ds_read_b64_tr_b16 v[24:25], %[b01] offset:256\n\t"
      "v_mfma_f32_16x16x32_bf16 v[88:91], v[48:51], v[60:63], v[88:91]\n\t"
      "ds_read_b64_tr_b16 v[26:27], %[b01] offset:4352\n\t"
      "s_waitcnt lgkmcnt(8)\n\t"
      "v_mfma_f32_16x16x32_bf16 v[76:79], v[52:55], v[56:59], v[76:79]\n\t"
      "ds_read_b64_tr_b16 v[28:29], %[b11] offset:256\n\t"
      "ds_read_b64_tr_b16 v[30:31], %[b11] offset:4352\n\t"
      "v_mfma_f32_16x16x32_bf16 v[92:95], v[52:55], v[60:63], v[92:95]\n\t"
      "s_waitcnt lgkmcnt(7)\n\t"
      "v_mfma_f32_16x16x32_bf16 v[64:67], v[16:19], v[32:35], v[64:67]\n\t"
      "s_waitcnt lgkmcnt(5)\n\t"
      "v_mfma_f32_16x16x32_bf16 v[68:71], v[20:23], v[32:35], v[68:71]\n\t"
      "s_waitcnt vmcnt(10) lgkmcnt(0)\n\t"
      "s_barrier\n\t"
      "ds_read_b64_tr_b16 v[40:41], %[b01] offset:8192\n\t"
      "ds_read_b64_tr_b16 v[42:43], %[b01] offset:12288\n\t"
      "v_mfma_f32_16x16x32_bf16 v[80:83], v[16:19], v[36:39], v[80:83]\n\t"
      "ds_read_b128 v[56:59], %[a1] offset:24576\n\t"
      "ds_read_b64_tr_b16 v[44:45], %[b11] offset:8192\n\t"
      "v_mfma_f32_16x16x32_bf16 v[84:87], v[20:23], v[36:39], v[84:87]\n\t"
      "ds_read_b64_tr_b16 v[46:47], %[b11] offset:12288\n\t"
      "ds_read_b128 v[60:63], %[a1] offset:25600\n\t"
      "v_mfma_f32_16x16x32_bf16 v[72:75], v[24:27], v[32:35], v[72:75]\n\t"
      "ds_read_b64_tr_b16 v[48:49], %[b01] offset:8448\n\t"
      "ds_read_b64_tr_b16 v[50:51], %[b01] offset:12544\n\t"
      "v_mfma_f32_16x16x32_bf16 v[88:91], v[24:27], v[36:39], v[88:91]\n\t"
      "ds_read_b64_tr_b16 v[52:53], %[b11] offset:8448\n\t"
      "v_mfma_f32_16x16x32_bf16 v[76:79], v[28:31], v[32:35], v[76:79]\n\t"
      "ds_read_b64_tr_b16 v[54:55], %[b11] offset:12544\n\t"
      "v_mfma_f32_16x16x32_bf16 v[92:95], v[28:31], v[36:39], v[92:95]\n\t"
      "s_waitcnt lgkmcnt(7)\n\t"
      "v_mfma_f32_16x16x32_bf16 v[64:67], v[40:43], v[56:59], v[64:67]\n\t"
      "ds_read_b64_tr_b16 v[16:17], %[b00] offset:0\n\t"
      "ds_read_b64_tr_b16 v[18:19], %[b00] offset:4096\n\t"
      "s_waitcnt lgkmcnt(7)\n\t"
      "v_mfma_f32_16x16x32_bf16 v[68:71], v[44:47], v[56:59], v[68:71]\n\t"
      "ds_read_b128 v[32:35], %[a1] offset:32768\n\t"
      "s_waitcnt lgkmcnt(7)\n\t"
      "v_mfma_f32_16x16x32_bf16 v[80:83], v[40:43], v[60:63], v[80:83]\n\t"
      "ds_read_b64_tr_b16 v[20:21], %[b10] offset:0\n\t"
      "buffer_load_dword v102, off, %[rs], %[fw] sc1\n\t"
      "buffer_load_dword v101, off, %[rs], %[fa] sc1\n\t"
      "v_mfma_f32_16x16x32_bf16 v[84:87], v[44:47], v[60:63], v[84:87]\n\t"
      "ds_read_b64_tr_b16 v[22:23], %[b10] offset:4096\n\t"
      "ds_read_b128 v[36:39], %[a1] offset:33792\n\t"
      "s_waitcnt lgkmcnt(8)\n\t"
      "v_mfma_f32_16x16x32_bf16 v[72:75], v[48:51], v[56:59], v[72:75]\n\t"
      "ds_read_b64_tr_b16 v[24:25], %[b00] offset:256\n\t"
      "v_mfma_f32_16x16x32_bf16 v[88:91], v[48:51], v[60:63], v[88:91]\n\t"
      "ds_read_b64_tr_b16 v[26:27], %[b00] offset:4352\n\t"
      "s_waitcnt lgkmcnt(8)\n\t"
      "v_mfma_f32_16x16x32_bf16 v[76:79], v[52:55], v[56:59], v[76:79]\n\t"
      "ds_read_b64_tr_b16 v[28:29], %[b10] offset:256\n\t"
      "ds_read_b64_tr_b16 v[30:31], %[b10] offset:4352\n\t"
      "v_mfma_f32_16x16x32_bf16 v[92:95], v[52:55], v[60:63], v[92:95]\n\t"
      "s_waitcnt lgkmcnt(7)\n\t"
      "v_mfma_f32_16x16x32_bf16 v[64:67], v[16:19], v[32:35], v[64:67]\n\t"
      "s_waitcnt lgkmcnt(5)\n\t"
      "v_mfma_f32_16x16x32_bf16 v[68:71], v[20:23], v[32:35], v[68:71]\n\t"
      "s_waitcnt vmcnt(0) lgkmcnt(0)\n\t"
      "v_readfirstlane_b32 %[t0], v101\n\t"
      "s_cmp_lt_u32 %[t0], 4\n\t"
      "s_cbranch_scc1 .Lw%=\n\t"
      "v_readfirstlane_b32 %[t0], v102\n\t"
      "s_cmp_lt_u32 %[t0], 16\n\t"
      "s_cbranch_scc1 .Lw%=\n\t"
      ".Lr%=:\n\t"
      "s_barrier\n\t"
      "ds_read_b64_tr_b16 v[40:41], %[b00] offset:8192\n\t"
      "ds_read_b64_tr_b16 v[42:43], %[b00] offset:12288\n\t"
      "s_add_u32 %[t0], %[sa1], 15360\n\t"
      "s_add_u32 m0, %[da], 147456\n\t"
      "s_nop 0\n\t"
      "buffer_load_dwordx4 %[vl], %[rs], %[t0] offen sc1 lds\n\t"
      "v_mfma_f32_16x16x32_bf16 v[80:83], v[16:19], v[36:39], v[80:83]\n\t"
      "ds_read_b128 v[56:59], %[a1] offset:40960\n\t"
      "ds_read_b64_tr_b16 v[44:45], %[b10] offset:8192\n\t"
      "v_mfma_f32_16x16x32_bf16 v[84:87], v[20:23], v[36:39], v[84:87]\n\t"
      "ds_read_b64_tr_b16 v[46:47], %[b10] offset:12288\n\t"
      "ds_read_b128 v[60:63], %[a1] offset:41984\n\t"
      "v_mfma_f32_16x16x32_bf16 v[72:75], v[24:27], v[32:35], v[72:75]\n\t"
      "ds_read_b64_tr_b16 v[48:49], %[b00] offset:8448\n\t"
      "ds_read_b64_tr_b16 v[50:51], %[b00] offset:12544\n\t"
      "v_mfma_f32_16x16x32_bf16 v[88:91], v[24:27], v[36:39], v[88:91]\n\t"
      "ds_read_b64_tr_b16 v[52:53], %[b10] offset:8448\n\t"
      "s_add_u32 %[t0], %[sa1], 14336\n\t"
      "s_add_u32 m0, %[da], 155648\n\t"
      "s_nop 0\n\t"
      "buffer_load_dwordx4 %[vl], %[rs], %[t0] offen sc1 lds\n\t"
      "v_mfma_f32_16x16x32_bf16 v[76:79], v[28:31], v[32:35], v[76:79]\n\t"
      "ds_read_b64_tr_b16 v[54:55], %[b10] offset:12544\n\t"
      "s_add_u32 %[t0], %[sa1], 13312\n\t"
      "s_add_u32 m0, %[da], 81920\n\t"
      "s_nop 0\n\t"
      "buffer_load_dwordx4 %[vl], %[rs], %[t0] offen sc1 lds\n\t"
      "v_mfma_f32_16x16x32_bf16 v[92:95], v[28:31], v[36:39], v[92:95]\n\t"
      "s_add_u32 %[t0], %[sa1], 12288\n\t"
      "s_add_u32 m0, %[da], 90112\n\t"
      "s_nop 0\n\t"
      "buffer_load_dwordx4 %[vl], %[rs], %[t0] offen sc1 lds\n\t"
      "s_waitcnt lgkmcnt(7)\n\t"
      "v_mfma_f32_16x16x32_bf16 v[64:67], v[40:43], v[56:59], v[64:67]\n\t"
      "ds_read_b64_tr_b16 v[16:17], %[b00] offset:16384\n\t"
      "ds_read_b64_tr_b16 v[18:19], %[b00] offset:20480\n\t"
      "s_add_u32 %[t0], %[sa1], 11264\n\t"
      "s_add_u32 m0, %[da], 98304\n\t"
      "s_nop 0\n\t"
      "buffer_load_dwordx4 %[vl], %[rs], %[t0] offen sc1 lds\n\t"
      "s_waitcnt lgkmcnt(7)\n\t"
      "v_mfma_f32_16x16x32_bf16 v[68:71], v[44:47], v[56:59], v[68:71]\n\t"
      "ds_read_b128 v[32:35], %[a1] offset:49152\n\t"
      "s_waitcnt lgkmcnt(7)\n\t"
      "v_mfma_f32_16x16x32_bf16 v[80:83], v[40:43], v[60:63], v[80:83]\n\t"
      "ds_read_b64_tr_b16 v[20:21], %[b10] offset:16384\n\t"
      "v_mfma_f32_16x16x32_bf16 v[84:87], v[44:47], v[60:63], v[84:87]\n\t"
      "ds_read_b64_tr_b16 v[22:23], %[b10] offset:20480\n\t"
      "ds_read_b128 v[36:39], %[a1] offset:50176\n\t"
      "s_waitcnt lgkmcnt(8)\n\t"
      "v_mfma_f32_16x16x32_bf16 v[72:75], v[48:51], v[56:59], v[72:75]\n\t"
      "ds_read_b64_tr_b16 v[24:25], %[b00] offset:16640\n\t"
      "v_mfma_f32_16x16x32_bf16 v[88:91], v[48:51], v[60:63], v[88:91]\n\t"
      "ds_read_b64_tr_b16 v[26:27], %[b00] offset:20736\n\t"
      "s_waitcnt lgkmcnt(8)\n\t"
      "v_mfma_f32_16x16x32_bf16 v[76:79], v[52:55], v[56:59], v[76:79]\n\t"
      "ds_read_b64_tr_b16 v[28:29], %[b10] offset:16640\n\t"
      "ds_read_b64_tr_b16 v[30:31], %[b10] offset:20736\n\t"
      "v_mfma_f32_16x16x32_bf16 v[92:95], v[52:55], v[60:63], v[92:95]\n\t"
      "s_waitcnt lgkmcnt(7)\n\t"
      "v_mfma_f32_16x16x32_bf16 v[64:67], v[16:19], v[32:35], v[64:67]\n\t"
      "s_waitcnt lgkmcnt(5)\n\t"
      "v_mfma_f32_16x16x32_bf16 v[68:71], v[20:23], v[32:35], v[68:71]\n\t"
      "s_waitcnt vmcnt(9) lgkmcnt(0)\n\t"
      "s_barrier\n\t"
      "ds_read_b64_tr_b16 v[40:41], %[b00] offset:24576\n\t"
      "ds_read_b64_tr_b16 v[42:43], %[b00] offset:28672\n\t"
      "v_mfma_f32_16x16x32_bf16 v[80:83], v[16:19], v[36:39], v[80:83]\n\t"
      "ds_read_b128 v[56:59], %[a1] offset:57344\n\t"
      "ds_read_b64_tr_b16 v[44:45], %[b10] offset:24576\n\t"
      "v_mfma_f32_16x16x32_bf16 v[84:87], v[20:23], v[36:39], v[84:87]\n\t"
      "ds_read_b64_tr_b16 v[46:47], %[b10] offset:28672\n\t"
      "ds_read_b128 v[60:63], %[a1] offset:58368\n\t"
      "v_mfma_f32_16x16x32_bf16 v[72:75], v[24:27], v[32:35], v[72:75]\n\t"
      "ds_read_b64_tr_b16 v[48:49], %[b00] offset:24832\n\t"
      "ds_read_b64_tr_b16 v[50:51], %[b00] offset:28928\n\t"
      "v_mfma_f32_16x16x32_bf16 v[88:91], v[24:27], v[36:39], v[88:91]\n\t"
      "ds_read_b64_tr_b16 v[52:53], %[b10] offset:24832\n\t"
      "s_add_u32 %[t0], %[sa1], 10240\n\t"
      "s_add_u32 m0, %[da], 106496\n\t"
      "s_nop 0\n\t"
      "buffer_load_dwordx4 %[vl], %[rs], %[t0] offen sc1 lds\n\t"
      "v_mfma_f32_16x16x32_bf16 v[76:79], v[28:31], v[32:35], v[76:79]\n\t"
      "ds_read_b64_tr_b16 v[54:55], %[b10] offset:28928\n\t"
      "s_add_u32 %[t0], %[sa1], 9216\n\t"
      "s_add_u32 m0, %[da], 114688\n\t"
      "s_nop 0\n\t"
      "buffer_load_dwordx4 %[vl], %[rs], %[t0] offen sc1 lds\n\t"
      "v_mfma_f32_16x16x32_bf16 v[92:95], v[28:31], v[36:39], v[92:95]\n\t"
      "s_waitcnt lgkmcnt(7)\n\t"
      "v_mfma_f32_16x16x32_bf16 v[64:67], v[40:43], v[56:59], v[64:67]\n\t"
      "ds_read_b64_tr_b16 v[16:17], %[b00] offset:32768\n\t"
      "ds_read_b64_tr_b16 v[18:19], %[b00] offset:36864\n\t"
      "s_waitcnt lgkmcnt(7)\n\t"
      "v_mfma_f32_16x16x32_bf16 v[68:71], v[44:47], v[56:59], v[68:71]\n\t"
      "ds_read_b128 v[32:35], %[a2] offset:0\n\t"
      "s_waitcnt lgkmcnt(7)\n\t"
      "v_mfma_f32_16x16x32_bf16 v[80:83], v[40:43], v[60:63], v[80:83]\n\t"
      "ds_read_b64_tr_b16 v[20:21], %[b10] offset:32768\n\t"
      "v_mfma_f32_16x16x32_bf16 v[84:87], v[44:47], v[60:63], v[84:87]\n\t"
      "ds_read_b64_tr_b16 v[22:23], %[b10] offset:36864\n\t"
      "ds_read_b128 v[36:39], %[a2] offset:1024\n\t"
      "s_waitcnt lgkmcnt(8)\n\t"
      "v_mfma_f32_16x16x32_bf16 v[72:75], v[48:51], v[56:59], v[72:75]\n\t"
      "ds_read_b64_tr_b16 v[24:25], %[b00] offset:33024\n\t"
      "v_mfma_f32_16x16x32_bf16 v[88:91], v[48:51], v[60:63], v[88:91]\n\t"
      "ds_read_b64_tr_b16 v[26:27], %[b00] offset:37120\n\t"
      "s_waitcnt lgkmcnt(8)\n\t"
      "v_mfma_f32_16x16x32_bf16 v[76:79], v[52:55], v[56:59], v[76:79]\n\t"
      "ds_read_b64_tr_b16 v[28:29], %[b10] offset:33024\n\t"
      "ds_read_b64_tr_b16 v[30:31], %[b10] offset:37120\n\t"
      "v_mfma_f32_16x16x32_bf16 v[92:95], v[52:55], v[60:63], v[92:95]\n\t"
      "s_waitcnt lgkmcnt(7)\n\t"
      "v_mfma_f32_16x16x32_bf16 v[64:67], v[16:19], v[32:35], v[64:67]\n\t"
      "s_waitcnt lgkmcnt(5)\n\t"
      "v_mfma_f32_16x16x32_bf16 v[68:71], v[20:23], v[32:35], v[68:71]\n\t"
      "s_waitcnt vmcnt(6) lgkmcnt(0)\n\t"
      "s_barrier\n\t"
      "ds_read_b64_tr_b16 v[40:41], %[b00] offset:40960\n\t"
      "ds_read_b64_tr_b16 v[42:43], %[b00] offset:45056\n\t"
      "v_mfma_f32_16x16x32_bf16 v[80:83], v[16:19], v[36:39], v[80:83]\n\t"
      "ds_read_b128 v[56:59], %[a2] offset:8192\n\t"
      "ds_read_b64_tr_b16 v[44:45], %[b10] offset:40960\n\t"
      "v_mfma_f32_16x16x32_bf16 v[84:87], v[20:23], v[36:39], v[84:87]\n\t"
      "ds_read_b64_tr_b16 v[46:47], %[b10] offset:45056\n\t"
      "ds_read_b128 v[60:63], %[a2] offset:9216\n\t"
      "v_mfma_f32_16x16x32_bf16 v[72:75], v[24:27], v[32:35], v[72:75]\n\t"
      "ds_read_b64_tr_b16 v[48:49], %[b00] offset:41216\n\t"
      "ds_read_b64_tr_b16 v[50:51], %[b00] offset:45312\n\t"
      "v_mfma_f32_16x16x32_bf16 v[88:91], v[24:27], v[36:39], v[88:91]\n\t"
      "ds_read_b64_tr_b16 v[52:53], %[b10] offset:41216\n\t"
      "s_add_u32 %[t0], %[sa1], 8192\n\t"
      "s_add_u32 m0, %[da], 122880\n\t"
      "s_nop 0\n\t"
      "buffer_load_dwordx4 %[vl], %[rs], %[t0] offen sc1 lds\n\t"
      "v_mfma_f32_16x16x32_bf16 v[76:79], v[28:31], v[32:35], v[76:79]\n\t"
      "ds_read_b64_tr_b16 v[54:55], %[b10] offset:45312\n\t"
      "s_add_u32 %[t0], %[sa1], 7168\n\t"
      "s_add_u32 m0, %[da], 131072\n\t"
      "s_nop 0\n\t"
      "buffer_load_dwordx4 %[vl], %[rs], %[t0] offen sc1 lds\n\t"
      "v_mfma_f32_16x16x32_bf16 v[92:95], v[28:31], v[36:39], v[92:95]\n\t"
      "s_waitcnt lgkmcnt(7)\n\t"
      "v_mfma_f32_16x16x32_bf16 v[64:67], v[40:43], v[56:59], v[64:67]\n\t"
      "ds_read_b64_tr_b16 v[16:17], %[b00] offset:40960\n\t"
      "ds_read_b64_tr_b16 v[18:19], %[b00] offset:45056\n\t"
      "s_waitcnt lgkmcnt(7)\n\t"
      "v_mfma_f32_16x16x32_bf16 v[68:71], v[44:47], v[56:59], v[68:71]\n\t"
      "ds_read_b128 v[32:35], %[a2] offset:16384\n\t"
      "s_waitcnt lgkmcnt(7)\n\t"
      "v_mfma_f32_16x16x32_bf16 v[80:83], v[40:43], v[60:63], v[80:83]\n\t"
      "ds_read_b64_tr_b16 v[20:21], %[b10] offset:40960\n\t"
      "v_mfma_f32_16x16x32_bf16 v[84:87], v[44:47], v[60:63], v[84:87]\n\t"
      "ds_read_b64_tr_b16 v[22:23], %[b10] offset:45056\n\t"
      "ds_read_b128 v[36:39], %[a2] offset:17408\n\t"
      "s_waitcnt lgkmcnt(8)\n\t"
      "v_mfma_f32_16x16x32_bf16 v[72:75], v[48:51], v[56:59], v[72:75]\n\t"
      "ds_read_b64_tr_b16 v[24:25], %[b00] offset:41216\n\t"
      "v_mfma_f32_16x16x32_bf16 v[88:91], v[48:51], v[60:63], v[88:91]\n\t"
      "ds_read_b64_tr_b16 v[26:27], %[b00] offset:45312\n\t"
      "s_waitcnt lgkmcnt(8)\n\t"
      "v_mfma_f32_16x16x32_bf16 v[76:79], v[52:55], v[56:59], v[76:79]\n\t"
      "ds_read_b64_tr_b16 v[28:29], %[b10] offset:41216\n\t"
      "ds_read_b64_tr_b16 v[30:31], %[b10] offset:45312\n\t"
      "v_mfma_f32_16x16x32_bf16 v[92:95], v[52:55], v[60:63], v[92:95]\n\t"
      "s_waitcnt lgkmcnt(7)\n\t"
      "v_mfma_f32_16x16x32_bf16 v[96:99], v[16:19], v[32:35], 0\n\t"
      "s_waitcnt lgkmcnt(5)\n\t"
      "v_mfma_f32_16x16x32_bf16 v[100:103], v[20:23], v[32:35], 0\n\t"
      "s_waitcnt vmcnt(6) lgkmcnt(0)\n\t"
      "s_barrier\n\t"
      "ds_read_b64_tr_b16 v[40:41], %[b00] offset:32768\n\t"
      "ds_read_b64_tr_b16 v[42:43], %[b00] offset:36864\n\t"
      "v_mfma_f32_16x16x32_bf16 v[112:115], v[16:19], v[36:39], 0\n\t"
      "ds_read_b128 v[56:59], %[a2] offset:24576\n\t"
      "ds_read_b64_tr_b16 v[44:45], %[b10] offset:32768\n\t"
      "v_mfma_f32_16x16x32_bf16 v[116:119], v[20:23], v[36:39], 0\n\t"
      "ds_read_b64_tr_b16 v[46:47], %[b10] offset:36864\n\t"
      "ds_read_b128 v[60:63], %[a2] offset:25600\n\t"
      "v_mfma_f32_16x16x32_bf16 v[104:107], v[24:27], v[32:35], 0\n\t"
      "ds_read_b64_tr_b16 v[48:49], %[b00] offset:33024\n\t"
      "ds_read_b64_tr_b16 v[50:51], %[b00] offset:37120\n\t"
      "v_mfma_f32_16x16x32_bf16 v[120:123], v[24:27], v[36:39], 0\n\t"
      "ds_read_b64_tr_b16 v[52:53], %[b10] offset:33024\n\t"
      "s_add_u32 %[t0], %[sa1], 6144\n\t"
      "s_add_u32 m0, %[da], 139264\n\t"
      "s_nop 0\n\t"
      "buffer_load_dwordx4 %[vl], %[rs], %[t0] offen sc1 lds\n\t"
      "v_mfma_f32_16x16x32_bf16 v[108:111], v[28:31], v[32:35], 0\n\t"
      "ds_read_b64_tr_b16 v[54:55], %[b10] offset:37120\n\t"
      "s_add_u32 %[t0], %[sa1], 5120\n\t"
      "s_add_u32 m0, %[da], 147456\n\t"
      "s_nop 0\n\t"
      "buffer_load_dwordx4 %[vl], %[rs], %[t0] offen sc1 lds\n\t"
      "v_mfma_f32_16x16x32_bf16 v[124:127], v[28:31], v[36:39], 0\n\t"
      "s_add_u32 %[t0], %[sw], 163840\n\t"
      "s_add_u32 m0, %[da], 40960\n\t"
      "s_nop 0\n\t"
      "buffer_load_dwordx4 %[vw], %[rw], %[t0] offen lds\n\t"
      "s_waitcnt lgkmcnt(7)\n\t"
      "v_mfma_f32_16x16x32_bf16 v[96:99], v[40:43], v[56:59], v[96:99]\n\t"
      "ds_read_b64_tr_b16 v[16:17], %[b00] offset:24576\n\t"
      "ds_read_b64_tr_b16 v[18:19], %[b00] offset:28672\n\t"
      "v_cvt_pk_bf16_f32 v64, v64, v65\n\t"
      "v_cvt_pk_bf16_f32 v65, v66, v67\n\t"
      "s_waitcnt lgkmcnt(7)\n\t"
      "v_mfma_f32_16x16x32_bf16 v[100:103], v[44:47], v[56:59], v[100:103]\n\t"
      "ds_read_b128 v[32:35], %[a1] offset:16384\n\t"
      "v_cvt_pk_bf16_f32 v66, v68, v69\n\t"
      "v_cvt_pk_bf16_f32 v67, v70, v71\n\t"
      "s_waitcnt lgkmcnt(7)\n\t"
      "v_mfma_f32_16x16x32_bf16 v[112:115], v[40:43], v[60:63], v[112:115]\n\t"
      "ds_read_b64_tr_b16 v[20:21], %[b10] offset:24576\n\t"
      "s_add_u32 %[t0], %[so0], 0\n\t"
      "buffer_store_dwordx4 v[64:67], %[vl], %[rs], %[t0] offen sc1\n\t"
      "v_mfma_f32_16x16x32_bf16 v[116:119], v[44:47], v[60:63], v[116:119]\n\t"
      "ds_read_b64_tr_b16 v[22:23], %[b10] offset:28672\n\t"
      "ds_read_b128 v[36:39], %[a1] offset:17408\n\t"
      "s_waitcnt lgkmcnt(8)\n\t"
      "v_mfma_f32_16x16x32_bf16 v[104:107], v[48:51], v[56:59], v[104:107]\n\t"
      "ds_read_b64_tr_b16 v[24:25], %[b00] offset:24832\n\t"
      "v_cvt_pk_bf16_f32 v72, v72, v73\n\t"
      "v_cvt_pk_bf16_f32 v73, v74, v75\n\t"
      "v_mfma_f32_16x16x32_bf16 v[120:123], v[48:51], v[60:63], v[120:123]\n\t"
      "ds_read_b64_tr_b16 v[26:27], %[b00] offset:28928\n\t"
      "v_cvt_pk_bf16_f32 v74, v76, v77\n\t"
      "v_cvt_pk_bf16_f32 v75, v78, v79\n\t"
      "s_waitcnt lgkmcnt(8)\n\t"
      "v_mfma_f32_16x16x32_bf16 v[108:111], v[52:55], v[56:59], v[108:111]\n\t"
      "ds_read_b64_tr_b16 v[28:29], %[b10] offset:24832\n\t"
      "ds_read_b64_tr_b16 v[30:31], %[b10] offset:28928\n\t"
      "s_add_u32 %[t0], %[so0], 1024\n\t"
      "buffer_store_dwordx4 v[72:75], %[vl], %[rs], %[t0] offen sc1\n\t"
      "v_mfma_f32_16x16x32_bf16 v[124:127], v[52:55], v[60:63], v[124:127]\n\t"
      "s_waitcnt lgkmcnt(7)\n\t"
      "v_mfma_f32_16x16x32_bf16 v[96:99], v[16:19], v[32:35], v[96:99]\n\t"
      "v_cvt_pk_bf16_f32 v80, v80, v81\n\t"
      "v_cvt_pk_bf16_f32 v81, v82, v83\n\t"
      "s_waitcnt lgkmcnt(5)\n\t"
      "v_mfma_f32_16x16x32_bf16 v[100:103], v[20:23], v[32:35], v[100:103]\n\t"
      "s_waitcnt vmcnt(9) lgkmcnt(0)\n\t"
      "s_barrier\n\t"
      "ds_read_b64_tr_b16 v[40:41], %[b00] offset:16384\n\t"
      "ds_read_b64_tr_b16 v[42:43], %[b00] offset:20480\n\t"
      "v_cvt_pk_bf16_f32 v82, v84, v85\n\t"
      "v_cvt_pk_bf16_f32 v83, v86, v87\n\t"
      "v_mfma_f32_16x16x32_bf16 v[112:115], v[16:19], v[36:39], v[112:115]\n\t"
      "ds_read_b128 v[56:59], %[a1] offset:24576\n\t"
      "ds_read_b64_tr_b16 v[44:45], %[b10] offset:16384\n\t"
      "s_add_u32 %[t0], %[so0], 16384\n\t"
      "buffer_store_dwordx4 v[80:83], %[vl], %[rs], %[t0] offen sc1\n\t"
      "v_mfma_f32_16x16x32_bf16 v[116:119], v[20:23], v[36:39], v[116:119]\n\t"
      "ds_read_b64_tr_b16 v[46:47], %[b10] offset:20480\n\t"
      "ds_read_b128 v[60:63], %[a1] offset:25600\n\t"
      "v_mfma_f32_16x16x32_bf16 v[104:107], v[24:27], v[32:35], v[104:107]\n\t"
      "ds_read_b64_tr_b16 v[48:49], %[b00] offset:16640\n\t"
      "ds_read_b64_tr_b16 v[50:51], %[b00] offset:20736\n\t"
      "v_cvt_pk_bf16_f32 v88, v88, v89\n\t"
      "v_cvt_pk_bf16_f32 v89, v90, v91\n\t"
      "v_mfma_f32_16x16x32_bf16 v[120:123], v[24:27], v[36:39], v[120:123]\n\t"
      "ds_read_b64_tr_b16 v[52:53], %[b10] offset:16640\n\t"
      "v_cvt_pk_bf16_f32 v90, v92, v93\n\t"
      "v_cvt_pk_bf16_f32 v91, v94, v95\n\t"
      "s_add_u32 %[t0], %[sa1], 4096\n\t"
      "s_add_u32 m0, %[da], 155648\n\t"
      "s_nop 0\n\t"
      "buffer_load_dwordx4 %[vl], %[rs], %[t0] offen sc1 lds\n\t"
      "v_mfma_f32_16x16x32_bf16 v[108:111], v[28:31], v[32:35], v[108:111]\n\t"
      "ds_read_b64_tr_b16 v[54:55], %[b10] offset:20736\n\t"
      "s_add_u32 %[t0], %[so0], 17408\n\t"
      "buffer_store_dwordx4 v[88:91], %[vl], %[rs], %[t0] offen sc1\n\t"
      "s_add_u32 %[t0], %[sa1], 3072\n\t"
      "s_add_u32 m0, %[da], 81920\n\t"
      "s_nop 0\n\t"
      "buffer_load_dwordx4 %[vl], %[rs], %[t0] offen sc1 lds\n\t"
      "v_mfma_f32_16x16x32_bf16 v[124:127], v[28:31], v[36:39], v[124:127]\n\t"
      "s_add_u32 %[t0], %[sw], 131072\n\t"
      "s_add_u32 m0, %[da], 32768\n\t"
      "s_nop 0\n\t"
      "buffer_load_dwordx4 %[vw], %[rw], %[t0] offen lds\n\t"
      "s_waitcnt lgkmcnt(7)\n\t"
      "v_mfma_f32_16x16x32_bf16 v[96:99], v[40:43], v[56:59], v[96:99]\n\t"
      "ds_read_b64_tr_b16 v[16:17], %[b00] offset:8192\n\t"
      "ds_read_b64_tr_b16 v[18:19], %[b00] offset:12288\n\t"
      "s_add_u32 %[t0], %[sw], 98304\n\t"
      "s_add_u32 m0, %[da], 24576\n\t"
      "s_nop 0\n\t"
      "buffer_load_dwordx4 %[vw], %[rw], %[t0] offen lds\n\t"
      "s_waitcnt lgkmcnt(7)\n\t"
      "v_mfma_f32_16x16x32_bf16 v[100:103], v[44:47], v[56:59], v[100:103]\n\t"
      "ds_read_b128 v[32:35], %[a1] offset:32768\n\t"
      "s_waitcnt lgkmcnt(7)\n\t"
      "v_mfma_f32_16x16x32_bf16 v[112:115], v[40:43], v[60:63], v[112:115]\n\t"
      "ds_read_b64_tr_b16 v[20:21], %[b10] offset:8192\n\t"
      "v_mfma_f32_16x16x32_bf16 v[116:119], v[44:47], v[60:63], v[116:119]\n\t"
      "ds_read_b64_tr_b16 v[22:23], %[b10] offset:12288\n\t"
      "ds_read_b128 v[36:39], %[a1] offset:33792\n\t"
      "s_waitcnt lgkmcnt(8)\n\t"
      "v_mfma_f32_16x16x32_bf16 v[104:107], v[48:51], v[56:59], v[104:107]\n\t"
      "ds_read_b64_tr_b16 v[24:25], %[b00] offset:8448\n\t"
      "v_mfma_f32_16x16x32_bf16 v[120:123], v[48:51], v[60:63], v[120:123]\n\t"
      "ds_read_b64_tr_b16 v[26:27], %[b00] offset:12544\n\t"
      "s_waitcnt lgkmcnt(8)\n\t"
      "v_mfma_f32_16x16x32_bf16 v[108:111], v[52:55], v[56:59], v[108:111]\n\t"
      "ds_read_b64_tr_b16 v[28:29], %[b10] offset:8448\n\t"
      "ds_read_b64_tr_b16 v[30:31], %[b10] offset:12544\n\t"
      "v_mfma_f32_16x16x32_bf16 v[124:127], v[52:55], v[60:63], v[124:127]\n\t"
      "s_waitcnt lgkmcnt(7)\n\t"
      "v_mfma_f32_16x16x32_bf16 v[96:99], v[16:19], v[32:35], v[96:99]\n\t"
      "s_waitcnt lgkmcnt(5)\n\t"
      "v_mfma_f32_16x16x32_bf16 v[100:103], v[20:23], v[32:35], v[100:103]\n\t"
      "s_waitcnt vmcnt(13) lgkmcnt(0)\n\t"
      "s_barrier\n\t"
      "ds_read_b64_tr_b16 v[40:41], %[b00] offset:0\n\t"
      "ds_read_b64_tr_b16 v[42:43], %[b00] offset:4096\n\t"
      "v_mfma_f32_16x16x32_bf16 v[112:115], v[16:19], v[36:39], v[112:115]\n\t"
      "ds_read_b128 v[56:59], %[a1] offset:40960\n\t"
      "ds_read_b64_tr_b16 v[44:45], %[b10] offset:0\n\t"
      "v_mfma_f32_16x16x32_bf16 v[116:119], v[20:23], v[36:39], v[116:119]\n\t"
      "ds_read_b64_tr_b16 v[46:47], %[b10] offset:4096\n\t"
      "ds_read_b128 v[60:63], %[a1] offset:41984\n\t"
      "v_mfma_f32_16x16x32_bf16 v[104:107], v[24:27], v[32:35], v[104:107]\n\t"
      "ds_read_b64_tr_b16 v[48:49], %[b00] offset:256\n\t"
      "ds_read_b64_tr_b16 v[50:51], %[b00] offset:4352\n\t"
      "v_mfma_f32_16x16x32_bf16 v[120:123], v[24:27], v[36:39], v[120:123]\n\t"
      "ds_read_b64_tr_b16 v[52:53], %[b10] offset:256\n\t"
      "s_add_u32 %[t0], %[sa1], 2048\n\t"
      "s_add_u32 m0, %[da], 90112\n\t"
      "s_nop 0\n\t"
      "buffer_load_dwordx4 %[vl], %[rs], %[t0] offen sc1 lds\n\t"
      "v_mfma_f32_16x16x32_bf16 v[108:111], v[28:31], v[32:35], v[108:111]\n\t"
      "ds_read_b64_tr_b16 v[54:55], %[b10] offset:4352\n\t"
      "s_add_u32 %[t0], %[sa1], 1024\n\t"
      "s_add_u32 m0, %[da], 98304\n\t"
      "s_nop 0\n\t"
      "buffer_load_dwordx4 %[vl], %[rs], %[t0] offen sc1 lds\n\t"
      "v_mfma_f32_16x16x32_bf16 v[124:127], v[28:31], v[36:39], v[124:127]\n\t"
      "s_add_u32 %[t0], %[sw], 65536\n\t"
      "s_add_u32 m0, %[da], 16384\n\t"
      "s_nop 0\n\t"
      "buffer_load_dwordx4 %[vw], %[rw], %[t0] offen lds\n\t"
      "s_waitcnt lgkmcnt(7)\n\t"
      "v_mfma_f32_16x16x32_bf16 v[96:99], v[40:43], v[56:59], v[96:99]\n\t"
      "ds_read_b64_tr_b16 v[16:17], %[b01] offset:8192\n\t"
      "ds_read_b64_tr_b16 v[18:19], %[b01] offset:12288\n\t"
      "s_add_u32 %[t0], %[sw], 32768\n\t"
      "s_add_u32 m0, %[da], 8192\n\t"
      "s_nop 0\n\t"
      "buffer_load_dwordx4 %[vw], %[rw], %[t0] offen lds\n\t"
      "s_waitcnt lgkmcnt(7)\n\t"
      "v_mfma_f32_16x16x32_bf16 v[100:103], v[44:47], v[56:59], v[100:103]\n\t"
      "ds_read_b128 v[32:35], %[a1] offset:49152\n\t"
      "s_waitcnt lgkmcnt(7)\n\t"
      "v_mfma_f32_16x16x32_bf16 v[112:115], v[40:43], v[60:63], v[112:115]\n\t"
      "ds_read_b64_tr_b16 v[20:21], %[b11] offset:8192\n\t"
      "v_mfma_f32_16x16x32_bf16 v[116:119], v[44:47], v[60:63], v[116:119]\n\t"
      "ds_read_b64_tr_b16 v[22:23], %[b11] offset:12288\n\t"
      "ds_read_b128 v[36:39], %[a1] offset:50176\n\t"
      "s_waitcnt lgkmcnt(8)\n\t"
      "v_mfma_f32_16x16x32_bf16 v[104:107], v[48:51], v[56:59], v[104:107]\n\t"
      "ds_read_b64_tr_b16 v[24:25], %[b01] offset:8448\n\t"
      "v_mfma_f32_16x16x32_bf16 v[120:123], v[48:51], v[60:63], v[120:123]\n\t"
      "ds_read_b64_tr_b16 v[26:27], %[b01] offset:12544\n\t"
      "s_waitcnt lgkmcnt(8)\n\t"
      "v_mfma_f32_16x16x32_bf16 v[108:111], v[52:55], v[56:59], v[108:111]\n\t"
      "ds_read_b64_tr_b16 v[28:29], %[b11] offset:8448\n\t"
      "ds_read_b64_tr_b16 v[30:31], %[b11] offset:12544\n\t"
      "v_mfma_f32_16x16x32_bf16 v[124:127], v[52:55], v[60:63], v[124:127]\n\t"
      "s_waitcnt lgkmcnt(7)\n\t"
      "v_mfma_f32_16x16x32_bf16 v[96:99], v[16:19], v[32:35], v[96:99]\n\t"
      "s_waitcnt lgkmcnt(5)\n\t"
      "v_mfma_f32_16x16x32_bf16 v[100:103], v[20:23], v[32:35], v[100:103]\n\t"
      "s_waitcnt vmcnt(15) lgkmcnt(0)\n\t"
      "s_barrier\n\t"
      "ds_read_b64_tr_b16 v[40:41], %[b01] offset:0\n\t"
      "ds_read_b64_tr_b16 v[42:43], %[b01] offset:4096\n\t"
      "v_mfma_f32_16x16x32_bf16 v[112:115], v[16:19], v[36:39], v[112:115]\n\t"
      "ds_read_b128 v[56:59], %[a1] offset:57344\n\t"
      "ds_read_b64_tr_b16 v[44:45], %[b11] offset:0\n\t"
      "v_mfma_f32_16x16x32_bf16 v[116:119], v[20:23], v[36:39], v[116:119]\n\t"
      "ds_read_b64_tr_b16 v[46:47], %[b11] offset:4096\n\t"
      "ds_read_b128 v[60:63], %[a1] offset:58368\n\t"
      "v_mfma_f32_16x16x32_bf16 v[104:107], v[24:27], v[32:35], v[104:107]\n\t"
      "ds_read_b64_tr_b16 v[48:49], %[b01] offset:256\n\t"
      "ds_read_b64_tr_b16 v[50:51], %[b01] offset:4352\n\t"
      "v_mfma_f32_16x16x32_bf16 v[120:123], v[24:27], v[36:39], v[120:123]\n\t"
      "ds_read_b64_tr_b16 v[52:53], %[b11] offset:256\n\t"
      "s_add_u32 %[t0], %[sa1], 0\n\t"
      "s_add_u32 m0, %[da], 106496\n\t"
      "s_nop 0\n\t"
      "buffer_load_dwordx4 %[vl], %[rs], %[t0] offen sc1 lds\n\t"
      "v_mfma_f32_16x16x32_bf16 v[108:111], v[28:31], v[32:35], v[108:111]\n\t"
      "ds_read_b64_tr_b16 v[54:55], %[b11] offset:4352\n\t"
      "s_add_u32 %[t0], %[sw], 0\n\t"
      "s_add_u32 m0, %[da], 0\n\t"
      "s_nop 0\n\t"
      "buffer_load_dwordx4 %[vw], %[rw], %[t0] offen lds\n\t"
      "v_mfma_f32_16x16x32_bf16 v[124:127], v[28:31], v[36:39], v[124:127]\n\t"
      "s_waitcnt lgkmcnt(7)\n\t"
      "v_mfma_f32_16x16x32_bf16 v[96:99], v[40:43], v[56:59], v[96:99]\n\t"
      "ds_read_b64_tr_b16 v[16:17], %[b00] offset:57344\n\t"
      "ds_read_b64_tr_b16 v[18:19], %[b00] offset:61440\n\t"
      "s_waitcnt lgkmcnt(7)\n\t"
      "v_mfma_f32_16x16x32_bf16 v[100:103], v[44:47], v[56:59], v[100:103]\n\t"
      "ds_read_b128 v[32:35], %[a2] offset:0\n\t"
      "s_waitcnt lgkmcnt(7)\n\t"
      "v_mfma_f32_16x16x32_bf16 v[112:115], v[40:43], v[60:63], v[112:115]\n\t"
      "ds_read_b64_tr_b16 v[20:21], %[b10] offset:57344\n\t"
      "v_mfma_f32_16x16x32_bf16 v[116:119], v[44:47], v[60:63], v[116:119]\n\t"
      "ds_read_b64_tr_b16 v[22:23], %[b10] offset:61440\n\t"
      "ds_read_b128 v[36:39], %[a2] offset:1024\n\t"
      "s_waitcnt lgkmcnt(8)\n\t"
      "v_mfma_f32_16x16x32_bf16 v[104:107], v[48:51], v[56:59], v[104:107]\n\t"
      "ds_read_b64_tr_b16 v[24:25], %[b00] offset:57600\n\t"
      "v_mfma_f32_16x16x32_bf16 v[120:123], v[48:51], v[60:63], v[120:123]\n\t"
      "ds_read_b64_tr_b16 v[26:27], %[b00] offset:61696\n\t"
      "s_waitcnt lgkmcnt(8)\n\t"
      "v_mfma_f32_16x16x32_bf16 v[108:111], v[52:55], v[56:59], v[108:111]\n\t"
      "ds_read_b64_tr_b16 v[28:29], %[b10] offset:57600\n\t"
      "ds_read_b64_tr_b16 v[30:31], %[b10] offset:61696\n\t"
      "v_mfma_f32_16x16x32_bf16 v[124:127], v[52:55], v[60:63], v[124:127]\n\t"
      "s_waitcnt lgkmcnt(7)\n\t"
      "v_mfma_f32_16x16x32_bf16 v[96:99], v[16:19], v[32:35], v[96:99]\n\t"
      "s_waitcnt lgkmcnt(5)\n\t"
      "v_mfma_f32_16x16x32_bf16 v[100:103], v[20:23], v[32:35], v[100:103]\n\t"
      "s_waitcnt vmcnt(9) lgkmcnt(0)\n\t"
      "s_barrier\n\t"
      "s_cmp_lg_u32 %[wid], 0\n\t"
      "s_cbranch_scc1 .Ls0%=\n\t"
      "s_mov_b64 exec, 1\n\t"
      "v_mov_b32 v68, 1\n\t"
      "buffer_atomic_add v68, off, %[rs], %[f0]\n\t"
      "s_mov_b64 exec, -1\n\t"
      ".Ls0%=:\n\t"
      "ds_read_b64_tr_b16 v[40:41], %[b00] offset:49152\n\t"
      "ds_read_b64_tr_b16 v[42:43], %[b00] offset:53248\n\t"
      "v_mfma_f32_16x16x32_bf16 v[112:115], v[16:19], v[36:39], v[112:115]\n\t"
      "ds_read_b128 v[56:59], %[a2] offset:8192\n\t"
      "ds_read_b64_tr_b16 v[44:45], %[b10] offset:49152\n\t"
      "v_mfma_f32_16x16x32_bf16 v[116:119], v[20:23], v[36:39], v[116:119]\n\t"
      "ds_read_b64_tr_b16 v[46:47], %[b10] offset:53248\n\t"
      "ds_read_b128 v[60:63], %[a2] offset:9216\n\t"
      "v_mfma_f32_16x16x32_bf16 v[104:107], v[24:27], v[32:35], v[104:107]\n\t"
      "ds_read_b64_tr_b16 v[48:49], %[b00] offset:49408\n\t"
      "ds_read_b64_tr_b16 v[50:51], %[b00] offset:53504\n\t"
      "v_mfma_f32_16x16x32_bf16 v[120:123], v[24:27], v[36:39], v[120:123]\n\t"
      "ds_read_b64_tr_b16 v[52:53], %[b10] offset:49408\n\t"
      "v_mfma_f32_16x16x32_bf16 v[108:111], v[28:31], v[32:35], v[108:111]\n\t"
      "ds_read_b64_tr_b16 v[54:55], %[b10] offset:53504\n\t"
      "v_mfma_f32_16x16x32_bf16 v[124:127], v[28:31], v[36:39], v[124:127]\n\t"
      "s_waitcnt lgkmcnt(7)\n\t"
      "v_mfma_f32_16x16x32_bf16 v[96:99], v[40:43], v[56:59], v[96:99]\n\t"
      "ds_read_b64_tr_b16 v[16:17], %[b00] offset:40960\n\t"
      "ds_read_b64_tr_b16 v[18:19], %[b00] offset:45056\n\t"
      "s_waitcnt lgkmcnt(7)\n\t"
      "v_mfma_f32_16x16x32_bf16 v[100:103], v[44:47], v[56:59], v[100:103]\n\t"
      "ds_read_b128 v[32:35], %[a2] offset:16384\n\t"
      "s_waitcnt lgkmcnt(7)\n\t"
      "v_mfma_f32_16x16x32_bf16 v[112:115], v[40:43], v[60:63], v[112:115]\n\t"
      "ds_read_b64_tr_b16 v[20:21], %[b10] offset:40960\n\t"
      "v_mfma_f32_16x16x32_bf16 v[116:119], v[44:47], v[60:63], v[116:119]\n\t"
      "ds_read_b64_tr_b16 v[22:23], %[b10] offset:45056\n\t"
      "ds_read_b128 v[36:39], %[a2] offset:17408\n\t"
      "s_waitcnt lgkmcnt(8)\n\t"
      "v_mfma_f32_16x16x32_bf16 v[104:107], v[48:51], v[56:59], v[104:107]\n\t"
      "ds_read_b64_tr_b16 v[24:25], %[b00] offset:41216\n\t"
      "v_mfma_f32_16x16x32_bf16 v[120:123], v[48:51], v[60:63], v[120:123]\n\t"
      "ds_read_b64_tr_b16 v[26:27], %[b00] offset:45312\n\t"
      "s_waitcnt lgkmcnt(8)\n\t"
      "v_mfma_f32_16x16x32_bf16 v[108:111], v[52:55], v[56:59], v[108:111]\n\t"
      "ds_read_b64_tr_b16 v[28:29], %[b10] offset:41216\n\t"
      "ds_read_b64_tr_b16 v[30:31], %[b10] offset:45312\n\t"
      "v_mfma_f32_16x16x32_bf16 v[124:127], v[52:55], v[60:63], v[124:127]\n\t"
      "s_waitcnt lgkmcnt(7)\n\t"
      "v_mfma_f32_16x16x32_bf16 v[96:99], v[16:19], v[32:35], v[96:99]\n\t"
      "s_waitcnt lgkmcnt(5)\n\t"
      "v_mfma_f32_16x16x32_bf16 v[100:103], v[20:23], v[32:35], v[100:103]\n\t"
      "s_waitcnt vmcnt(6) lgkmcnt(0)\n\t"
      "s_barrier\n\t"
      "ds_read_b64_tr_b16 v[40:41], %[b00] offset:32768\n\t"
      "ds_read_b64_tr_b16 v[42:43], %[b00] offset:36864\n\t"
      "v_mfma_f32_16x16x32_bf16 v[112:115], v[16:19], v[36:39], v[112:115]\n\t"
      "ds_read_b128 v[56:59], %[a2] offset:24576\n\t"
      "ds_read_b64_tr_b16 v[44:45], %[b10] offset:32768\n\t"
      "v_mfma_f32_16x16x32_bf16 v[116:119], v[20:23], v[36:39], v[116:119]\n\t"
      "ds_read_b64_tr_b16 v[46:47], %[b10] offset:36864\n\t"
      "ds_read_b128 v[60:63], %[a2] offset:25600\n\t"
      "v_mfma_f32_16x16x32_bf16 v[104:107], v[24:27], v[32:35], v[104:107]\n\t"
      "ds_read_b64_tr_b16 v[48:49], %[b00] offset:33024\n\t"
      "ds_read_b64_tr_b16 v[50:51], %[b00] offset:37120\n\t"
      "v_mfma_f32_16x16x32_bf16 v[120:123], v[24:27], v[36:39], v[120:123]\n\t"
      "ds_read_b64_tr_b16 v[52:53], %[b10] offset:33024\n\t"
      "v_mfma_f32_16x16x32_bf16 v[108:111], v[28:31], v[32:35], v[108:111]\n\t"
      "ds_read_b64_tr_b16 v[54:55], %[b10] offset:37120\n\t"
      "v_mfma_f32_16x16x32_bf16 v[124:127], v[28:31], v[36:39], v[124:127]\n\t"
      "s_waitcnt lgkmcnt(7)\n\t"
      "v_mfma_f32_16x16x32_bf16 v[96:99], v[40:43], v[56:59], v[96:99]\n\t"
      "ds_read_b64_tr_b16 v[16:17], %[b00] offset:24576\n\t"
      "ds_read_b64_tr_b16 v[18:19], %[b00] offset:28672\n\t"
      "s_waitcnt lgkmcnt(7)\n\t"
      "v_mfma_f32_16x16x32_bf16 v[100:103], v[44:47], v[56:59], v[100:103]\n\t"
      "ds_read_b128 v[32:35], %[a1] offset:16384\n\t"
      "s_waitcnt lgkmcnt(7)\n\t"
      "v_mfma_f32_16x16x32_bf16 v[112:115], v[40:43], v[60:63], v[112:115]\n\t"
      "ds_read_b64_tr_b16 v[20:21], %[b10] offset:24576\n\t"
      "v_mfma_f32_16x16x32_bf16 v[116:119], v[44:47], v[60:63], v[116:119]\n\t"
      "ds_read_b64_tr_b16 v[22:23], %[b10] offset:28672\n\t"
      "ds_read_b128 v[36:39], %[a1] offset:17408\n\t"
      "s_waitcnt lgkmcnt(8)\n\t"
      "v_mfma_f32_16x16x32_bf16 v[104:107], v[48:51], v[56:59], v[104:107]\n\t"
      "ds_read_b64_tr_b16 v[24:25], %[b00] offset:24832\n\t"
      "v_mfma_f32_16x16x32_bf16 v[120:123], v[48:51], v[60:63], v[120:123]\n\t"
      "ds_read_b64_tr_b16 v[26:27], %[b00] offset:28928\n\t"
      "s_waitcnt lgkmcnt(8)\n\t"
      "v_mfma_f32_16x16x32_bf16 v[108:111], v[52:55], v[56:59], v[108:111]\n\t"
      "ds_read_b64_tr_b16 v[28:29], %[b10] offset:24832\n\t"
      "ds_read_b64_tr_b16 v[30:31], %[b10] offset:28928\n\t"
      "v_mfma_f32_16x16x32_bf16 v[124:127], v[52:55], v[60:63], v[124:127]\n\t"
      "s_waitcnt lgkmcnt(7)\n\t"
      "v_mfma_f32_16x16x32_bf16 v[96:99], v[16:19], v[32:35], v[96:99]\n\t"
      "s_waitcnt lgkmcnt(5)\n\t"
      "v_mfma_f32_16x16x32_bf16 v[100:103], v[20:23], v[32:35], v[100:103]\n\t"
      "s_waitcnt vmcnt(2) lgkmcnt(0)\n\t"
      "s_barrier\n\t"
      "ds_read_b64_tr_b16 v[40:41], %[b00] offset:16384\n\t"
      "ds_read_b64_tr_b16 v[42:43], %[b00] offset:20480\n\t"
      "v_mfma_f32_16x16x32_bf16 v[112:115], v[16:19], v[36:39], v[112:115]\n\t"
      "ds_read_b128 v[56:59], %[a1] offset:24576\n\t"
      "ds_read_b64_tr_b16 v[44:45], %[b10] offset:16384\n\t"
      "v_mfma_f32_16x16x32_bf16 v[116:119], v[20:23], v[36:39], v[116:119]\n\t"
      "ds_read_b64_tr_b16 v[46:47], %[b10] offset:20480\n\t"
      "ds_read_b128 v[60:63], %[a1] offset:25600\n\t"
      "v_mfma_f32_16x16x32_bf16 v[104:107], v[24:27], v[32:35], v[104:107]\n\t"
      "ds_read_b64_tr_b16 v[48:49], %[b00] offset:16640\n\t"
      "ds_read_b64_tr_b16 v[50:51], %[b00] offset:20736\n\t"
      "v_mfma_f32_16x16x32_bf16 v[120:123], v[24:27], v[36:39], v[120:123]\n\t"
      "ds_read_b64_tr_b16 v[52:53], %[b10] offset:16640\n\t"
      "v_mfma_f32_16x16x32_bf16 v[108:111], v[28:31], v[32:35], v[108:111]\n\t"
      "ds_read_b64_tr_b16 v[54:55], %[b10] offset:20736\n\t"
      "v_mfma_f32_16x16x32_bf16 v[124:127], v[28:31], v[36:39], v[124:127]\n\t"
      "s_waitcnt lgkmcnt(7)\n\t"
      "v_mfma_f32_16x16x32_bf16 v[96:99], v[40:43], v[56:59], v[96:99]\n\t"
      "ds_read_b64_tr_b16 v[16:17], %[b00] offset:8192\n\t"
      "ds_read_b64_tr_b16 v[18:19], %[b00] offset:12288\n\t"
      "s_waitcnt lgkmcnt(7)\n\t"
      "v_mfma_f32_16x16x32_bf16 v[100:103], v[44:47], v[56:59], v[100:103]\n\t"
      "ds_read_b128 v[32:35], %[a1] offset:32768\n\t"
      "s_waitcnt lgkmcnt(7)\n\t"
      "v_mfma_f32_16x16x32_bf16 v[112:115], v[40:43], v[60:63], v[112:115]\n\t"
      "ds_read_b64_tr_b16 v[20:21], %[b10] offset:8192\n\t"
      "v_mfma_f32_16x16x32_bf16 v[116:119], v[44:47], v[60:63], v[116:119]\n\t"
      "ds_read_b64_tr_b16 v[22:23], %[b10] offset:12288\n\t"
      "ds_read_b128 v[36:39], %[a1] offset:33792\n\t"
      "s_waitcnt lgkmcnt(8)\n\t"
      "v_mfma_f32_16x16x32_bf16 v[104:107], v[48:51], v[56:59], v[104:107]\n\t"
      "ds_read_b64_tr_b16 v[24:25], %[b00] offset:8448\n\t"
      "v_mfma_f32_16x16x32_bf16 v[120:123], v[48:51], v[60:63], v[120:123]\n\t"
      "ds_read_b64_tr_b16 v[26:27], %[b00] offset:12544\n\t"
      "s_waitcnt lgkmcnt(8)\n\t"
      "v_mfma_f32_16x16x32_bf16 v[108:111], v[52:55], v[56:59], v[108:111]\n\t"
      "ds_read_b64_tr_b16 v[28:29], %[b10] offset:8448\n\t"
      "ds_read_b64_tr_b16 v[30:31], %[b10] offset:12544\n\t"
      "v_mfma_f32_16x16x32_bf16 v[124:127], v[52:55], v[60:63], v[124:127]\n\t"
      "s_waitcnt lgkmcnt(7)\n\t"
      "v_mfma_f32_16x16x32_bf16 v[96:99], v[16:19], v[32:35], v[96:99]\n\t"
      "s_waitcnt lgkmcnt(5)\n\t"
      "v_mfma_f32_16x16x32_bf16 v[100:103], v[20:23], v[32:35], v[100:103]\n\t"
      "s_waitcnt vmcnt(0) lgkmcnt(0)\n\t"
      "s_barrier\n\t"
      "ds_read_b64_tr_b16 v[40:41], %[b00] offset:0\n\t"
      "ds_read_b64_tr_b16 v[42:43], %[b00] offset:4096\n\t"
      "v_mfma_f32_16x16x32_bf16 v[112:115], v[16:19], v[36:39], v[112:115]\n\t"
      "ds_read_b128 v[56:59], %[a1] offset:40960\n\t"
      "ds_read_b64_tr_b16 v[44:45], %[b10] offset:0\n\t"
      "v_mfma_f32_16x16x32_bf16 v[116:119], v[20:23], v[36:39], v[116:119]\n\t"
      "ds_read_b64_tr_b16 v[46:47], %[b10] offset:4096\n\t"
      "ds_read_b128 v[60:63], %[a1] offset:41984\n\t"
      "v_mfma_f32_16x16x32_bf16 v[104:107], v[24:27], v[32:35], v[104:107]\n\t"
      "ds_read_b64_tr_b16 v[48:49], %[b00] offset:256\n\t"
      "ds_read_b64_tr_b16 v[50:51], %[b00] offset:4352\n\t"
      "v_mfma_f32_16x16x32_bf16 v[120:123], v[24:27], v[36:39], v[120:123]\n\t"
      "ds_read_b64_tr_b16 v[52:53], %[b10] offset:256\n\t"
      "v_mfma_f32_16x16x32_bf16 v[108:111], v[28:31], v[32:35], v[108:111]\n\t"
      "ds_read_b64_tr_b16 v[54:55], %[b10] offset:4352\n\t"
      "v_mfma_f32_16x16x32_bf16 v[124:127], v[28:31], v[36:39], v[124:127]\n\t"
      "s_waitcnt lgkmcnt(7)\n\t"
      "v_mfma_f32_16x16x32_bf16 v[96:99], v[40:43], v[56:59], v[96:99]\n\t"
      "s_waitcnt lgkmcnt(5)\n\t"
      "v_mfma_f32_16x16x32_bf16 v[100:103], v[44:47], v[56:59], v[100:103]\n\t"
      "s_waitcnt lgkmcnt(4)\n\t"
      "v_mfma_f32_16x16x32_bf16 v[112:115], v[40:43], v[60:63], v[112:115]\n\t"
      "v_mfma_f32_16x16x32_bf16 v[116:119], v[44:47], v[60:63], v[116:119]\n\t"
      "s_waitcnt lgkmcnt(2)\n\t"
      "v_mfma_f32_16x16x32_bf16 v[104:107], v[48:51], v[56:59], v[104:107]\n\t"
      "v_mfma_f32_16x16x32_bf16 v[120:123], v[48:51], v[60:63], v[120:123]\n\t"
      "s_waitcnt lgkmcnt(0)\n\t"
      "v_mfma_f32_16x16x32_bf16 v[108:111], v[52:55], v[56:59], v[108:111]\n\t"
      "v_mfma_f32_16x16x32_bf16 v[124:127], v[52:55], v[60:63], v[124:127]\n\t"
      "s_nop 7\n\t"
      "v_cvt_pk_bf16_f32 v96, v96, v97\n\t"
      "v_cvt_pk_bf16_f32 v97, v98, v99\n\t"
      "v_cvt_pk_bf16_f32 v98, v100, v101\n\t"
      "v_cvt_pk_bf16_f32 v99, v102, v103\n\t"
      "s_add_u32 %[t0], %[so1], 0\n\t"
      "buffer_store_dwordx4 v[96:99], %[vl], %[rs], %[t0] offen sc1\n\t"
      "v_cvt_pk_bf16_f32 v104, v104, v105\n\t"
      "v_cvt_pk_bf16_f32 v105, v106, v107\n\t"
      "v_cvt_pk_bf16_f32 v106, v108, v109\n\t"
      "v_cvt_pk_bf16_f32 v107, v110, v111\n\t"
      "s_add_u32 %[t0], %[so1], 1024\n\t"
      "buffer_store_dwordx4 v[104:107], %[vl], %[rs], %[t0] offen sc1\n\t"
      "v_cvt_pk_bf16_f32 v112, v112, v113\n\t"
      "v_cvt_pk_bf16_f32 v113, v114, v115\n\t"
      "v_cvt_pk_bf16_f32 v114, v116, v117\n\t"
      "v_cvt_pk_bf16_f32 v115, v118, v119\n\t"
      "s_add_u32 %[t0], %[so1], 16384\n\t"
      "buffer_store_dwordx4 v[112:115], %[vl], %[rs], %[t0] offen sc1\n\t"
      "v_cvt_pk_bf16_f32 v120, v120, v121\n\t"
      "v_cvt_pk_bf16_f32 v121, v122, v123\n\t"
      "v_cvt_pk_bf16_f32 v122, v124, v125\n\t"
      "v_cvt_pk_bf16_f32 v123, v126, v127\n\t"
      "s_add_u32 %[t0], %[so1], 17408\n\t"
      "buffer_store_dwordx4 v[120:123], %[vl], %[rs], %[t0] offen sc1\n\t"
      "s_add_u32 %[t0], %[sg], 0\n\t"
      "buffer_load_dwordx4 v[96:99], %[vl], %[rs], %[t0] offen offset:0 sc1\n\t"
      "s_add_u32 %[t0], %[sg], 0\n\t"
      "buffer_load_dwordx4 v[100:103], %[vl], %[rs], %[t0] offen offset:1024 sc1\n\t"
      "s_add_u32 %[t0], %[sg], 0\n\t"
      "buffer_load_dwordx4 v[104:107], %[vl], %[rs], %[t0] offen offset:2048 sc1\n\t"
      "s_add_u32 %[t0], %[sg], 0\n\t"
      "buffer_load_dwordx4 v[108:111], %[vl], %[rs], %[t0] offen offset:3072 sc1\n\t"
      "s_add_u32 %[t0], %[sg], 4096\n\t"
      "buffer_load_dwordx4 v[112:115], %[vl], %[rs], %[t0] offen offset:0 sc1\n\t"
      "s_add_u32 %[t0], %[sg], 4096\n\t"
      "buffer_load_dwordx4 v[116:119], %[vl], %[rs], %[t0] offen offset:1024 sc1\n\t"
      "s_add_u32 %[t0], %[sg], 4096\n\t"
      "buffer_load_dwordx4 v[120:123], %[vl], %[rs], %[t0] offen offset:2048 sc1\n\t"
      "s_add_u32 %[t0], %[sg], 4096\n\t"
      "buffer_load_dwordx4 v[124:127], %[vl], %[rs], %[t0] offen offset:3072 sc1\n\t"
      "s_branch .Lx%=\n\t"
      ".Lw%=:\n\t"
      "s_sleep 1\n\t"
      "buffer_load_dword v102, off, %[rs], %[fw] sc1\n\t"
      "buffer_load_dword v101, off, %[rs], %[fa] sc1\n\t"
      "s_waitcnt vmcnt(0)\n\t"
      "v_readfirstlane_b32 %[t0], v101\n\t"
      "s_cmp_lt_u32 %[t0], 4\n\t"
      "s_cbranch_scc1 .Lw%=\n\t"
      "v_readfirstlane_b32 %[t0], v102\n\t"
      "s_cmp_lt_u32 %[t0], 16\n\t"
      "s_cbranch_scc1 .Lw%=\n\t"
      "s_branch .Lr%=\n\t"
      ".Lx%=:\n\t"
      : [t0] "=&s"(t0), "=&{v[96:127]}"(WV)
      : [sw] "s"(sW3), [vw] "v"(vW3), [rw] "s"(srdW), [da] "s"(dA3), [vl] "v"(vL), [rs] "s"(srdS), [sa0] "s"(sA0), [sa1] "s"(sA1), [so0] "s"(sO0), [so1] "s"(sO1), [sg] "s"(sG), [wid] "s"(wid), [f0] "s"(f0), [fa] "s"(fCA), [fw] "s"(fW), [a1] "v"(aB + 65536u), [a2] "v"(aB + 131072u), [b00] "v"((b0 ^ 0u) + 0u), [b01] "v"((b0 ^ 0u) + 65536u), [b10] "v"((b0 ^ 32u) + 0u), [b11] "v"((b0 ^ 32u) + 65536u)
      : "memory", "scc", "v16", "v17", "v18", "v19", "v20", "v21", "v22", "v23", "v24", "v25", "v26", "v27", "v28", "v29", "v30", "v31", "v32", "v33", "v34", "v35", "v36", "v37", "v38", "v39", "v40", "v41", "v42", "v43", "v44", "v45", "v46", "v47", "v48", "v49", "v50", "v51", "v52", "v53", "v54", "v55", "v56", "v57", "v58", "v59", "v60", "v61", "v62", "v63", "v64", "v65", "v66", "v67", "v68", "v69", "v70", "v71", "v72", "v73", "v74", "v75", "v76", "v77", "v78", "v79", "v80", "v81", "v82", "v83", "v84", "v85", "v86", "v87", "v88", "v89", "v90", "v91", "v92", "v93", "v94", "v95", "a0", "a1", "a2", "a3", "a4", "a5", "a6", "a7", "a8", "a9", "a10", "a11", "a12", "a13", "a14", "a15", "a16", "a17", "a18", "a19", "a20", "a21", "a22", "a23", "a24", "a25", "a26", "a27", "a28", "a29", "a30", "a31", "a32", "a33", "a34", "a35", "a36", "a37", "a38", "a39", "a40", "a41", "a42", "a43", "a44", "a45", "a46", "a47", "a48", "a49", "a50", "a51", "a52", "a53", "a54", "a55", "a56", "a57", "a58", "a59", "a60", "a61", "a62", "a63", "a64", "a65", "a66", "a67", "a68", "a69", "a70", "a71", "a72", "a73", "a74", "a75", "a76", "a77", "a78", "a79", "a80", "a81", "a82", "a83", "a84", "a85", "a86", "a87", "a88", "a89", "a90", "a91", "a92", "a93", "a94", "a95", "a96", "a97", "a98", "a99", "a100", "a101", "a102", "a103", "a104", "a105", "a106", "a107", "a108", "a109", "a110", "a111", "a112", "a113", "a114", "a115", "a116", "a117", "a118", "a119", "a120", "a121", "a122", "a123", "a124", "a125", "a126", "a127");
  }
  TS(1);
  const unsigned q8 = 128u * par + 16u * bi + 2u * sp + (bn >> 1);
  u32x4 x1;
  {
    LANES;
    const unsigned rb = q8 >> 1, r = 8u * (q8 & 1u) + (lane & 7u), g4 = (lane >> 3) & 3u, s = 2u * wid + (lane >> 5);
    const unsigned fo = (rb * 16u + s) * 1024u + (r + 16u * g4) * 16u;
    const unsigned col = 32u * s + 4u * g4;
    u32x2 xa, xb; f32x4 ga0, ga1, sb0, sb1, cb0, cb1;
    asm volatile("global_load_dwordx2 %0, %8, off\n\tglobal_load_dwordx2 %1, %8, off offset:32\n\t"
                 "global_load_dwordx4 %2, %9, off sc1\n\tglobal_load_dwordx4 %3, %9, off offset:64 sc1\n\t"
                 "global_load_dwordx4 %4, %9, off offset:2048 sc1\n\tglobal_load_dwordx4 %5, %9, off offset:2112 sc1\n\t"
                 "global_load_dwordx4 %6, %10, off sc1\n\tglobal_load_dwordx4 %7, %10, off offset:64 sc1\n\t"
                 "s_waitcnt vmcnt(16)"
                 : "=&v"(xa), "=&v"(xb), "=&v"(ga0), "=&v"(ga1), "=&v"(sb0), "=&v"(sb1), "=&v"(cb0), "=&v"(cb1)
                 : "v"(x2 + (rb * 16u + r) * 512u + col), "v"(mod + 1024 + col), "v"(mod + 2048 + col) : "memory");
    BAR();
    count_up(srdS, F_S3 + qbA2, wid == 0u);
    TS(2);
    if (wid != 0) {
    asm volatile(
      "s_add_u32 %[t0], %[sg], 8192\n\t"
      "buffer_load_dwordx4 a[0:3], %[vl], %[rs], %[t0] offen offset:0 sc1\n\t"
      "s_add_u32 %[t0], %[sg], 8192\n\t"
      "buffer_load_dwordx4 a[4:7], %[vl], %[rs], %[t0] offen offset:1024 sc1\n\t"
      "s_add_u32 %[t0], %[sg], 8192\n\t"
      "buffer_load_dwordx4 a[8:11], %[vl], %[rs], %[t0] offen offset:2048 sc1\n\t"
      "s_add_u32 %[t0], %[sg], 8192\n\t"
      "buffer_load_dwordx4 a[12:15], %[vl], %[rs], %[t0] offen offset:3072 sc1\n\t"
      "s_add_u32 %[t0], %[sg], 12288\n\t"
      "buffer_load_dwordx4 a[16:19], %[vl], %[rs], %[t0] offen offset:0 sc1\n\t"
      "s_add_u32 %[t0], %[sg], 12288\n\t"
      "buffer_load_dwordx4 a[20:23], %[vl], %[rs], %[t0] offen offset:1024 sc1\n\t"
      "s_add_u32 %[t0], %[sg], 12288\n\t"
      "buffer_load_dwordx4 a[24:27], %[vl], %[rs], %[t0] offen offset:2048 sc1\n\t"
      "s_add_u32 %[t0], %[sg], 12288\n\t"
      "buffer_load_dwordx4 a[28:31], %[vl], %[rs], %[t0] offen offset:3072 sc1\n\t"
      "s_add_u32 %[t0], %[sg], 16384\n\t"
      "buffer_load_dwordx4 a[32:35], %[vl], %[rs], %[t0] offen offset:0 sc1\n\t"
      "s_add_u32 %[t0], %[sg], 16384\n\t"
      "buffer_load_dwordx4 a[36:39], %[vl], %[rs], %[t0] offen offset:1024 sc1\n\t"
      "s_add_u32 %[t0], %[sg], 16384\n\t"
      "buffer_load_dwordx4 a[40:43], %[vl], %[rs], %[t0] offen offset:2048 sc1\n\t"
      "s_add_u32 %[t0], %[sg], 16384\n\t"
      "buffer_load_dwordx4 a[44:47], %[vl], %[rs], %[t0] offen offset:3072 sc1\n\t"
      "s_add_u32 %[t0], %[sg], 20480\n\t"
      "buffer_load_dwordx4 a[48:51], %[vl], %[rs], %[t0] offen offset:0 sc1\n\t"
      "s_add_u32 %[t0], %[sg], 20480\n\t"
      "buffer_load_dwordx4 a[52:55], %[vl], %[rs], %[t0] offen offset:1024 sc1\n\t"
      "s_add_u32 %[t0], %[sg], 20480\n\t"
      "buffer_load_dwordx4 a[56:59], %[vl], %[rs], %[t0] offen offset:2048 sc1\n\t"
      "s_add_u32 %[t0], %[sg], 20480\n\t"
      "buffer_load_dwordx4 a[60:63], %[vl], %[rs], %[t0] offen offset:3072 sc1\n\t"
      "s_add_u32 %[t0], %[sg], 24576\n\t"
      "buffer_load_dwordx4 a[64:67], %[vl], %[rs], %[t0] offen offset:0 sc1\n\t"
      "s_add_u32 %[t0], %[sg], 24576\n\t"
      "buffer_load_dwordx4 a[68:71], %[vl], %[rs], %[t0] offen offset:1024 sc1\n\t"
      "s_add_u32 %[t0], %[sg], 24576\n\t"
      "buffer_load_dwordx4 a[72:75], %[vl], %[rs], %[t0] offen offset:2048 sc1\n\t"
      "s_add_u32 %[t0], %[sg], 24576\n\t"
      "buffer_load_dwordx4 a[76:79], %[vl], %[rs], %[t0] offen offset:3072 sc1\n\t"
      "s_add_u32 %[t0], %[sg], 28672\n\t"
      "buffer_load_dwordx4 a[80:83], %[vl], %[rs], %[t0] offen offset:0 sc1\n\t"
      "s_add_u32 %[t0], %[sg], 28672\n\t"
      "buffer_load_dwordx4 a[84:87], %[vl], %[rs], %[t0] offen offset:1024 sc1\n\t"
      "s_add_u32 %[t0], %[sg], 28672\n\t"
      "buffer_load_dwordx4 a[88:91], %[vl], %[rs], %[t0] offen offset:2048 sc1\n\t"
      "s_add_u32 %[t0], %[sg], 28672\n\t"
      "buffer_load_dwordx4 a[92:95], %[vl], %[rs], %[t0] offen offset:3072 sc1\n\t"
      : [t0] "=&s"(t0) : [vl] "v"(vL), [rs] "s"(srdS), [sg] "s"(sG) : "memory", "scc", "a0", "a1", "a2", "a3", "a4", "a5", "a6", "a7", "a8", "a9", "a10", "a11", "a12", "a13", "a14", "a15", "a16", "a17", "a18", "a19", "a20", "a21", "a22", "a23", "a24", "a25", "a26", "a27", "a28", "a29", "a30", "a31", "a32", "a33", "a34", "a35", "a36", "a37", "a38", "a39", "a40", "a41", "a42", "a43", "a44", "a45", "a46", "a47", "a48", "a49", "a50", "a51", "a52", "a53", "a54", "a55", "a56", "a57", "a58", "a59", "a60", "a61", "a62", "a63", "a64", "a65", "a66", "a67", "a68", "a69", "a70", "a71", "a72", "a73", "a74", "a75", "a76", "a77", "a78", "a79", "a80", "a81", "a82", "a83", "a84", "a85", "a86", "a87", "a88", "a89", "a90", "a91", "a92", "a93", "a94", "a95");
    }
    await(srdS, F_S3 + (q8 >> 4), 32u, wid);
    u32x4 p[8];
#pragma unroll
    for (int i = 0; i < 8; ++i) asm volatile("buffer_load_dwordx4 %0, %1, %2, %3 offen sc1" : "=v"(p[i]) : "v"(fo), "s"(srdS), "s"((unsigned)WS_PART + (unsigned)i * 2097152u) : "memory");
    asm volatile("s_waitcnt vmcnt(8)" : "+v"(xa), "+v"(xb), "+v"(ga0), "+v"(ga1), "+v"(sb0), "+v"(sb1), "+v"(cb0), "+v"(cb1) :: "memory");
    const f32x2 xv[4] = {up2(xa[0]), up2(xa[1]), up2(xb[0]), up2(xb[1])};
    const f32x2 ga[4] = {{ga0[0], ga0[1]}, {ga0[2], ga0[3]}, {ga1[0], ga1[1]}, {ga1[2], ga1[3]}};
    const f32x2 sb[4] = {{sb0[0], sb0[1]}, {sb0[2], sb0[3]}, {sb1[0], sb1[1]}, {sb1[2], sb1[3]}};
    f32x2 c1[4] = {{cb0[0], cb0[1]}, {cb0[2], cb0[3]}, {cb1[0], cb1[1]}, {cb1[2], cb1[3]}};
#pragma unroll
    for (int e = 0; e < 4; ++e) c1[e] = rbf2(c1[e] + 1.f);
    asm volatile("s_waitcnt vmcnt(0)" : "+v"(p[0]), "+v"(p[1]), "+v"(p[2]), "+v"(p[3]), "+v"(p[4]), "+v"(p[5]), "+v"(p[6]), "+v"(p[7]), "+v"(c1[0]), "+v"(c1[1]), "+v"(c1[2]), "+v"(c1[3]) :: "memory");
    TS(3);
    f32x2 v[4], s2 = {0.f, 0.f};
#pragma unroll
    for (int e = 0; e < 4; ++e) {
      f32x2 a = up2(p[0][e]);
#pragma unroll
      for (int i = 1; i < 8; ++i) a += up2(p[i][e]);
      x1[e] = cvtpk((xv[e] + rbf2(ga[e] * rbf2(a)))[0], (xv[e] + rbf2(ga[e] * rbf2(a)))[1]);
      v[e] = up2(x1[e]); s2 += v[e];
    }
    const float sum = red_hi(s2[0] + s2[1]);
    f32x2 q2 = {0.f, 0.f};
#pragma unroll
    for (int e = 0; e < 4; ++e) { const f32x2 d = v[e] - sum * (1.f / 64.f); q2 += d * d; }
    const float q = red_hi(q2[0] + q2[1]);
    const unsigned red = lds0 + 98304u;
    if (lane < 8) *(LDSP(f32x2))(uintptr_t)(red + (lane * 8u + wid) * 8u) = f32x2{sum, q};
    WAIT_L(0); BAR();
    f32x4 st[4];
#pragma unroll
    for (int i = 0; i < 4; ++i) st[i] = lds_f4(red + (lane & 7u) * 64u + i * 16u);
    const float mean = (((st[0][0] + st[0][2]) + (st[1][0] + st[1][2])) + ((st[2][0] + st[2][2]) + (st[3][0] + st[3][2]))) * (1.f / 512.f);
    float m2 = 0.f;
#pragma unroll
    for (int i = 0; i < 4; ++i) {
      const float d0 = st[i][0] * (1.f / 64.f) - mean, d1 = st[i][2] * (1.f / 64.f) - mean;
      m2 += (st[i][1] + st[i][3]) + 64.f * (d0 * d0 + d1 * d1);
    }
    const float rstd = rsqrtf(m2 * (1.f / 512.f) + 1e-5f);
    u32x4 h2;
#pragma unroll
    for (int e = 0; e < 4; ++e) { const f32x2 h = __builtin_elementwise_fma(rbf2((v[e] - mean) * rstd), c1[e], sb[e]); h2[e] = cvtpk(h[0], h[1]); }
    sstore16(h2, srdS, fo, (unsigned)WS_H2);
    TS(4);
    WAIT_V(0); BAR();
    count_up(srdS, F_S4 + (q8 >> 3), wid == 0u);
    if (wid == 0) {
    asm volatile(
      "s_add_u32 %[t0], %[sg], 8192\n\t"
      "buffer_load_dwordx4 a[0:3], %[vl], %[rs], %[t0] offen offset:0 sc1\n\t"
      "s_add_u32 %[t0], %[sg], 8192\n\t"
      "buffer_load_dwordx4 a[4:7], %[vl], %[rs], %[t0] offen offset:1024 sc1\n\t"
      "s_add_u32 %[t0], %[sg], 8192\n\t"
      "buffer_load_dwordx4 a[8:11], %[vl], %[rs], %[t0] offen offset:2048 sc1\n\t"
      "s_add_u32 %[t0], %[sg], 8192\n\t"
      "buffer_load_dwordx4 a[12:15], %[vl], %[rs], %[t0] offen offset:3072 sc1\n\t"
      "s_add_u32 %[t0], %[sg], 12288\n\t"
      "buffer_load_dwordx4 a[16:19], %[vl], %[rs], %[t0] offen offset:0 sc1\n\t"
      "s_add_u32 %[t0], %[sg], 12288\n\t"
      "buffer_load_dwordx4 a[20:23], %[vl], %[rs], %[t0] offen offset:1024 sc1\n\t"
      "s_add_u32 %[t0], %[sg], 12288\n\t"
      "buffer_load_dwordx4 a[24:27], %[vl], %[rs], %[t0] offen offset:2048 sc1\n\t"
      "s_add_u32 %[t0], %[sg], 12288\n\t"
      "buffer_load_dwordx4 a[28:31], %[vl], %[rs], %[t0] offen offset:3072 sc1\n\t"
      "s_add_u32 %[t0], %[sg], 16384\n\t"
      "buffer_load_dwordx4 a[32:35], %[vl], %[rs], %[t0] offen offset:0 sc1\n\t"
      "s_add_u32 %[t0], %[sg], 16384\n\t"
      "buffer_load_dwordx4 a[36:39], %[vl], %[rs], %[t0] offen offset:1024 sc1\n\t"
      "s_add_u32 %[t0], %[sg], 16384\n\t"
      "buffer_load_dwordx4 a[40:43], %[vl], %[rs], %[t0] offen offset:2048 sc1\n\t"
      "s_add_u32 %[t0], %[sg], 16384\n\t"
      "buffer_load_dwordx4 a[44:47], %[vl], %[rs], %[t0] offen offset:3072 sc1\n\t"
      "s_add_u32 %[t0], %[sg], 20480\n\t"
      "buffer_load_dwordx4 a[48:51], %[vl], %[rs], %[t0] offen offset:0 sc1\n\t"
      "s_add_u32 %[t0], %[sg], 20480\n\t"
      "buffer_load_dwordx4 a[52:55], %[vl], %[rs], %[t0] offen offset:1024 sc1\n\t"
      "s_add_u32 %[t0], %[sg], 20480\n\t"
      "buffer_load_dwordx4 a[56:59], %[vl], %[rs], %[t0] offen offset:2048 sc1\n\t"
      "s_add_u32 %[t0], %[sg], 20480\n\t"
      "buffer_load_dwordx4 a[60:63], %[vl], %[rs], %[t0] offen offset:3072 sc1\n\t"
      "s_add_u32 %[t0], %[sg], 24576\n\t"
      "buffer_load_dwordx4 a[64:67], %[vl], %[rs], %[t0] offen offset:0 sc1\n\t"
      "s_add_u32 %[t0], %[sg], 24576\n\t"
      "buffer_load_dwordx4 a[68:71], %[vl], %[rs], %[t0] offen offset:1024 sc1\n\t"
      "s_add_u32 %[t0], %[sg], 24576\n\t"
      "buffer_load_dwordx4 a[72:75], %[vl], %[rs], %[t0] offen offset:2048 sc1\n\t"
      "s_add_u32 %[t0], %[sg], 24576\n\t"
      "buffer_load_dwordx4 a[76:79], %[vl], %[rs], %[t0] offen offset:3072 sc1\n\t"
      "s_add_u32 %[t0], %[sg], 28672\n\t"
      "buffer_load_dwordx4 a[80:83], %[vl], %[rs], %[t0] offen offset:0 sc1\n\t"
      "s_add_u32 %[t0], %[sg], 28672\n\t"
      "buffer_load_dwordx4 a[84:87], %[vl], %[rs], %[t0] offen offset:1024 sc1\n\t"
      "s_add_u32 %[t0], %[sg], 28672\n\t"
      "buffer_load_dwordx4 a[88:91], %[vl], %[rs], %[t0] offen offset:2048 sc1\n\t"
      "s_add_u32 %[t0], %[sg], 28672\n\t"
      "buffer_load_dwordx4 a[92:95], %[vl], %[rs], %[t0] offen offset:3072 sc1\n\t"
      : [t0] "=&s"(t0) : [vl] "v"(vL), [rs] "s"(srdS), [sg] "s"(sG) : "memory", "scc", "a0", "a1", "a2", "a3", "a4", "a5", "a6", "a7", "a8", "a9", "a10", "a11", "a12", "a13", "a14", "a15", "a16", "a17", "a18", "a19", "a20", "a21", "a22", "a23", "a24", "a25", "a26", "a27", "a28", "a29", "a30", "a31", "a32", "a33", "a34", "a35", "a36", "a37", "a38", "a39", "a40", "a41", "a42", "a43", "a44", "a45", "a46", "a47", "a48", "a49", "a50", "a51", "a52", "a53", "a54", "a55", "a56", "a57", "a58", "a59", "a60", "a61", "a62", "a63", "a64", "a65", "a66", "a67", "a68", "a69", "a70", "a71", "a72", "a73", "a74", "a75", "a76", "a77", "a78", "a79", "a80", "a81", "a82", "a83", "a84", "a85", "a86", "a87", "a88", "a89", "a90", "a91", "a92", "a93", "a94", "a95");
    }
  }
  if (sp < 7u) {
    LANES;
    const unsigned sA = (unsigned)WS_H2 + ((4u * tb + (wid & 3u)) * 16u + (wid >> 2)) * 1024u, dA = lds0 + wid * 1024u;
    const unsigned sD = (unsigned)WS_WDF + (256u * fg + 4u * wid) * 1024u;
    const unsigned aB = lds0 + vL, aW = lds0 + 65536u + (wid >> 1) * 4096u + vL + (wid & 1u) * 8u;
    const unsigned sO = (unsigned)WS_PT2 + fg * 2097152u + (64u * tb + 2u * wid) * 1024u;
    constexpr float KG = -2.f * 0.7978845608028654f * 1.4426950408889634f;
    const float c1 = 0.044715f * KG, c2 = KG;
    await(srdS, F_S4 + tb, 8u, wid ^ 1u);
    TS(5);
    asm volatile(
      "s_add_u32 %[t0], %[sa], 0\n\t"
      "s_add_u32 m0, %[da], 0\n\t"
      "s_nop 0\n\t"
      "buffer_load_dwordx4 %[vl], %[rs], %[t0] offen sc1 lds\n\t"
      "s_add_u32 %[t0], %[sa], 2048\n\t"
      "s_add_u32 m0, %[da], 8192\n\t"
      "s_nop 0\n\t"
      "buffer_load_dwordx4 %[vl], %[rs], %[t0] offen sc1 lds\n\t"
      "s_add_u32 %[t0], %[sa], 4096\n\t"
      "s_add_u32 m0, %[da], 16384\n\t"
      "s_nop 0\n\t"
      "buffer_load_dwordx4 %[vl], %[rs], %[t0] offen sc1 lds\n\t"
      "s_add_u32 %[t0], %[sa], 6144\n\t"
      "s_add_u32 m0, %[da], 24576\n\t"
      "s_nop 0\n\t"
      "buffer_load_dwordx4 %[vl], %[rs], %[t0] offen sc1 lds\n\t"
      "s_add_u32 %[t0], %[sa], 8192\n\t"
      "s_add_u32 m0, %[da], 32768\n\t"
      "s_nop 0\n\t"
      "buffer_load_dwordx4 %[vl], %[rs], %[t0] offen sc1 lds\n\t"
      "s_add_u32 %[t0], %[sa], 10240\n\t"
      "s_add_u32 m0, %[da], 40960\n\t"
      "s_nop 0\n\t"
      "buffer_load_dwordx4 %[vl], %[rs], %[t0] offen sc1 lds\n\t"
      "s_add_u32 %[t0], %[sa], 12288\n\t"
      "s_add_u32 m0, %[da], 49152\n\t"
      "s_nop 0\n\t"
      "buffer_load_dwordx4 %[vl], %[rs], %[t0] offen sc1 lds\n\t"
      "s_add_u32 %[t0], %[sa], 14336\n\t"
      "s_add_u32 m0, %[da], 57344\n\t"
      "s_nop 0\n\t"
      "buffer_load_dwordx4 %[vl], %[rs], %[t0] offen sc1 lds\n\t"
      "s_waitcnt vmcnt(6)\n\t"
      "s_barrier\n\t"
      "ds_read_b128 v[16:19], %[ab] offset:0\n\t"
      "ds_read_b128 v[20:23], %[ab] offset:1024\n\t"
      "ds_read_b128 v[24:27], %[ab] offset:2048\n\t"
      "ds_read_b128 v[28:31], %[ab] offset:3072\n\t"
      "s_waitcnt vmcnt(39) lgkmcnt(3)\n\t"
      "v_mfma_f32_16x16x32_bf16 v[64:67], v[96:99], v[16:19], 0\n\t"
      "s_waitcnt vmcnt(38)\n\t"
      "v_mfma_f32_16x16x32_bf16 v[68:71], v[100:103], v[16:19], 0\n\t"
      "ds_read_b128 v[32:35], %[ab] offset:4096\n\t"
      "s_waitcnt lgkmcnt(3)\n\t"
      "v_mfma_f32_16x16x32_bf16 v[72:75], v[96:99], v[20:23], 0\n\t"
      "v_mfma_f32_16x16x32_bf16 v[76:79], v[100:103], v[20:23], 0\n\t"
      "ds_read_b128 v[36:39], %[ab] offset:5120\n\t"
      "s_waitcnt lgkmcnt(3)\n\t"
      "v_mfma_f32_16x16x32_bf16 v[80:83], v[96:99], v[24:27], 0\n\t"
      "v_mfma_f32_16x16x32_bf16 v[84:87], v[100:103], v[24:27], 0\n\t"
      "ds_read_b128 v[40:43], %[ab] offset:6144\n\t"
      "s_waitcnt lgkmcnt(3)\n\t"
      "v_mfma_f32_16x16x32_bf16 v[88:91], v[96:99], v[28:31], 0\n\t"
      "v_mfma_f32_16x16x32_bf16 v[92:95], v[100:103], v[28:31], 0\n\t"
      "ds_read_b128 v[44:47], %[ab] offset:7168\n\t"
      "s_waitcnt vmcnt(37) lgkmcnt(3)\n\t"
      "v_mfma_f32_16x16x32_bf16 v[64:67], v[104:107], v[32:35], v[64:67]\n\t"
      "s_waitcnt vmcnt(36)\n\t"
      "v_mfma_f32_16x16x32_bf16 v[68:71], v[108:111], v[32:35], v[68:71]\n\t"
      "ds_read_b128 v[16:19], %[ab] offset:8192\n\t"
      "s_waitcnt lgkmcnt(3)\n\t"
      "v_mfma_f32_16x16x32_bf16 v[72:75], v[104:107], v[36:39], v[72:75]\n\t"
      "v_mfma_f32_16x16x32_bf16 v[76:79], v[108:111], v[36:39], v[76:79]\n\t"
      "ds_read_b128 v[20:23], %[ab] offset:9216\n\t"
      "s_waitcnt lgkmcnt(3)\n\t"
      "v_mfma_f32_16x16x32_bf16 v[80:83], v[104:107], v[40:43], v[80:83]\n\t"
      "v_mfma_f32_16x16x32_bf16 v[84:87], v[108:111], v[40:43], v[84:87]\n\t"
      "ds_read_b128 v[24:27], %[ab] offset:10240\n\t"
      "s_waitcnt lgkmcnt(3)\n\t"
      "v_mfma_f32_16x16x32_bf16 v[88:91], v[104:107], v[44:47], v[88:91]\n\t"
      "v_mfma_f32_16x16x32_bf16 v[92:95], v[108:111], v[44:47], v[92:95]\n\t"
      "ds_read_b128 v[28:31], %[ab] offset:11264\n\t"
      "s_waitcnt vmcnt(35) lgkmcnt(3)\n\t"
      "v_mfma_f32_16x16x32_bf16 v[64:67], v[112:115], v[16:19], v[64:67]\n\t"
      "s_waitcnt vmcnt(34)\n\t"
      "v_mfma_f32_16x16x32_bf16 v[68:71], v[116:119], v[16:19], v[68:71]\n\t"
      "s_waitcnt vmcnt(3)\n\t"
      "s_barrier\n\t"
      "ds_read_b128 v[32:35], %[ab] offset:12288\n\t"
      "s_waitcnt lgkmcnt(3)\n\t"
      "v_mfma_f32_16x16x32_bf16 v[72:75], v[112:115], v[20:23], v[72:75]\n\t"
      "v_mfma_f32_16x16x32_bf16 v[76:79], v[116:119], v[20:23], v[76:79]\n\t"
      "ds_read_b128 v[36:39], %[ab] offset:13312\n\t"
      "s_waitcnt lgkmcnt(3)\n\t"
      "v_mfma_f32_16x16x32_bf16 v[80:83], v[112:115], v[24:27], v[80:83]\n\t"
      "v_mfma_f32_16x16x32_bf16 v[84:87], v[116:119], v[24:27], v[84:87]\n\t"
      "ds_read_b128 v[40:43], %[ab] offset:14336\n\t"
      "s_waitcnt lgkmcnt(3)\n\t"
      "v_mfma_f32_16x16x32_bf16 v[88:91], v[112:115], v[28:31], v[88:91]\n\t"
      "v_mfma_f32_16x16x32_bf16 v[92:95], v[116:119], v[28:31], v[92:95]\n\t"
      "ds_read_b128 v[44:47], %[ab] offset:15360\n\t"
      "s_waitcnt vmcnt(33) lgkmcnt(3)\n\t"
      "v_mfma_f32_16x16x32_bf16 v[64:67], v[120:123], v[32:35], v[64:67]\n\t"
      "s_waitcnt vmcnt(32)\n\t"
      "v_mfma_f32_16x16x32_bf16 v[68:71], v[124:127], v[32:35], v[68:71]\n\t"
      "ds_read_b128 v[16:19], %[ab] offset:16384\n\t"
      "s_waitcnt lgkmcnt(3)\n\t"
      "v_mfma_f32_16x16x32_bf16 v[72:75], v[120:123], v[36:39], v[72:75]\n\t"
      "v_mfma_f32_16x16x32_bf16 v[76:79], v[124:127], v[36:39], v[76:79]\n\t"
      "ds_read_b128 v[20:23], %[ab] offset:17408\n\t"
      "s_waitcnt lgkmcnt(3)\n\t"
      "v_mfma_f32_16x16x32_bf16 v[80:83], v[120:123], v[40:43], v[80:83]\n\t"
      "v_mfma_f32_16x16x32_bf16 v[84:87], v[124:127], v[40:43], v[84:87]\n\t"
      "ds_read_b128 v[24:27], %[ab] offset:18432\n\t"
      "s_waitcnt lgkmcnt(3)\n\t"
      "v_mfma_f32_16x16x32_bf16 v[88:91], v[120:123], v[44:47], v[88:91]\n\t"
      "v_mfma_f32_16x16x32_bf16 v[92:95], v[124:127], v[44:47], v[92:95]\n\t"
      "ds_read_b128 v[28:31], %[ab] offset:19456\n\t"
      "s_waitcnt vmcnt(31) lgkmcnt(3)\n\t"
      "v_mfma_f32_16x16x32_bf16 v[64:67], a[0:3], v[16:19], v[64:67]\n\t"
      "s_waitcnt vmcnt(30)\n\t"
      "v_mfma_f32_16x16x32_bf16 v[68:71], a[4:7], v[16:19], v[68:71]\n\t"
      "ds_read_b128 v[32:35], %[ab] offset:20480\n\t"
      "s_waitcnt lgkmcnt(3)\n\t"
      "v_mfma_f32_16x16x32_bf16 v[72:75], a[0:3], v[20:23], v[72:75]\n\t"
      "v_mfma_f32_16x16x32_bf16 v[76:79], a[4:7], v[20:23], v[76:79]\n\t"
      "ds_read_b128 v[36:39], %[ab] offset:21504\n\t"
      "s_waitcnt lgkmcnt(3)\n\t"
      "v_mfma_f32_16x16x32_bf16 v[80:83], a[0:3], v[24:27], v[80:83]\n\t"
      "v_mfma_f32_16x16x32_bf16 v[84:87], a[4:7], v[24:27], v[84:87]\n\t"
      "ds_read_b128 v[40:43], %[ab] offset:22528\n\t"
      "s_waitcnt lgkmcnt(3)\n\t"
      "v_mfma_f32_16x16x32_bf16 v[88:91], a[0:3], v[28:31], v[88:91]\n\t"
      "v_mfma_f32_16x16x32_bf16 v[92:95], a[4:7], v[28:31], v[92:95]\n\t"
      "ds_read_b128 v[44:47], %[ab] offset:23552\n\t"
      "s_waitcnt vmcnt(29) lgkmcnt(3)\n\t"
      "v_mfma_f32_16x16x32_bf16 v[64:67], a[8:11], v[32:35], v[64:67]\n\t"
      "s_waitcnt vmcnt(28)\n\t"
      "v_mfma_f32_16x16x32_bf16 v[68:71], a[12:15], v[32:35], v[68:71]\n\t"
      "ds_read_b128 v[16:19], %[ab] offset:24576\n\t"
      "s_waitcnt lgkmcnt(3)\n\t"
      "v_mfma_f32_16x16x32_bf16 v[72:75], a[8:11], v[36:39], v[72:75]\n\t"
      "s_add_u32 %[t0], %[sg], 262144\n\t"
      "buffer_load_dwordx4 a[0:3], %[vl], %[rs], %[t0] offen offset:0 sc1\n\t"
      "v_mfma_f32_16x16x32_bf16 v[76:79], a[12:15], v[36:39], v[76:79]\n\t"
      "ds_read_b128 v[20:23], %[ab] offset:25600\n\t"
      "s_waitcnt lgkmcnt(3)\n\t"
      "v_mfma_f32_16x16x32_bf16 v[80:83], a[8:11], v[40:43], v[80:83]\n\t"
      "v_mfma_f32_16x16x32_bf16 v[84:87], a[12:15], v[40:43], v[84:87]\n\t"
      "ds_read_b128 v[24:27], %[ab] offset:26624\n\t"
      "s_waitcnt lgkmcnt(3)\n\t"
      "v_mfma_f32_16x16x32_bf16 v[88:91], a[8:11], v[44:47], v[88:91]\n\t"
      "s_add_u32 %[t0], %[sg], 262144\n\t"
      "buffer_load_dwordx4 a[4:7], %[vl], %[rs], %[t0] offen offset:1024 sc1\n\t"
      "v_mfma_f32_16x16x32_bf16 v[92:95], a[12:15], v[44:47], v[92:95]\n\t"
      "ds_read_b128 v[28:31], %[ab] offset:27648\n\t"
      "s_waitcnt vmcnt(29) lgkmcnt(3)\n\t"
      "v_mfma_f32_16x16x32_bf16 v[64:67], a[16:19], v[16:19], v[64:67]\n\t"
      "s_waitcnt vmcnt(28)\n\t"
      "v_mfma_f32_16x16x32_bf16 v[68:71], a[20:23], v[16:19], v[68:71]\n\t"
      "ds_read_b128 v[32:35], %[ab] offset:28672\n\t"
      "s_waitcnt lgkmcnt(3)\n\t"
      "v_mfma_f32_16x16x32_bf16 v[72:75], a[16:19], v[20:23], v[72:75]\n\t"
      "s_add_u32 %[t0], %[sg], 262144\n\t"
      "buffer_load_dwordx4 a[8:11], %[vl], %[rs], %[t0] offen offset:2048 sc1\n\t"
      "v_mfma_f32_16x16x32_bf16 v[76:79], a[20:23], v[20:23], v[76:79]\n\t"
      "ds_read_b128 v[36:39], %[ab] offset:29696\n\t"
      "s_waitcnt lgkmcnt(3)\n\t"
      "v_mfma_f32_16x16x32_bf16 v[80:83], a[16:19], v[24:27], v[80:83]\n\t"
      "v_mfma_f32_16x16x32_bf16 v[84:87], a[20:23], v[24:27], v[84:87]\n\t"
      "ds_read_b128 v[40:43], %[ab] offset:30720\n\t"
      "s_waitcnt lgkmcnt(3)\n\t"
      "v_mfma_f32_16x16x32_bf16 v[88:91], a[16:19], v[28:31], v[88:91]\n\t"
      "s_add_u32 %[t0], %[sg], 262144\n\t"
      "buffer_load_dwordx4 a[12:15], %[vl], %[rs], %[t0] offen offset:3072 sc1\n\t"
      "v_mfma_f32_16x16x32_bf16 v[92:95], a[20:23], v[28:31], v[92:95]\n\t"
      "ds_read_b128 v[44:47], %[ab] offset:31744\n\t"
      "s_waitcnt vmcnt(29) lgkmcnt(3)\n\t"
      "v_mfma_f32_16x16x32_bf16 v[64:67], a[24:27], v[32:35], v[64:67]\n\t"
      "s_waitcnt vmcnt(28)\n\t"
      "v_mfma_f32_16x16x32_bf16 v[68:71], a[28:31], v[32:35], v[68:71]\n\t"
      "ds_read_b128 v[16:19], %[ab] offset:32768\n\t"
      "s_waitcnt lgkmcnt(3)\n\t"
      "v_mfma_f32_16x16x32_bf16 v[72:75], a[24:27], v[36:39], v[72:75]\n\t"
      "s_add_u32 %[t0], %[sg], 266240\n\t"
      "buffer_load_dwordx4 a[16:19], %[vl], %[rs], %[t0] offen offset:0 sc1\n\t"
      "v_mfma_f32_16x16x32_bf16 v[76:79], a[28:31], v[36:39], v[76:79]\n\t"
      "ds_read_b128 v[20:23], %[ab] offset:33792\n\t"
      "s_waitcnt lgkmcnt(3)\n\t"
      "v_mfma_f32_16x16x32_bf16 v[80:83], a[24:27], v[40:43], v[80:83]\n\t"
      "v_mfma_f32_16x16x32_bf16 v[84:87], a[28:31], v[40:43], v[84:87]\n\t"
      "ds_read_b128 v[24:27], %[ab] offset:34816\n\t"
      "s_waitcnt lgkmcnt(3)\n\t"
      "v_mfma_f32_16x16x32_bf16 v[88:91], a[24:27], v[44:47], v[88:91]\n\t"
      "s_add_u32 %[t0], %[sg], 266240\n\t"
      "buffer_load_dwordx4 a[20:23], %[vl], %[rs], %[t0] offen offset:1024 sc1\n\t"
      "v_mfma_f32_16x16x32_bf16 v[92:95], a[28:31], v[44:47], v[92:95]\n\t"
      "ds_read_b128 v[28:31], %[ab] offset:35840\n\t"
      "s_waitcnt vmcnt(29) lgkmcnt(3)\n\t"
      "v_mfma_f32_16x16x32_bf16 v[64:67], a[32:35], v[16:19], v[64:67]\n\t"
      "s_waitcnt vmcnt(28)\n\t"
      "v_mfma_f32_16x16x32_bf16 v[68:71], a[36:39], v[16:19], v[68:71]\n\t"
      "s_waitcnt vmcnt(6)\n\t"
      "s_barrier\n\t"
      "ds_read_b128 v[32:35], %[ab] offset:36864\n\t"
      "s_waitcnt lgkmcnt(3)\n\t"
      "v_mfma_f32_16x16x32_bf16 v[72:75], a[32:35], v[20:23], v[72:75]\n\t"
      "s_add_u32 %[t0], %[sg], 266240\n\t"
      "buffer_load_dwordx4 a[24:27], %[vl], %[rs], %[t0] offen offset:2048 sc1\n\t"
      "v_mfma_f32_16x16x32_bf16 v[76:79], a[36:39], v[20:23], v[76:79]\n\t"
      "ds_read_b128 v[36:39], %[ab] offset:37888\n\t"
      "s_waitcnt lgkmcnt(3)\n\t"
      "v_mfma_f32_16x16x32_bf16 v[80:83], a[32:35], v[24:27], v[80:83]\n\t"
      "v_mfma_f32_16x16x32_bf16 v[84:87], a[36:39], v[24:27], v[84:87]\n\t"
      "ds_read_b128 v[40:43], %[ab] offset:38912\n\t"
      "s_waitcnt lgkmcnt(3)\n\t"
      "v_mfma_f32_16x16x32_bf16 v[88:91], a[32:35], v[28:31], v[88:91]\n\t"
      "s_add_u32 %[t0], %[sg], 266240\n\t"
      "buffer_load_dwordx4 a[28:31], %[vl], %[rs], %[t0] offen offset:3072 sc1\n\t"
      "v_mfma_f32_16x16x32_bf16 v[92:95], a[36:39], v[28:31], v[92:95]\n\t"
      "ds_read_b128 v[44:47], %[ab] offset:39936\n\t"
      "s_waitcnt vmcnt(29) lgkmcnt(3)\n\t"
      "v_mfma_f32_16x16x32_bf16 v[64:67], a[40:43], v[32:35], v[64:67]\n\t"
      "s_waitcnt vmcnt(28)\n\t"
      "v_mfma_f32_16x16x32_bf16 v[68:71], a[44:47], v[32:35], v[68:71]\n\t"
      "ds_read_b128 v[16:19], %[ab] offset:40960\n\t"
      "s_waitcnt lgkmcnt(3)\n\t"
      "v_mfma_f32_16x16x32_bf16 v[72:75], a[40:43], v[36:39], v[72:75]\n\t"
      "s_add_u32 %[t0], %[sg], 270336\n\t"
      "buffer_load_dwordx4 a[32:35], %[vl], %[rs], %[t0] offen offset:0 sc1\n\t"
      "v_mfma_f32_16x16x32_bf16 v[76:79], a[44:47], v[36:39], v[76:79]\n\t"
      "ds_read_b128 v[20:23], %[ab] offset:41984\n\t"
      "s_waitcnt lgkmcnt(3)\n\t"
      "v_mfma_f32_16x16x32_bf16 v[80:83], a[40:43], v[40:43], v[80:83]\n\t"
      "v_mfma_f32_16x16x32_bf16 v[84:87], a[44:47], v[40:43], v[84:87]\n\t"
      "ds_read_b128 v[24:27], %[ab] offset:43008\n\t"
      "s_waitcnt lgkmcnt(3)\n\t"
      "v_mfma_f32_16x16x32_bf16 v[88:91], a[40:43], v[44:47], v[88:91]\n\t"
      "s_add_u32 %[t0], %[sg], 270336\n\t"
      "buffer_load_dwordx4 a[36:39], %[vl], %[rs], %[t0] offen offset:1024 sc1\n\t"
      "v_mfma_f32_16x16x32_bf16 v[92:95], a[44:47], v[44:47], v[92:95]\n\t"
      "ds_read_b128 v[28:31], %[ab] offset:44032\n\t"
      "s_waitcnt vmcnt(29) lgkmcnt(3)\n\t"
      "v_mfma_f32_16x16x32_bf16 v[64:67], a[48:51], v[16:19], v[64:67]\n\t"
      "s_waitcnt vmcnt(28)\n\t"
      "v_mfma_f32_16x16x32_bf16 v[68:71], a[52:55], v[16:19], v[68:71]\n\t"
      "ds_read_b128 v[32:35], %[ab] offset:45056\n\t"
      "s_waitcnt lgkmcnt(3)\n\t"
      "v_mfma_f32_16x16x32_bf16 v[72:75], a[48:51], v[20:23], v[72:75]\n\t"
      "s_add_u32 %[t0], %[sg], 270336\n\t"
      "buffer_load_dwordx4 a[40:43], %[vl], %[rs], %[t0] offen offset:2048 sc1\n\t"
      "v_mfma_f32_16x16x32_bf16 v[76:79], a[52:55], v[20:23], v[76:79]\n\t"
      "ds_read_b128 v[36:39], %[ab] offset:46080\n\t"
      "s_waitcnt lgkmcnt(3)\n\t"
      "v_mfma_f32_16x16x32_bf16 v[80:83], a[48:51], v[24:27], v[80:83]\n\t"
      "v_mfma_f32_16x16x32_bf16 v[84:87], a[52:55], v[24:27], v[84:87]\n\t"
      "ds_read_b128 v[40:43], %[ab] offset:47104\n\t"
      "s_waitcnt lgkmcnt(3)\n\t"
      "v_mfma_f32_16x16x32_bf16 v[88:91], a[48:51], v[28:31], v[88:91]\n\t"
      "s_add_u32 %[t0], %[sg], 270336\n\t"
      "buffer_load_dwordx4 a[44:47], %[vl], %[rs], %[t0] offen offset:3072 sc1\n\t"
      "v_mfma_f32_16x16x32_bf16 v[92:95], a[52:55], v[28:31], v[92:95]\n\t"
      "ds_read_b128 v[44:47], %[ab] offset:48128\n\t"
      "s_waitcnt vmcnt(29) lgkmcnt(3)\n\t"
      "v_mfma_f32_16x16x32_bf16 v[64:67], a[56:59], v[32:35], v[64:67]\n\t"
      "s_waitcnt vmcnt(28)\n\t"
      "v_mfma_f32_16x16x32_bf16 v[68:71], a[60:63], v[32:35], v[68:71]\n\t"
      "ds_read_b128 v[16:19], %[ab] offset:49152\n\t"
      "s_waitcnt lgkmcnt(3)\n\t"
      "v_mfma_f32_16x16x32_bf16 v[72:75], a[56:59], v[36:39], v[72:75]\n\t"
      "s_add_u32 %[t0], %[sg], 274432\n\t"
      "buffer_load_dwordx4 a[48:51], %[vl], %[rs], %[t0] offen offset:0 sc1\n\t"
      "v_mfma_f32_16x16x32_bf16 v[76:79], a[60:63], v[36:39], v[76:79]\n\t"
      "ds_read_b128 v[20:23], %[ab] offset:50176\n\t"
      "s_waitcnt lgkmcnt(3)\n\t"
      "v_mfma_f32_16x16x32_bf16 v[80:83], a[56:59], v[40:43], v[80:83]\n\t"
      "v_mfma_f32_16x16x32_bf16 v[84:87], a[60:63], v[40:43], v[84:87]\n\t"
      "ds_read_b128 v[24:27], %[ab] offset:51200\n\t"
      "s_waitcnt lgkmcnt(3)\n\t"
      "v_mfma_f32_16x16x32_bf16 v[88:91], a[56:59], v[44:47], v[88:91]\n\t"
      "s_add_u32 %[t0], %[sg], 274432\n\t"
      "buffer_load_dwordx4 a[52:55], %[vl], %[rs], %[t0] offen offset:1024 sc1\n\t"
      "v_mfma_f32_16x16x32_bf16 v[92:95], a[60:63], v[44:47], v[92:95]\n\t"
      "ds_read_b128 v[28:31], %[ab] offset:52224\n\t"
      "s_waitcnt vmcnt(29) lgkmcnt(3)\n\t"
      "v_mfma_f32_16x16x32_bf16 v[64:67], a[64:67], v[16:19], v[64:67]\n\t"
      "s_waitcnt vmcnt(28)\n\t"
      "v_mfma_f32_16x16x32_bf16 v[68:71], a[68:71], v[16:19], v[68:71]\n\t"
      "ds_read_b128 v[32:35], %[ab] offset:53248\n\t"
      "s_waitcnt lgkmcnt(3)\n\t"
      "v_mfma_f32_16x16x32_bf16 v[72:75], a[64:67], v[20:23], v[72:75]\n\t"
      "s_add_u32 %[t0], %[sg], 274432\n\t"
      "buffer_load_dwordx4 a[56:59], %[vl], %[rs], %[t0] offen offset:2048 sc1\n\t"
      "v_mfma_f32_16x16x32_bf16 v[76:79], a[68:71], v[20:23], v[76:79]\n\t"
      "ds_read_b128 v[36:39], %[ab] offset:54272\n\t"
      "s_waitcnt lgkmcnt(3)\n\t"
      "v_mfma_f32_16x16x32_bf16 v[80:83], a[64:67], v[24:27], v[80:83]\n\t"
      "v_mfma_f32_16x16x32_bf16 v[84:87], a[68:71], v[24:27], v[84:87]\n\t"
      "ds_read_b128 v[40:43], %[ab] offset:55296\n\t"
      "s_waitcnt lgkmcnt(3)\n\t"
      "v_mfma_f32_16x16x32_bf16 v[88:91], a[64:67], v[28:31], v[88:91]\n\t"
      "s_add_u32 %[t0], %[sg], 274432\n\t"
      "buffer_load_dwordx4 a[60:63], %[vl], %[rs], %[t0] offen offset:3072 sc1\n\t"
      "v_mfma_f32_16x16x32_bf16 v[92:95], a[68:71], v[28:31], v[92:95]\n\t"
      "ds_read_b128 v[44:47], %[ab] offset:56320\n\t"
      "s_waitcnt vmcnt(29) lgkmcnt(3)\n\t"
      "v_mfma_f32_16x16x32_bf16 v[64:67], a[72:75], v[32:35], v[64:67]\n\t"
      "s_waitcnt vmcnt(28)\n\t"
      "v_mfma_f32_16x16x32_bf16 v[68:71], a[76:79], v[32:35], v[68:71]\n\t"
      "ds_read_b128 v[16:19], %[ab] offset:57344\n\t"
      "s_waitcnt lgkmcnt(3)\n\t"
      "v_mfma_f32_16x16x32_bf16 v[72:75], a[72:75], v[36:39], v[72:75]\n\t"
      "s_add_u32 %[t0], %[sg], 278528\n\t"
      "buffer_load_dwordx4 a[64:67], %[vl], %[rs], %[t0] offen offset:0 sc1\n\t"
      "v_mfma_f32_16x16x32_bf16 v[76:79], a[76:79], v[36:39], v[76:79]\n\t"
      "ds_read_b128 v[20:23], %[ab] offset:58368\n\t"
      "s_waitcnt lgkmcnt(3)\n\t"
      "v_mfma_f32_16x16x32_bf16 v[80:83], a[72:75], v[40:43], v[80:83]\n\t"
      "v_mfma_f32_16x16x32_bf16 v[84:87], a[76:79], v[40:43], v[84:87]\n\t"
      "ds_read_b128 v[24:27], %[ab] offset:59392\n\t"
      "s_waitcnt lgkmcnt(3)\n\t"
      "v_mfma_f32_16x16x32_bf16 v[88:91], a[72:75], v[44:47], v[88:91]\n\t"
      "s_add_u32 %[t0], %[sg], 278528\n\t"
      "buffer_load_dwordx4 a[68:71], %[vl], %[rs], %[t0] offen offset:1024 sc1\n\t"
      "v_mfma_f32_16x16x32_bf16 v[92:95], a[76:79], v[44:47], v[92:95]\n\t"
      "ds_read_b128 v[28:31], %[ab] offset:60416\n\t"
      "s_waitcnt vmcnt(29) lgkmcnt(3)\n\t"
      "v_mfma_f32_16x16x32_bf16 v[64:67], a[80:83], v[16:19], v[64:67]\n\t"
      "s_waitcnt vmcnt(28)\n\t"
      "v_mfma_f32_16x16x32_bf16 v[68:71], a[84:87], v[16:19], v[68:71]\n\t"
      "ds_read_b128 v[32:35], %[ab] offset:61440\n\t"
      "s_waitcnt lgkmcnt(3)\n\t"
      "v_mfma_f32_16x16x32_bf16 v[72:75], a[80:83], v[20:23], v[72:75]\n\t"
      "s_add_u32 %[t0], %[sg], 278528\n\t"
      "buffer_load_dwordx4 a[72:75], %[vl], %[rs], %[t0] offen offset:2048 sc1\n\t"
      "v_mfma_f32_16x16x32_bf16 v[76:79], a[84:87], v[20:23], v[76:79]\n\t"
      "ds_read_b128 v[36:39], %[ab] offset:62464\n\t"
      "s_waitcnt lgkmcnt(3)\n\t"
      "v_mfma_f32_16x16x32_bf16 v[80:83], a[80:83], v[24:27], v[80:83]\n\t"
      "v_mfma_f32_16x16x32_bf16 v[84:87], a[84:87], v[24:27], v[84:87]\n\t"
      "ds_read_b128 v[40:43], %[ab] offset:63488\n\t"
      "s_waitcnt lgkmcnt(3)\n\t"
      "v_mfma_f32_16x16x32_bf16 v[88:91], a[80:83], v[28:31], v[88:91]\n\t"
      "s_add_u32 %[t0], %[sg], 278528\n\t"
      "buffer_load_dwordx4 a[76:79], %[vl], %[rs], %[t0] offen offset:3072 sc1\n\t"
      "v_mfma_f32_16x16x32_bf16 v[92:95], a[84:87], v[28:31], v[92:95]\n\t"
      "ds_read_b128 v[44:47], %[ab] offset:64512\n\t"
      "s_waitcnt vmcnt(29) lgkmcnt(3)\n\t"
      "v_mfma_f32_16x16x32_bf16 v[64:67], a[88:91], v[32:35], v[64:67]\n\t"
      "s_waitcnt vmcnt(28)\n\t"
      "v_mfma_f32_16x16x32_bf16 v[68:71], a[92:95], v[32:35], v[68:71]\n\t"
      "ds_read_b128 v[16:19], %[ab] offset:0\n\t"
      "s_waitcnt lgkmcnt(3)\n\t"
      "v_mfma_f32_16x16x32_bf16 v[72:75], a[88:91], v[36:39], v[72:75]\n\t"
      "s_add_u32 %[t0], %[sg], 282624\n\t"
      "buffer_load_dwordx4 a[80:83], %[vl], %[rs], %[t0] offen offset:0 sc1\n\t"
      "v_mfma_f32_16x16x32_bf16 v[76:79], a[92:95], v[36:39], v[76:79]\n\t"
      "ds_read_b128 v[20:23], %[ab] offset:1024\n\t"
      "s_waitcnt lgkmcnt(3)\n\t"
      "v_mfma_f32_16x16x32_bf16 v[80:83], a[88:91], v[40:43], v[80:83]\n\t"
      "v_mfma_f32_16x16x32_bf16 v[84:87], a[92:95], v[40:43], v[84:87]\n\t"
      "ds_read_b128 v[24:27], %[ab] offset:2048\n\t"
      "s_waitcnt lgkmcnt(3)\n\t"
      "v_mfma_f32_16x16x32_bf16 v[88:91], a[88:91], v[44:47], v[88:91]\n\t"
      "s_add_u32 %[t0], %[sg], 282624\n\t"
      "buffer_load_dwordx4 a[84:87], %[vl], %[rs], %[t0] offen offset:1024 sc1\n\t"
      "v_mfma_f32_16x16x32_bf16 v[92:95], a[92:95], v[44:47], v[92:95]\n\t"
      "ds_read_b128 v[28:31], %[ab] offset:3072\n\t"
      "s_waitcnt vmcnt(21) lgkmcnt(3)\n\t"
      "v_mfma_f32_16x16x32_bf16 a[96:99], a[0:3], v[16:19], 0\n\t"
      "s_waitcnt vmcnt(20)\n\t"
      "v_mfma_f32_16x16x32_bf16 a[100:103], a[4:7], v[16:19], 0\n\t"
      "ds_read_b128 v[32:35], %[ab] offset:4096\n\t"
      "s_waitcnt lgkmcnt(3)\n\t"
      "v_mfma_f32_16x16x32_bf16 a[104:107], a[0:3], v[20:23], 0\n\t"
      "s_add_u32 %[t0], %[sg], 282624\n\t"
      "buffer_load_dwordx4 a[88:91], %[vl], %[rs], %[t0] offen offset:2048 sc1\n\t"
      "v_mfma_f32_16x16x32_bf16 a[108:111], a[4:7], v[20:23], 0\n\t"
      "ds_read_b128 v[36:39], %[ab] offset:5120\n\t"
      "s_waitcnt lgkmcnt(3)\n\t"
      "v_mfma_f32_16x16x32_bf16 a[112:115], a[0:3], v[24:27], 0\n\t"
      "v_mfma_f32_16x16x32_bf16 a[116:119], a[4:7], v[24:27], 0\n\t"
      "ds_read_b128 v[40:43], %[ab] offset:6144\n\t"
      "s_waitcnt lgkmcnt(3)\n\t"
      "v_mfma_f32_16x16x32_bf16 a[120:123], a[0:3], v[28:31], 0\n\t"
      "s_add_u32 %[t0], %[sg], 282624\n\t"
      "buffer_load_dwordx4 a[92:95], %[vl], %[rs], %[t0] offen offset:3072 sc1\n\t"
      "v_mfma_f32_16x16x32_bf16 a[124:127], a[4:7], v[28:31], 0\n\t"
      "ds_read_b128 v[44:47], %[ab] offset:7168\n\t"
      "s_waitcnt vmcnt(21) lgkmcnt(3)\n\t"
      "v_mfma_f32_16x16x32_bf16 a[96:99], a[8:11], v[32:35], a[96:99]\n\t"
      "v_mul_f32 v48, v64, v64\n\t"
      "v_mul_f32 v50, v65, v65\n\t"
      "s_waitcnt vmcnt(20)\n\t"
      "v_mfma_f32_16x16x32_bf16 a[100:103], a[12:15], v[32:35], a[100:103]\n\t"
      "ds_read_b128 v[16:19], %[ab] offset:8192\n\t"
      "v_mul_f32 v49, v64, v68\n\t"
      "v_mul_f32 v51, v65, v69\n\t"
      "s_waitcnt lgkmcnt(3)\n\t"
      "v_mfma_f32_16x16x32_bf16 a[104:107], a[8:11], v[36:39], a[104:107]\n\t"
      "v_fma_f32 v48, v48, %[c1], %[c2]\n\t"
      "v_fma_f32 v50, v50, %[c1], %[c2]\n\t"
      "s_add_u32 %[t0], %[sg], 286720\n\t"
      "buffer_load_dwordx4 a[0:3], %[vl], %[rs], %[t0] offen offset:0 sc1\n\t"
      "v_mfma_f32_16x16x32_bf16 a[108:111], a[12:15], v[36:39], a[108:111]\n\t"
      "ds_read_b128 v[20:23], %[ab] offset:9216\n\t"
      "v_mul_f32 v48, v64, v48\n\t"
      "v_mul_f32 v50, v65, v50\n\t"
      "s_waitcnt lgkmcnt(3)\n\t"
      "v_mfma_f32_16x16x32_bf16 a[112:115], a[8:11], v[40:43], a[112:115]\n\t"
      "v_exp_f32 v48, v48\n\t"
      "v_mfma_f32_16x16x32_bf16 a[116:119], a[12:15], v[40:43], a[116:119]\n\t"
      "ds_read_b128 v[24:27], %[ab] offset:10240\n\t"
      "v_exp_f32 v50, v50\n\t"
      "s_waitcnt lgkmcnt(3)\n\t"
      "v_mfma_f32_16x16x32_bf16 a[120:123], a[8:11], v[44:47], a[120:123]\n\t"
      "v_add_f32 v48, 1.0, v48\n\t"
      "v_add_f32 v50, 1.0, v50\n\t"
      "s_add_u32 %[t0], %[sg], 286720\n\t"
      "buffer_load_dwordx4 a[4:7], %[vl], %[rs], %[t0] offen offset:1024 sc1\n\t"
      "v_mfma_f32_16x16x32_bf16 a[124:127], a[12:15], v[44:47], a[124:127]\n\t"
      "ds_read_b128 v[28:31], %[ab] offset:11264\n\t"
      "v_rcp_f32 v48, v48\n\t"
      "s_waitcnt vmcnt(21) lgkmcnt(3)\n\t"
      "v_mfma_f32_16x16x32_bf16 a[96:99], a[16:19], v[16:19], a[96:99]\n\t"
      "v_rcp_f32 v50, v50\n\t"
      "s_waitcnt vmcnt(20)\n\t"
      "v_mfma_f32_16x16x32_bf16 a[100:103], a[20:23], v[16:19], a[100:103]\n\t"
      "ds_read_b128 v[32:35], %[ab] offset:12288\n\t"
      "v_mul_f32 v48, v49, v48\n\t"
      "v_mul_f32 v50, v51, v50\n\t"
      "s_waitcnt lgkmcnt(3)\n\t"
      "v_mfma_f32_16x16x32_bf16 a[104:107], a[16:19], v[20:23], a[104:107]\n\t"
      "v_cvt_pk_bf16_f32 v52, v48, v50\n\t"
      "s_add_u32 %[t0], %[sg], 286720\n\t"
      "buffer_load_dwordx4 a[8:11], %[vl], %[rs], %[t0] offen offset:2048 sc1\n\t"
      "v_mfma_f32_16x16x32_bf16 a[108:111], a[20:23], v[20:23], a[108:111]\n\t"
      "ds_read_b128 v[36:39], %[ab] offset:13312\n\t"
      "v_mul_f32 v48, v66, v66\n\t"
      "v_mul_f32 v50, v67, v67\n\t"
      "s_waitcnt lgkmcnt(3)\n\t"
      "v_mfma_f32_16x16x32_bf16 a[112:115], a[16:19], v[24:27], a[112:115]\n\t"
      "v_mul_f32 v49, v66, v70\n\t"
      "v_mul_f32 v51, v67, v71\n\t"
      "v_mfma_f32_16x16x32_bf16 a[116:119], a[20:23], v[24:27], a[116:119]\n\t"
      "ds_read_b128 v[40:43], %[ab] offset:14336\n\t"
      "v_fma_f32 v48, v48, %[c1], %[c2]\n\t"
      "v_fma_f32 v50, v50, %[c1], %[c2]\n\t"
      "s_waitcnt lgkmcnt(3)\n\t"
      "v_mfma_f32_16x16x32_bf16 a[120:123], a[16:19], v[28:31], a[120:123]\n\t"
      "v_mul_f32 v48, v66, v48\n\t"
      "v_mul_f32 v50, v67, v50\n\t"
      "s_add_u32 %[t0], %[sg], 286720\n\t"
      "buffer_load_dwordx4 a[12:15], %[vl], %[rs], %[t0] offen offset:3072 sc1\n\t"
      "v_mfma_f32_16x16x32_bf16 a[124:127], a[20:23], v[28:31], a[124:127]\n\t"
      "ds_read_b128 v[44:47], %[ab] offset:15360\n\t"
      "v_exp_f32 v48, v48\n\t"
      "s_waitcnt vmcnt(21) lgkmcnt(3)\n\t"
      "v_mfma_f32_16x16x32_bf16 a[96:99], a[24:27], v[32:35], a[96:99]\n\t"
      "v_exp_f32 v50, v50\n\t"
      "s_waitcnt vmcnt(20)\n\t"
      "v_mfma_f32_16x16x32_bf16 a[100:103], a[28:31], v[32:35], a[100:103]\n\t"
      "ds_read_b128 v[16:19], %[ab] offset:16384\n\t"
      "v_add_f32 v48, 1.0, v48\n\t"
      "v_add_f32 v50, 1.0, v50\n\t"
      "s_waitcnt lgkmcnt(3)\n\t"
      "v_mfma_f32_16x16x32_bf16 a[104:107], a[24:27], v[36:39], a[104:107]\n\t"
      "v_rcp_f32 v48, v48\n\t"
      "s_add_u32 %[t0], %[sg], 290816\n\t"
      "buffer_load_dwordx4 a[16:19], %[vl], %[rs], %[t0] offen offset:0 sc1\n\t"
      "v_mfma_f32_16x16x32_bf16 a[108:111], a[28:31], v[36:39], a[108:111]\n\t"
      "ds_read_b128 v[20:23], %[ab] offset:17408\n\t"
      "v_rcp_f32 v50, v50\n\t"
      "s_waitcnt lgkmcnt(3)\n\t"
      "v_mfma_f32_16x16x32_bf16 a[112:115], a[24:27], v[40:43], a[112:115]\n\t"
      "v_mul_f32 v48, v49, v48\n\t"
      "v_mul_f32 v50, v51, v50\n\t"
      "v_mfma_f32_16x16x32_bf16 a[116:119], a[28:31], v[40:43], a[116:119]\n\t"
      "ds_read_b128 v[24:27], %[ab] offset:18432\n\t"
      "v_cvt_pk_bf16_f32 v53, v48, v50\n\t"
      "s_waitcnt lgkmcnt(3)\n\t"
      "v_mfma_f32_16x16x32_bf16 a[120:123], a[24:27], v[44:47], a[120:123]\n\t"
      "ds_write_b64 %[aw], v[52:53] offset:0\n\t"
      "s_add_u32 %[t0], %[sg], 290816\n\t"
      "buffer_load_dwordx4 a[20:23], %[vl], %[rs], %[t0] offen offset:1024 sc1\n\t"
      "v_mfma_f32_16x16x32_bf16 a[124:127], a[28:31], v[44:47], a[124:127]\n\t"
      "ds_read_b128 v[28:31], %[ab] offset:19456\n\t"
      "v_mul_f32 v48, v72, v72\n\t"
      "v_mul_f32 v50, v73, v73\n\t"
      "s_waitcnt vmcnt(21) lgkmcnt(4)\n\t"
      "v_mfma_f32_16x16x32_bf16 a[96:99], a[32:35], v[16:19], a[96:99]\n\t"
      "v_mul_f32 v49, v72, v76\n\t"
      "v_mul_f32 v51, v73, v77\n\t"
      "s_waitcnt vmcnt(20)\n\t"
      "v_mfma_f32_16x16x32_bf16 a[100:103], a[36:39], v[16:19], a[100:103]\n\t"
      "ds_read_b128 v[32:35], %[ab] offset:20480\n\t"
      "v_fma_f32 v48, v48, %[c1], %[c2]\n\t"
      "v_fma_f32 v50, v50, %[c1], %[c2]\n\t"
      "s_waitcnt lgkmcnt(4)\n\t"
      "v_mfma_f32_16x16x32_bf16 a[104:107], a[32:35], v[20:23], a[104:107]\n\t"
      "v_mul_f32 v48, v72, v48\n\t"
      "v_mul_f32 v50, v73, v50\n\t"
      "s_add_u32 %[t0], %[sg], 290816\n\t"
      "buffer_load_dwordx4 a[24:27], %[vl], %[rs], %[t0] offen offset:2048 sc1\n\t"
      "v_mfma_f32_16x16x32_bf16 a[108:111], a[36:39], v[20:23], a[108:111]\n\t"
      "ds_read_b128 v[36:39], %[ab] offset:21504\n\t"
      "v_exp_f32 v48, v48\n\t"
      "s_waitcnt lgkmcnt(4)\n\t"
      "v_mfma_f32_16x16x32_bf16 a[112:115], a[32:35], v[24:27], a[112:115]\n\t"
      "v_exp_f32 v50, v50\n\t"
      "v_mfma_f32_16x16x32_bf16 a[116:119], a[36:39], v[24:27], a[116:119]\n\t"
      "ds_read_b128 v[40:43], %[ab] offset:22528\n\t"
      "v_add_f32 v48, 1.0, v48\n\t"
      "v_add_f32 v50, 1.0, v50\n\t"
      "s_waitcnt lgkmcnt(3)\n\t"
      "v_mfma_f32_16x16x32_bf16 a[120:123], a[32:35], v[28:31], a[120:123]\n\t"
      "v_rcp_f32 v48, v48\n\t"
      "s_add_u32 %[t0], %[sg], 290816\n\t"
      "buffer_load_dwordx4 a[28:31], %[vl], %[rs], %[t0] offen offset:3072 sc1\n\t"
      "v_mfma_f32_16x16x32_bf16 a[124:127], a[36:39], v[28:31], a[124:127]\n\t"
      "ds_read_b128 v[44:47], %[ab] offset:23552\n\t"
      "v_rcp_f32 v50, v50\n\t"
      "s_waitcnt vmcnt(21) lgkmcnt(3)\n\t"
      "v_mfma_f32_16x16x32_bf16 a[96:99], a[40:43], v[32:35], a[96:99]\n\t"
      "v_mul_f32 v48, v49, v48\n\t"
      "v_mul_f32 v50, v51, v50\n\t"
      "s_waitcnt vmcnt(20)\n\t"
      "v_mfma_f32_16x16x32_bf16 a[100:103], a[44:47], v[32:35], a[100:103]\n\t"
      "ds_read_b128 v[16:19], %[ab] offset:24576\n\t"
      "v_cvt_pk_bf16_f32 v54, v48, v50\n\t"
      "s_waitcnt lgkmcnt(3)\n\t"
      "v_mfma_f32_16x16x32_bf16 a[104:107], a[40:43], v[36:39], a[104:107]\n\t"
      "v_mul_f32 v48, v74, v74\n\t"
      "v_mul_f32 v50, v75, v75\n\t"
      "s_add_u32 %[t0], %[sd], 0\n\t"
      "buffer_load_dwordx4 a[32:35], %[vl], %[rs], %[t0] offen offset:0 sc1\n\t"
      "v_mfma_f32_16x16x32_bf16 a[108:111], a[44:47], v[36:39], a[108:111]\n\t"
      "ds_read_b128 v[20:23], %[ab] offset:25600\n\t"
      "v_mul_f32 v49, v74, v78\n\t"
      "v_mul_f32 v51, v75, v79\n\t"
      "s_waitcnt lgkmcnt(3)\n\t"
      "v_mfma_f32_16x16x32_bf16 a[112:115], a[40:43], v[40:43], a[112:115]\n\t"
      "v_fma_f32 v48, v48, %[c1], %[c2]\n\t"
      "v_fma_f32 v50, v50, %[c1], %[c2]\n\t"
      "v_mfma_f32_16x16x32_bf16 a[116:119], a[44:47], v[40:43], a[116:119]\n\t"
      "ds_read_b128 v[24:27], %[ab] offset:26624\n\t"
      "v_mul_f32 v48, v74, v48\n\t"
      "v_mul_f32 v50, v75, v50\n\t"
      "s_waitcnt lgkmcnt(3)\n\t"
      "v_mfma_f32_16x16x32_bf16 a[120:123], a[40:43], v[44:47], a[120:123]\n\t"
      "v_exp_f32 v48, v48\n\t"
      "s_add_u32 %[t0], %[sd], 0\n\t"
      "buffer_load_dwordx4 a[36:39], %[vl], %[rs], %[t0] offen offset:1024 sc1\n\t"
      "v_mfma_f32_16x16x32_bf16 a[124:127], a[44:47], v[44:47], a[124:127]\n\t"
      "ds_read_b128 v[28:31], %[ab] offset:27648\n\t"
      "v_exp_f32 v50, v50\n\t"
      "s_waitcnt vmcnt(21) lgkmcnt(3)\n\t"
      "v_mfma_f32_16x16x32_bf16 a[96:99], a[48:51], v[16:19], a[96:99]\n\t"
      "v_add_f32 v48, 1.0, v48\n\t"
      "v_add_f32 v50, 1.0, v50\n\t"
      "s_waitcnt vmcnt(20)\n\t"
      "v_mfma_f32_16x16x32_bf16 a[100:103], a[52:55], v[16:19], a[100:103]\n\t"
      "ds_read_b128 v[32:35], %[ab] offset:28672\n\t"
      "v_rcp_f32 v48, v48\n\t"
      "s_waitcnt lgkmcnt(3)\n\t"
      "v_mfma_f32_16x16x32_bf16 a[104:107], a[48:51], v[20:23], a[104:107]\n\t"
      "v_rcp_f32 v50, v50\n\t"
      "s_add_u32 %[t0], %[sd], 0\n\t"
      "buffer_load_dwordx4 a[40:43], %[vl], %[rs], %[t0] offen offset:2048 sc1\n\t"
      "v_mfma_f32_16x16x32_bf16 a[108:111], a[52:55], v[20:23], a[108:111]\n\t"
      "ds_read_b128 v[36:39], %[ab] offset:29696\n\t"
      "v_mul_f32 v48, v49, v48\n\t"
      "v_mul_f32 v50, v51, v50\n\t"
      "s_waitcnt lgkmcnt(3)\n\t"
      "v_mfma_f32_16x16x32_bf16 a[112:115], a[48:51], v[24:27], a[112:115]\n\t"
      "v_cvt_pk_bf16_f32 v55, v48, v50\n\t"
      "v_mfma_f32_16x16x32_bf16 a[116:119], a[52:55], v[24:27], a[116:119]\n\t"
      "ds_read_b128 v[40:43], %[ab] offset:30720\n\t"
      "ds_write_b64 %[aw], v[54:55] offset:1024\n\t"
      "s_waitcnt lgkmcnt(4)\n\t"
      "v_mfma_f32_16x16x32_bf16 a[120:123], a[48:51], v[28:31], a[120:123]\n\t"
      "v_mul_f32 v48, v80, v80\n\t"
      "v_mul_f32 v50, v81, v81\n\t"
      "s_add_u32 %[t0], %[sd], 0\n\t"
      "buffer_load_dwordx4 a[44:47], %[vl], %[rs], %[t0] offen offset:3072 sc1\n\t"
      "v_mfma_f32_16x16x32_bf16 a[124:127], a[52:55], v[28:31], a[124:127]\n\t"
      "ds_read_b128 v[44:47], %[ab] offset:31744\n\t"
      "v_mul_f32 v49, v80, v84\n\t"
      "v_mul_f32 v51, v81, v85\n\t"
      "s_waitcnt vmcnt(21) lgkmcnt(4)\n\t"
      "v_mfma_f32_16x16x32_bf16 a[96:99], a[56:59], v[32:35], a[96:99]\n\t"
      "v_fma_f32 v48, v48, %[c1], %[c2]\n\t"
      "v_fma_f32 v50, v50, %[c1], %[c2]\n\t"
      "s_waitcnt vmcnt(20)\n\t"
      "v_mfma_f32_16x16x32_bf16 a[100:103], a[60:63], v[32:35], a[100:103]\n\t"
      "ds_read_b128 v[16:19], %[ab] offset:32768\n\t"
      "v_mul_f32 v48, v80, v48\n\t"
      "v_mul_f32 v50, v81, v50\n\t"
      "s_waitcnt lgkmcnt(4)\n\t"
      "v_mfma_f32_16x16x32_bf16 a[104:107], a[56:59], v[36:39], a[104:107]\n\t"
      "v_exp_f32 v48, v48\n\t"
      "s_add_u32 %[t0], %[sd], 32768\n\t"
      "buffer_load_dwordx4 a[48:51], %[vl], %[rs], %[t0] offen offset:0 sc1\n\t"
      "v_mfma_f32_16x16x32_bf16 a[108:111], a[60:63], v[36:39], a[108:111]\n\t"
      "ds_read_b128 v[20:23], %[ab] offset:33792\n\t"
      "v_exp_f32 v50, v50\n\t"
      "s_waitcnt lgkmcnt(4)\n\t"
      "v_mfma_f32_16x16x32_bf16 a[112:115], a[56:59], v[40:43], a[112:115]\n\t"
      "v_add_f32 v48, 1.0, v48\n\t"
      "v_add_f32 v50, 1.0, v50\n\t"
      "v_mfma_f32_16x16x32_bf16 a[116:119], a[60:63], v[40:43], a[116:119]\n\t"
      "ds_read_b128 v[24:27], %[ab] offset:34816\n\t"
      "v_rcp_f32 v48, v48\n\t"
      "s_waitcnt lgkmcnt(3)\n\t"
      "v_mfma_f32_16x16x32_bf16 a[120:123], a[56:59], v[44:47], a[120:123]\n\t"
      "v_rcp_f32 v50, v50\n\t"
      "s_add_u32 %[t0], %[sd], 32768\n\t"
      "buffer_load_dwordx4 a[52:55], %[vl], %[rs], %[t0] offen offset:1024 sc1\n\t"
      "v_mfma_f32_16x16x32_bf16 a[124:127], a[60:63], v[44:47], a[124:127]\n\t"
      "ds_read_b128 v[28:31], %[ab] offset:35840\n\t"
      "v_mul_f32 v48, v49, v48\n\t"
      "v_mul_f32 v50, v51, v50\n\t"
      "s_waitcnt vmcnt(21) lgkmcnt(3)\n\t"
      "v_mfma_f32_16x16x32_bf16 a[96:99], a[64:67], v[16:19], a[96:99]\n\t"
      "v_cvt_pk_bf16_f32 v52, v48, v50\n\t"
      "s_waitcnt vmcnt(20)\n\t"
      "v_mfma_f32_16x16x32_bf16 a[100:103], a[68:71], v[16:19], a[100:103]\n\t"
      "ds_read_b128 v[32:35], %[ab] offset:36864\n\t"
      "v_mul_f32 v48, v82, v82\n\t"
      "v_mul_f32 v50, v83, v83\n\t"
      "s_waitcnt lgkmcnt(3)\n\t"
      "v_mfma_f32_16x16x32_bf16 a[104:107], a[64:67], v[20:23], a[104:107]\n\t"
      "v_mul_f32 v49, v82, v86\n\t"
      "v_mul_f32 v51, v83, v87\n\t"
      "s_add_u32 %[t0], %[sd], 32768\n\t"
      "buffer_load_dwordx4 a[56:59], %[vl], %[rs], %[t0] offen offset:2048 sc1\n\t"
      "v_mfma_f32_16x16x32_bf16 a[108:111], a[68:71], v[20:23], a[108:111]\n\t"
      "ds_read_b128 v[36:39], %[ab] offset:37888\n\t"
      "v_fma_f32 v48, v48, %[c1], %[c2]\n\t"
      "v_fma_f32 v50, v50, %[c1], %[c2]\n\t"
      "s_waitcnt lgkmcnt(3)\n\t"
      "v_mfma_f32_16x16x32_bf16 a[112:115], a[64:67], v[24:27], a[112:115]\n\t"
      "v_mul_f32 v48, v82, v48\n\t"
      "v_mul_f32 v50, v83, v50\n\t"
      "v_mfma_f32_16x16x32_bf16 a[116:119], a[68:71], v[24:27], a[116:119]\n\t"
      "ds_read_b128 v[40:43], %[ab] offset:38912\n\t"
      "v_exp_f32 v48, v48\n\t"
      "s_waitcnt lgkmcnt(3)\n\t"
      "v_mfma_f32_16x16x32_bf16 a[120:123], a[64:67], v[28:31], a[120:123]\n\t"
      "v_exp_f32 v50, v50\n\t"
      "s_add_u32 %[t0], %[sd], 32768\n\t"
      "buffer_load_dwordx4 a[60:63], %[vl], %[rs], %[t0] offen offset:3072 sc1\n\t"
      "v_mfma_f32_16x16x32_bf16 a[124:127], a[68:71], v[28:31], a[124:127]\n\t"
      "ds_read_b128 v[44:47], %[ab] offset:39936\n\t"
      "v_add_f32 v48, 1.0, v48\n\t"
      "v_add_f32 v50, 1.0, v50\n\t"
      "s_waitcnt vmcnt(21) lgkmcnt(3)\n\t"
      "v_mfma_f32_16x16x32_bf16 a[96:99], a[72:75], v[32:35], a[96:99]\n\t"
      "v_rcp_f32 v48, v48\n\t"
      "s_waitcnt vmcnt(20)\n\t"
      "v_mfma_f32_16x16x32_bf16 a[100:103], a[76:79], v[32:35], a[100:103]\n\t"
      "ds_read_b128 v[16:19], %[ab] offset:40960\n\t"
      "v_rcp_f32 v50, v50\n\t"
      "s_waitcnt lgkmcnt(3)\n\t"
      "v_mfma_f32_16x16x32_bf16 a[104:107], a[72:75], v[36:39], a[104:107]\n\t"
      "v_mul_f32 v48, v49, v48\n\t"
      "v_mul_f32 v50, v51, v50\n\t"
      "s_add_u32 %[t0], %[sd], 65536\n\t"
      "buffer_load_dwordx4 a[64:67], %[vl], %[rs], %[t0] offen offset:0 sc1\n\t"
      "v_mfma_f32_16x16x32_bf16 a[108:111], a[76:79], v[36:39], a[108:111]\n\t"
      "ds_read_b128 v[20:23], %[ab] offset:41984\n\t"
      "v_cvt_pk_bf16_f32 v53, v48, v50\n\t"
      "s_waitcnt lgkmcnt(3)\n\t"
      "v_mfma_f32_16x16x32_bf16 a[112:115], a[72:75], v[40:43], a[112:115]\n\t"
      "ds_write_b64 %[aw], v[52:53] offset:2048\n\t"
      "v_mfma_f32_16x16x32_bf16 a[116:119], a[76:79], v[40:43], a[116:119]\n\t"
      "ds_read_b128 v[24:27], %[ab] offset:43008\n\t"
      "v_mul_f32 v48, v88, v88\n\t"
      "v_mul_f32 v50, v89, v89\n\t"
      "s_waitcnt lgkmcnt(4)\n\t"
      "v_mfma_f32_16x16x32_bf16 a[120:123], a[72:75], v[44:47], a[120:123]\n\t"
      "v_mul_f32 v49, v88, v92\n\t"
      "v_mul_f32 v51, v89, v93\n\t"
      "s_add_u32 %[t0], %[sd], 65536\n\t"
      "buffer_load_dwordx4 a[68:71], %[vl], %[rs], %[t0] offen offset:1024 sc1\n\t"
      "v_mfma_f32_16x16x32_bf16 a[124:127], a[76:79], v[44:47], a[124:127]\n\t"
      "ds_read_b128 v[28:31], %[ab] offset:44032\n\t"
      "v_fma_f32 v48, v48, %[c1], %[c2]\n\t"
      "v_fma_f32 v50, v50, %[c1], %[c2]\n\t"
      "s_waitcnt vmcnt(21) lgkmcnt(4)\n\t"
      "v_mfma_f32_16x16x32_bf16 a[96:99], a[80:83], v[16:19], a[96:99]\n\t"
      "v_mul_f32 v48, v88, v48\n\t"
      "v_mul_f32 v50, v89, v50\n\t"
      "s_waitcnt vmcnt(20)\n\t"
      "v_mfma_f32_16x16x32_bf16 a[100:103], a[84:87], v[16:19], a[100:103]\n\t"
      "ds_read_b128 v[32:35], %[ab] offset:45056\n\t"
      "v_exp_f32 v48, v48\n\t"
      "s_waitcnt lgkmcnt(4)\n\t"
      "v_mfma_f32_16x16x32_bf16 a[104:107], a[80:83], v[20:23], a[104:107]\n\t"
      "v_exp_f32 v50, v50\n\t"
      "s_add_u32 %[t0], %[sd], 65536\n\t"
      "buffer_load_dwordx4 a[72:75], %[vl], %[rs], %[t0] offen offset:2048 sc1\n\t"
      "v_mfma_f32_16x16x32_bf16 a[108:111], a[84:87], v[20:23], a[108:111]\n\t"
      "ds_read_b128 v[36:39], %[ab] offset:46080\n\t"
      "v_add_f32 v48, 1.0, v48\n\t"
      "v_add_f32 v50, 1.0, v50\n\t"
      "s_waitcnt lgkmcnt(3)\n\t"
      "v_mfma_f32_16x16x32_bf16 a[112:115], a[80:83], v[24:27], a[112:115]\n\t"
      "v_rcp_f32 v48, v48\n\t"
      "v_mfma_f32_16x16x32_bf16 a[116:119], a[84:87], v[24:27], a[116:119]\n\t"
      "ds_read_b128 v[40:43], %[ab] offset:47104\n\t"
      "v_rcp_f32 v50, v50\n\t"
      "s_waitcnt lgkmcnt(3)\n\t"
      "v_mfma_f32_16x16x32_bf16 a[120:123], a[80:83], v[28:31], a[120:123]\n\t"
      "v_mul_f32 v48, v49, v48\n\t"
      "v_mul_f32 v50, v51, v50\n\t"
      "s_add_u32 %[t0], %[sd], 65536\n\t"
      "buffer_load_dwordx4 a[76:79], %[vl], %[rs], %[t0] offen offset:3072 sc1\n\t"
      "v_mfma_f32_16x16x32_bf16 a[124:127], a[84:87], v[28:31], a[124:127]\n\t"
      "ds_read_b128 v[44:47], %[ab] offset:48128\n\t"
      "v_cvt_pk_bf16_f32 v54, v48, v50\n\t"
      "s_waitcnt vmcnt(21) lgkmcnt(3)\n\t"
      "v_mfma_f32_16x16x32_bf16 a[96:99], a[88:91], v[32:35], a[96:99]\n\t"
      "v_mul_f32 v48, v90, v90\n\t"
      "v_mul_f32 v50, v91, v91\n\t"
      "s_waitcnt vmcnt(20)\n\t"
      "v_mfma_f32_16x16x32_bf16 a[100:103], a[92:95], v[32:35], a[100:103]\n\t"
      "ds_read_b128 v[16:19], %[ab] offset:49152\n\t"
      "v_mul_f32 v49, v90, v94\n\t"
      "v_mul_f32 v51, v91, v95\n\t"
      "s_waitcnt lgkmcnt(3)\n\t"
      "v_mfma_f32_16x16x32_bf16 a[104:107], a[88:91], v[36:39], a[104:107]\n\t"
      "v_fma_f32 v48, v48, %[c1], %[c2]\n\t"
      "v_fma_f32 v50, v50, %[c1], %[c2]\n\t"
      "s_add_u32 %[t0], %[sd], 98304\n\t"
      "buffer_load_dwordx4 a[80:83], %[vl], %[rs], %[t0] offen offset:0 sc1\n\t"
      "v_mfma_f32_16x16x32_bf16 a[108:111], a[92:95], v[36:39], a[108:111]\n\t"
      "ds_read_b128 v[20:23], %[ab] offset:50176\n\t"
      "v_mul_f32 v48, v90, v48\n\t"
      "v_mul_f32 v50, v91, v50\n\t"
      "s_waitcnt lgkmcnt(3)\n\t"
      "v_mfma_f32_16x16x32_bf16 a[112:115], a[88:91], v[40:43], a[112:115]\n\t"
      "v_exp_f32 v48, v48\n\t"
      "v_mfma_f32_16x16x32_bf16 a[116:119], a[92:95], v[40:43], a[116:119]\n\t"
      "ds_read_b128 v[24:27], %[ab] offset:51200\n\t"
      "v_exp_f32 v50, v50\n\t"
      "s_waitcnt lgkmcnt(3)\n\t"
      "v_mfma_f32_16x16x32_bf16 a[120:123], a[88:91], v[44:47], a[120:123]\n\t"
      "v_add_f32 v48, 1.0, v48\n\t"
      "v_add_f32 v50, 1.0, v50\n\t"
      "s_add_u32 %[t0], %[sd], 98304\n\t"
      "buffer_load_dwordx4 a[84:87], %[vl], %[rs], %[t0] offen offset:1024 sc1\n\t"
      "v_mfma_f32_16x16x32_bf16 a[124:127], a[92:95], v[44:47], a[124:127]\n\t"
      "ds_read_b128 v[28:31], %[ab] offset:52224\n\t"
      "v_rcp_f32 v48, v48\n\t"
      "s_waitcnt vmcnt(21) lgkmcnt(3)\n\t"
      "v_mfma_f32_16x16x32_bf16 a[96:99], a[0:3], v[16:19], a[96:99]\n\t"
      "v_rcp_f32 v50, v50\n\t"
      "s_waitcnt vmcnt(20)\n\t"
      "v_mfma_f32_16x16x32_bf16 a[100:103], a[4:7], v[16:19], a[100:103]\n\t"
      "ds_read_b128 v[32:35], %[ab] offset:53248\n\t"
      "v_mul_f32 v48, v49, v48\n\t"
      "v_mul_f32 v50, v51, v50\n\t"
      "s_waitcnt lgkmcnt(3)\n\t"
      "v_mfma_f32_16x16x32_bf16 a[104:107], a[0:3], v[20:23], a[104:107]\n\t"
      "v_cvt_pk_bf16_f32 v55, v48, v50\n\t"
      "s_add_u32 %[t0], %[sd], 98304\n\t"
      "buffer_load_dwordx4 a[88:91], %[vl], %[rs], %[t0] offen offset:2048 sc1\n\t"
      "v_mfma_f32_16x16x32_bf16 a[108:111], a[4:7], v[20:23], a[108:111]\n\t"
      "ds_read_b128 v[36:39], %[ab] offset:54272\n\t"
      "ds_write_b64 %[aw], v[54:55] offset:3072\n\t"
      "s_waitcnt lgkmcnt(4)\n\t"
      "v_mfma_f32_16x16x32_bf16 a[112:115], a[0:3], v[24:27], a[112:115]\n\t"
      "v_mfma_f32_16x16x32_bf16 a[116:119], a[4:7], v[24:27], a[116:119]\n\t"
      "ds_read_b128 v[40:43], %[ab] offset:55296\n\t"
      "s_waitcnt lgkmcnt(4)\n\t"
      "v_mfma_f32_16x16x32_bf16 a[120:123], a[0:3], v[28:31], a[120:123]\n\t"
      "s_add_u32 %[t0], %[sd], 98304\n\t"
      "buffer_load_dwordx4 a[92:95], %[vl], %[rs], %[t0] offen offset:3072 sc1\n\t"
      "v_mfma_f32_16x16x32_bf16 a[124:127], a[4:7], v[28:31], a[124:127]\n\t"
      "ds_read_b128 v[44:47], %[ab] offset:56320\n\t"
      "s_waitcnt vmcnt(21) lgkmcnt(4)\n\t"
      "v_mfma_f32_16x16x32_bf16 a[96:99], a[8:11], v[32:35], a[96:99]\n\t"
      "s_waitcnt vmcnt(20)\n\t"
      "v_mfma_f32_16x16x32_bf16 a[100:103], a[12:15], v[32:35], a[100:103]\n\t"
      "ds_read_b128 v[16:19], %[ab] offset:57344\n\t"
      "s_waitcnt lgkmcnt(4)\n\t"
      "v_mfma_f32_16x16x32_bf16 a[104:107], a[8:11], v[36:39], a[104:107]\n\t"
      "s_add_u32 %[t0], %[sd], 131072\n\t"
      "buffer_load_dwordx4 a[0:3], %[vl], %[rs], %[t0] offen offset:0 sc1\n\t"
      "v_mfma_f32_16x16x32_bf16 a[108:111], a[12:15], v[36:39], a[108:111]\n\t"
      "ds_read_b128 v[20:23], %[ab] offset:58368\n\t"
      "s_waitcnt lgkmcnt(3)\n\t"
      "v_mfma_f32_16x16x32_bf16 a[112:115], a[8:11], v[40:43], a[112:115]\n\t"
      "v_mfma_f32_16x16x32_bf16 a[116:119], a[12:15], v[40:43], a[116:119]\n\t"
      "ds_read_b128 v[24:27], %[ab] offset:59392\n\t"
      "s_waitcnt lgkmcnt(3)\n\t"
      "v_mfma_f32_16x16x32_bf16 a[120:123], a[8:11], v[44:47], a[120:123]\n\t"
      "s_add_u32 %[t0], %[sd], 131072\n\t"
      "buffer_load_dwordx4 a[4:7], %[vl], %[rs], %[t0] offen offset:1024 sc1\n\t"
      "v_mfma_f32_16x16x32_bf16 a[124:127], a[12:15], v[44:47], a[124:127]\n\t"
      "ds_read_b128 v[28:31], %[ab] offset:60416\n\t"
      "s_waitcnt vmcnt(21) lgkmcnt(3)\n\t"
      "v_mfma_f32_16x16x32_bf16 a[96:99], a[16:19], v[16:19], a[96:99]\n\t"
      "s_waitcnt vmcnt(20)\n\t"
      "v_mfma_f32_16x16x32_bf16 a[100:103], a[20:23], v[16:19], a[100:103]\n\t"
      "s_waitcnt lgkmcnt(0)\n\t"
      "s_barrier\n\t"
      "ds_read_b128 v[32:35], %[ab] offset:61440\n\t"
      "s_waitcnt lgkmcnt(3)\n\t"
      "v_mfma_f32_16x16x32_bf16 a[104:107], a[16:19], v[20:23], a[104:107]\n\t"
      "s_add_u32 %[t0], %[sd], 131072\n\t"
      "buffer_load_dwordx4 a[8:11], %[vl], %[rs], %[t0] offen offset:2048 sc1\n\t"
      "v_mfma_f32_16x16x32_bf16 a[108:111], a[20:23], v[20:23], a[108:111]\n\t"
      "ds_read_b128 v[36:39], %[ab] offset:62464\n\t"
      "s_waitcnt lgkmcnt(3)\n\t"
      "v_mfma_f32_16x16x32_bf16 a[112:115], a[16:19], v[24:27], a[112:115]\n\t"
      "v_mfma_f32_16x16x32_bf16 a[116:119], a[20:23], v[24:27], a[116:119]\n\t"
      "ds_read_b128 v[40:43], %[ab] offset:63488\n\t"
      "s_waitcnt lgkmcnt(3)\n\t"
      "v_mfma_f32_16x16x32_bf16 a[120:123], a[16:19], v[28:31], a[120:123]\n\t"
      "s_add_u32 %[t0], %[sd], 131072\n\t"
      "buffer_load_dwordx4 a[12:15], %[vl], %[rs], %[t0] offen offset:3072 sc1\n\t"
      "v_mfma_f32_16x16x32_bf16 a[124:127], a[20:23], v[28:31], a[124:127]\n\t"
      "ds_read_b128 v[44:47], %[ab] offset:64512\n\t"
      "s_waitcnt vmcnt(21) lgkmcnt(3)\n\t"
      "v_mfma_f32_16x16x32_bf16 a[96:99], a[24:27], v[32:35], a[96:99]\n\t"
      "s_waitcnt vmcnt(20)\n\t"
      "v_mfma_f32_16x16x32_bf16 a[100:103], a[28:31], v[32:35], a[100:103]\n\t"
      "ds_read_b128 v[16:19], %[ac] offset:0\n\t"
      "s_waitcnt lgkmcnt(3)\n\t"
      "v_mfma_f32_16x16x32_bf16 a[104:107], a[24:27], v[36:39], a[104:107]\n\t"
      "s_add_u32 %[t0], %[sd], 163840\n\t"
      "buffer_load_dwordx4 a[16:19], %[vl], %[rs], %[t0] offen offset:0 sc1\n\t"
      "v_mfma_f32_16x16x32_bf16 a[108:111], a[28:31], v[36:39], a[108:111]\n\t"
      "ds_read_b128 v[20:23], %[ac] offset:1024\n\t"
      "s_waitcnt lgkmcnt(3)\n\t"
      "v_mfma_f32_16x16x32_bf16 a[112:115], a[24:27], v[40:43], a[112:115]\n\t"
      "v_mfma_f32_16x16x32_bf16 a[116:119], a[28:31], v[40:43], a[116:119]\n\t"
      "ds_read_b128 v[24:27], %[ac] offset:2048\n\t"
      "s_waitcnt lgkmcnt(3)\n\t"
      "v_mfma_f32_16x16x32_bf16 a[120:123], a[24:27], v[44:47], a[120:123]\n\t"
      "s_add_u32 %[t0], %[sd], 163840\n\t"
      "buffer_load_dwordx4 a[20:23], %[vl], %[rs], %[t0] offen offset:1024 sc1\n\t"
      "v_mfma_f32_16x16x32_bf16 a[124:127], a[28:31], v[44:47], a[124:127]\n\t"
      "ds_read_b128 v[28:31], %[ac] offset:3072\n\t"
      "s_waitcnt vmcnt(21) lgkmcnt(3)\n\t"
      "v_mfma_f32_16x16x32_bf16 v[64:67], a[32:35], v[16:19], 0\n\t"
      "s_waitcnt vmcnt(20)\n\t"
      "v_mfma_f32_16x16x32_bf16 v[68:71], a[36:39], v[16:19], 0\n\t"
      "ds_read_b128 v[32:35], %[ac] offset:4096\n\t"
      "s_waitcnt lgkmcnt(3)\n\t"
      "v_mfma_f32_16x16x32_bf16 v[80:83], a[32:35], v[20:23], 0\n\t"
      "s_add_u32 %[t0], %[sd], 163840\n\t"
      "buffer_load_dwordx4 a[24:27], %[vl], %[rs], %[t0] offen offset:2048 sc1\n\t"
      "v_mfma_f32_16x16x32_bf16 v[84:87], a[36:39], v[20:23], 0\n\t"
      "v_accvgpr_read_b32 v56, a96\n\t"
      "v_accvgpr_read_b32 v57, a97\n\t"
      "v_accvgpr_read_b32 v58, a100\n\t"
      "v_accvgpr_read_b32 v59, a101\n\t"
      "s_waitcnt vmcnt(20)\n\t"
      "v_mfma_f32_16x16x32_bf16 v[72:75], a[40:43], v[16:19], 0\n\t"
      "v_mul_f32 v48, v56, v56\n\t"
      "v_mul_f32 v50, v57, v57\n\t"
      "v_mul_f32 v49, v56, v58\n\t"
      "v_mul_f32 v51, v57, v59\n\t"
      "v_mfma_f32_16x16x32_bf16 v[88:91], a[40:43], v[20:23], 0\n\t"
      "ds_read_b128 v[36:39], %[ac] offset:5120\n\t"
      "v_fma_f32 v48, v48, %[c1], %[c2]\n\t"
      "v_fma_f32 v50, v50, %[c1], %[c2]\n\t"
      "v_mul_f32 v48, v56, v48\n\t"
      "v_mul_f32 v50, v57, v50\n\t"
      "s_waitcnt lgkmcnt(3)\n\t"
      "v_mfma_f32_16x16x32_bf16 v[96:99], a[32:35], v[24:27], 0\n\t"
      "v_exp_f32 v48, v48\n\t"
      "s_nop 0\n\t"
      "v_exp_f32 v50, v50\n\t"
      "s_add_u32 %[t0], %[sd], 163840\n\t"
      "buffer_load_dwordx4 a[28:31], %[vl], %[rs], %[t0] offen offset:3072 sc1\n\t"
      "v_mfma_f32_16x16x32_bf16 v[100:103], a[36:39], v[24:27], 0\n\t"
      "v_add_f32 v48, 1.0, v48\n\t"
      "v_add_f32 v50, 1.0, v50\n\t"
      "v_rcp_f32 v48, v48\n\t"
      "v_mfma_f32_16x16x32_bf16 v[104:107], a[40:43], v[24:27], 0\n\t"
      "v_rcp_f32 v50, v50\n\t"
      "s_nop 0\n\t"
      "v_mul_f32 v48, v49, v48\n\t"
      "v_mul_f32 v50, v51, v50\n\t"
      "s_waitcnt vmcnt(20)\n\t"
      "v_mfma_f32_16x16x32_bf16 v[76:79], a[44:47], v[16:19], 0\n\t"
      "ds_read_b128 v[40:43], %[ac] offset:6144\n\t"
      "v_cvt_pk_bf16_f32 v52, v48, v50\n\t"
      "v_accvgpr_read_b32 v56, a98\n\t"
      "v_accvgpr_read_b32 v57, a99\n\t"
      "v_mfma_f32_16x16x32_bf16 v[92:95], a[44:47], v[20:23], 0\n\t"
      "v_accvgpr_read_b32 v58, a102\n\t"
      "v_accvgpr_read_b32 v59, a103\n\t"
      "v_mul_f32 v48, v56, v56\n\t"
      "v_mul_f32 v50, v57, v57\n\t"
      "v_mfma_f32_16x16x32_bf16 v[108:111], a[44:47], v[24:27], 0\n\t"
      "v_mul_f32 v49, v56, v58\n\t"
      "v_mul_f32 v51, v57, v59\n\t"
      "v_fma_f32 v48, v48, %[c1], %[c2]\n\t"
      "v_fma_f32 v50, v50, %[c1], %[c2]\n\t"
      "s_waitcnt lgkmcnt(3)\n\t"
      "v_mfma_f32_16x16x32_bf16 v[112:115], a[32:35], v[28:31], 0\n\t"
      "v_mul_f32 v48, v56, v48\n\t"
      "v_mul_f32 v50, v57, v50\n\t"
      "v_exp_f32 v48, v48\n\t"
      "v_mfma_f32_16x16x32_bf16 v[116:119], a[36:39], v[28:31], 0\n\t"
      "ds_read_b128 v[44:47], %[ac] offset:7168\n\t"
      "v_exp_f32 v50, v50\n\t"
      "s_nop 0\n\t"
      "v_add_f32 v48, 1.0, v48\n\t"
      "v_add_f32 v50, 1.0, v50\n\t"
      "v_mfma_f32_16x16x32_bf16 v[120:123], a[40:43], v[28:31], 0\n\t"
      "v_rcp_f32 v48, v48\n\t"
      "s_nop 0\n\t"
      "v_rcp_f32 v50, v50\n\t"
      "v_mfma_f32_16x16x32_bf16 v[124:127], a[44:47], v[28:31], 0\n\t"
      "v_mul_f32 v48, v49, v48\n\t"
      "v_mul_f32 v50, v51, v50\n\t"
      "v_cvt_pk_bf16_f32 v53, v48, v50\n\t"
      "s_waitcnt vmcnt(19) lgkmcnt(3)\n\t"
      "v_mfma_f32_16x16x32_bf16 v[64:67], a[48:51], v[32:35], v[64:67]\n\t"
      "ds_write_b64 %[aw], v[52:53] offset:16384\n\t"
      "v_accvgpr_read_b32 v56, a104\n\t"
      "v_accvgpr_read_b32 v57, a105\n\t"
      "s_waitcnt vmcnt(18)\n\t"
      "v_mfma_f32_16x16x32_bf16 v[68:71], a[52:55], v[32:35], v[68:71]\n\t"
      "ds_read_b128 v[16:19], %[ac] offset:8192\n\t"
      "v_accvgpr_read_b32 v58, a108\n\t"
      "v_accvgpr_read_b32 v59, a109\n\t"
      "v_mul_f32 v48, v56, v56\n\t"
      "v_mul_f32 v50, v57, v57\n\t"
      "s_waitcnt lgkmcnt(4)\n\t"
      "v_mfma_f32_16x16x32_bf16 v[80:83], a[48:51], v[36:39], v[80:83]\n\t"
      "v_mul_f32 v49, v56, v58\n\t"
      "v_mul_f32 v51, v57, v59\n\t"
      "v_fma_f32 v48, v48, %[c1], %[c2]\n\t"
      "v_fma_f32 v50, v50, %[c1], %[c2]\n\t"
      "s_add_u32 %[t0], %[sd], 196608\n\t"
      "buffer_load_dwordx4 a[32:35], %[vl], %[rs], %[t0] offen offset:0 sc1\n\t"
      "v_mfma_f32_16x16x32_bf16 v[84:87], a[52:55], v[36:39], v[84:87]\n\t"
      "v_mul_f32 v48, v56, v48\n\t"
      "v_mul_f32 v50, v57, v50\n\t"
      "v_exp_f32 v48, v48\n\t"
      "s_waitcnt vmcnt(18)\n\t"
      "v_mfma_f32_16x16x32_bf16 v[72:75], a[56:59], v[32:35], v[72:75]\n\t"
      "v_exp_f32 v50, v50\n\t"
      "s_nop 0\n\t"
      "v_add_f32 v48, 1.0, v48\n\t"
      "v_add_f32 v50, 1.0, v50\n\t"
      "v_mfma_f32_16x16x32_bf16 v[88:91], a[56:59], v[36:39], v[88:91]\n\t"
      "ds_read_b128 v[20:23], %[ac] offset:9216\n\t"
      "v_rcp_f32 v48, v48\n\t"
      "s_nop 0\n\t"
      "v_rcp_f32 v50, v50\n\t"
      "s_waitcnt lgkmcnt(4)\n\t"
      "v_mfma_f32_16x16x32_bf16 v[96:99], a[48:51], v[40:43], v[96:99]\n\t"
      "v_mul_f32 v48, v49, v48\n\t"
      "v_mul_f32 v50, v51, v50\n\t"
      "v_cvt_pk_bf16_f32 v54, v48, v50\n\t"
      "s_add_u32 %[t0], %[sd], 196608\n\t"
      "buffer_load_dwordx4 a[36:39], %[vl], %[rs], %[t0] offen offset:1024 sc1\n\t"
      "v_mfma_f32_16x16x32_bf16 v[100:103], a[52:55], v[40:43], v[100:103]\n\t"
      "v_accvgpr_read_b32 v56, a106\n\t"
      "v_accvgpr_read_b32 v57, a107\n\t"
      "v_accvgpr_read_b32 v58, a110\n\t"
      "v_accvgpr_read_b32 v59, a111\n\t"
      "v_mfma_f32_16x16x32_bf16 v[104:107], a[56:59], v[40:43], v[104:107]\n\t"
      "v_mul_f32 v48, v56, v56\n\t"
      "v_mul_f32 v50, v57, v57\n\t"
      "v_mul_f32 v49, v56, v58\n\t"
      "v_mul_f32 v51, v57, v59\n\t"
      "s_waitcnt vmcnt(18)\n\t"
      "v_mfma_f32_16x16x32_bf16 v[76:79], a[60:63], v[32:35], v[76:79]\n\t"
      "ds_read_b128 v[24:27], %[ac] offset:10240\n\t"
      "v_fma_f32 v48, v48, %[c1], %[c2]\n\t"
      "v_fma_f32 v50, v50, %[c1], %[c2]\n\t"
      "v_mul_f32 v48, v56, v48\n\t"
      "v_mul_f32 v50, v57, v50\n\t"
      "v_mfma_f32_16x16x32_bf16 v[92:95], a[60:63], v[36:39], v[92:95]\n\t"
      "v_exp_f32 v48, v48\n\t"
      "s_nop 0\n\t"
      "v_exp_f32 v50, v50\n\t"
      "s_add_u32 %[t0], %[sd], 196608\n\t"
      "buffer_load_dwordx4 a[40:43], %[vl], %[rs], %[t0] offen offset:2048 sc1\n\t"
      "v_mfma_f32_16x16x32_bf16 v[108:111], a[60:63], v[40:43], v[108:111]\n\t"
      "v_add_f32 v48, 1.0, v48\n\t"
      "v_add_f32 v50, 1.0, v50\n\t"
      "v_rcp_f32 v48, v48\n\t"
      "s_waitcnt lgkmcnt(4)\n\t"
      "v_mfma_f32_16x16x32_bf16 v[112:115], a[48:51], v[44:47], v[112:115]\n\t"
      "v_rcp_f32 v50, v50\n\t"
      "s_nop 0\n\t"
      "v_mul_f32 v48, v49, v48\n\t"
      "v_mul_f32 v50, v51, v50\n\t"
      "v_mfma_f32_16x16x32_bf16 v[116:119], a[52:55], v[44:47], v[116:119]\n\t"
      "ds_read_b128 v[28:31], %[ac] offset:11264\n\t"
      "v_cvt_pk_bf16_f32 v55, v48, v50\n\t"
      "ds_write_b64 %[aw], v[54:55] offset:17408\n\t"
      "v_mfma_f32_16x16x32_bf16 v[120:123], a[56:59], v[44:47], v[120:123]\n\t"
      "v_accvgpr_read_b32 v56, a112\n\t"
      "v_accvgpr_read_b32 v57, a113\n\t"
      "v_accvgpr_read_b32 v58, a116\n\t"
      "v_accvgpr_read_b32 v59, a117\n\t"
      "s_add_u32 %[t0], %[sd], 196608\n\t"
      "buffer_load_dwordx4 a[44:47], %[vl], %[rs], %[t0] offen offset:3072 sc1\n\t"
      "v_mfma_f32_16x16x32_bf16 v[124:127], a[60:63], v[44:47], v[124:127]\n\t"
      "v_mul_f32 v48, v56, v56\n\t"
      "v_mul_f32 v50, v57, v57\n\t"
      "v_mul_f32 v49, v56, v58\n\t"
      "v_mul_f32 v51, v57, v59\n\t"
      "s_waitcnt vmcnt(19) lgkmcnt(4)\n\t"
      "v_mfma_f32_16x16x32_bf16 v[64:67], a[64:67], v[16:19], v[64:67]\n\t"
      "v_fma_f32 v48, v48, %[c1], %[c2]\n\t"
      "v_fma_f32 v50, v50, %[c1], %[c2]\n\t"
      "v_mul_f32 v48, v56, v48\n\t"
      "v_mul_f32 v50, v57, v50\n\t"
      "s_waitcnt vmcnt(18)\n\t"
      "v_mfma_f32_16x16x32_bf16 v[68:71], a[68:71], v[16:19], v[68:71]\n\t"
      "ds_read_b128 v[32:35], %[ac] offset:12288\n\t"
      "v_exp_f32 v48, v48\n\t"
      "s_nop 0\n\t"
      "v_exp_f32 v50, v50\n\t"
      "s_waitcnt lgkmcnt(4)\n\t"
      "v_mfma_f32_16x16x32_bf16 v[80:83], a[64:67], v[20:23], v[80:83]\n\t"
      "v_add_f32 v48, 1.0, v48\n\t"
      "v_add_f32 v50, 1.0, v50\n\t"
      "v_rcp_f32 v48, v48\n\t"
      "s_add_u32 %[t0], %[sd], 229376\n\t"
      "buffer_load_dwordx4 a[48:51], %[vl], %[rs], %[t0] offen offset:0 sc1\n\t"
      "v_mfma_f32_16x16x32_bf16 v[84:87], a[68:71], v[20:23], v[84:87]\n\t"
      "v_rcp_f32 v50, v50\n\t"
      "s_nop 0\n\t"
      "v_mul_f32 v48, v49, v48\n\t"
      "v_mul_f32 v50, v51, v50\n\t"
      "s_waitcnt vmcnt(18)\n\t"
      "v_mfma_f32_16x16x32_bf16 v[72:75], a[72:75], v[16:19], v[72:75]\n\t"
      "v_cvt_pk_bf16_f32 v52, v48, v50\n\t"
      "v_accvgpr_read_b32 v56, a114\n\t"
      "v_accvgpr_read_b32 v57, a115\n\t"
      "v_mfma_f32_16x16x32_bf16 v[88:91], a[72:75], v[20:23], v[88:91]\n\t"
      "ds_read_b128 v[36:39], %[ac] offset:13312\n\t"
      "v_accvgpr_read_b32 v58, a118\n\t"
      "v_accvgpr_read_b32 v59, a119\n\t"
      "v_mul_f32 v48, v56, v56\n\t"
      "v_mul_f32 v50, v57, v57\n\t"
      "s_waitcnt lgkmcnt(4)\n\t"
      "v_mfma_f32_16x16x32_bf16 v[96:99], a[64:67], v[24:27], v[96:99]\n\t"
      "v_mul_f32 v49, v56, v58\n\t"
      "v_mul_f32 v51, v57, v59\n\t"
      "v_fma_f32 v48, v48, %[c1], %[c2]\n\t"
      "v_fma_f32 v50, v50, %[c1], %[c2]\n\t"
      "s_add_u32 %[t0], %[sd], 229376\n\t"
      "buffer_load_dwordx4 a[52:55], %[vl], %[rs], %[t0] offen offset:1024 sc1\n\t"
      "v_mfma_f32_16x16x32_bf16 v[100:103], a[68:71], v[24:27], v[100:103]\n\t"
      "v_mul_f32 v48, v56, v48\n\t"
      "v_mul_f32 v50, v57, v50\n\t"
      "v_exp_f32 v48, v48\n\t"
      "v_mfma_f32_16x16x32_bf16 v[104:107], a[72:75], v[24:27], v[104:107]\n\t"
      "v_exp_f32 v50, v50\n\t"
      "s_nop 0\n\t"
      "v_add_f32 v48, 1.0, v48\n\t"
      "v_add_f32 v50, 1.0, v50\n\t"
      "s_waitcnt vmcnt(18)\n\t"
      "v_mfma_f32_16x16x32_bf16 v[76:79], a[76:79], v[16:19], v[76:79]\n\t"
      "ds_read_b128 v[40:43], %[ac] offset:14336\n\t"
      "v_rcp_f32 v48, v48\n\t"
      "s_nop 0\n\t"
      "v_rcp_f32 v50, v50\n\t"
      "v_mfma_f32_16x16x32_bf16 v[92:95], a[76:79], v[20:23], v[92:95]\n\t"
      "v_mul_f32 v48, v49, v48\n\t"
      "v_mul_f32 v50, v51, v50\n\t"
      "v_cvt_pk_bf16_f32 v53, v48, v50\n\t"
      "s_add_u32 %[t0], %[sd], 229376\n\t"
      "buffer_load_dwordx4 a[56:59], %[vl], %[rs], %[t0] offen offset:2048 sc1\n\t"
      "v_mfma_f32_16x16x32_bf16 v[108:111], a[76:79], v[24:27], v[108:111]\n\t"
      "ds_write_b64 %[aw], v[52:53] offset:18432\n\t"
      "v_accvgpr_read_b32 v56, a120\n\t"
      "v_accvgpr_read_b32 v57, a121\n\t"
      "s_waitcnt lgkmcnt(5)\n\t"
      "v_mfma_f32_16x16x32_bf16 v[112:115], a[64:67], v[28:31], v[112:115]\n\t"
      "v_accvgpr_read_b32 v58, a124\n\t"
      "v_accvgpr_read_b32 v59, a125\n\t"
      "v_mul_f32 v48, v56, v56\n\t"
      "v_mul_f32 v50, v57, v57\n\t"
      "v_mfma_f32_16x16x32_bf16 v[116:119], a[68:71], v[28:31], v[116:119]\n\t"
      "ds_read_b128 v[44:47], %[ac] offset:15360\n\t"
      "v_mul_f32 v49, v56, v58\n\t"
      "v_mul_f32 v51, v57, v59\n\t"
      "v_fma_f32 v48, v48, %[c1], %[c2]\n\t"
      "v_fma_f32 v50, v50, %[c1], %[c2]\n\t"
      "v_mfma_f32_16x16x32_bf16 v[120:123], a[72:75], v[28:31], v[120:123]\n\t"
      "v_mul_f32 v48, v56, v48\n\t"
      "v_mul_f32 v50, v57, v50\n\t"
      "v_exp_f32 v48, v48\n\t"
      "s_add_u32 %[t0], %[sd], 229376\n\t"
      "buffer_load_dwordx4 a[60:63], %[vl], %[rs], %[t0] offen offset:3072 sc1\n\t"
      "v_mfma_f32_16x16x32_bf16 v[124:127], a[76:79], v[28:31], v[124:127]\n\t"
      "v_exp_f32 v50, v50\n\t"
      "s_nop 0\n\t"
      "v_add_f32 v48, 1.0, v48\n\t"
      "v_add_f32 v50, 1.0, v50\n\t"
      "s_waitcnt vmcnt(19) lgkmcnt(4)\n\t"
      "v_mfma_f32_16x16x32_bf16 v[64:67], a[80:83], v[32:35], v[64:67]\n\t"
      "v_rcp_f32 v48, v48\n\t"
      "s_nop 0\n\t"
      "v_rcp_f32 v50, v50\n\t"
      "s_waitcnt vmcnt(18)\n\t"
      "v_mfma_f32_16x16x32_bf16 v[68:71], a[84:87], v[32:35], v[68:71]\n\t"
      "v_mul_f32 v48, v49, v48\n\t"
      "v_mul_f32 v50, v51, v50\n\t"
      "v_cvt_pk_bf16_f32 v54, v48, v50\n\t"
      "s_waitcnt lgkmcnt(3)\n\t"
      "v_mfma_f32_16x16x32_bf16 v[80:83], a[80:83], v[36:39], v[80:83]\n\t"
      "v_accvgpr_read_b32 v56, a122\n\t"
      "v_accvgpr_read_b32 v57, a123\n\t"
      "v_accvgpr_read_b32 v58, a126\n\t"
      "v_accvgpr_read_b32 v59, a127\n\t"
      "v_mfma_f32_16x16x32_bf16 v[84:87], a[84:87], v[36:39], v[84:87]\n\t"
      "v_mul_f32 v48, v56, v56\n\t"
      "v_mul_f32 v50, v57, v57\n\t"
      "s_waitcnt vmcnt(17)\n\t"
      "v_mfma_f32_16x16x32_bf16 v[72:75], a[88:91], v[32:35], v[72:75]\n\t"
      "v_mul_f32 v49, v56, v58\n\t"
      "v_mul_f32 v51, v57, v59\n\t"
      "v_mfma_f32_16x16x32_bf16 v[88:91], a[88:91], v[36:39], v[88:91]\n\t"
      "v_fma_f32 v48, v48, %[c1], %[c2]\n\t"
      "v_fma_f32 v50, v50, %[c1], %[c2]\n\t"
      "s_waitcnt lgkmcnt(2)\n\t"
      "v_mfma_f32_16x16x32_bf16 v[96:99], a[80:83], v[40:43], v[96:99]\n\t"
      "v_mul_f32 v48, v56, v48\n\t"
      "v_mul_f32 v50, v57, v50\n\t"
      "v_mfma_f32_16x16x32_bf16 v[100:103], a[84:87], v[40:43], v[100:103]\n\t"
      "v_exp_f32 v48, v48\n\t"
      "v_mfma_f32_16x16x32_bf16 v[104:107], a[88:91], v[40:43], v[104:107]\n\t"
      "v_exp_f32 v50, v50\n\t"
      "s_waitcnt vmcnt(16)\n\t"
      "v_mfma_f32_16x16x32_bf16 v[76:79], a[92:95], v[32:35], v[76:79]\n\t"
      "v_add_f32 v48, 1.0, v48\n\t"
      "v_add_f32 v50, 1.0, v50\n\t"
      "v_mfma_f32_16x16x32_bf16 v[92:95], a[92:95], v[36:39], v[92:95]\n\t"
      "v_rcp_f32 v48, v48\n\t"
      "v_mfma_f32_16x16x32_bf16 v[108:111], a[92:95], v[40:43], v[108:111]\n\t"
      "v_rcp_f32 v50, v50\n\t"
      "s_waitcnt lgkmcnt(0)\n\t"
      "v_mfma_f32_16x16x32_bf16 v[112:115], a[80:83], v[44:47], v[112:115]\n\t"
      "v_mul_f32 v48, v49, v48\n\t"
      "v_mul_f32 v50, v51, v50\n\t"
      "v_mfma_f32_16x16x32_bf16 v[116:119], a[84:87], v[44:47], v[116:119]\n\t"
      "v_cvt_pk_bf16_f32 v55, v48, v50\n\t"
      "v_mfma_f32_16x16x32_bf16 v[120:123], a[88:91], v[44:47], v[120:123]\n\t"
      "ds_write_b64 %[aw], v[54:55] offset:19456\n\t"
      "v_mfma_f32_16x16x32_bf16 v[124:127], a[92:95], v[44:47], v[124:127]\n\t"
      "s_waitcnt lgkmcnt(0)\n\t"
      "s_barrier\n\t"
      "ds_read_b128 v[16:19], %[ac] offset:16384\n\t"
      "ds_read_b128 v[20:23], %[ac] offset:17408\n\t"
      "ds_read_b128 v[24:27], %[ac] offset:18432\n\t"
      "ds_read_b128 v[28:31], %[ac] offset:19456\n\t"
      "ds_read_b128 v[32:35], %[ac] offset:20480\n\t"
      "ds_read_b128 v[36:39], %[ac] offset:21504\n\t"
      "s_waitcnt vmcnt(15) lgkmcnt(5)\n\t"
      "v_mfma_f32_16x16x32_bf16 v[64:67], a[0:3], v[16:19], v[64:67]\n\t"
      "s_waitcnt vmcnt(14)\n\t"
      "v_mfma_f32_16x16x32_bf16 v[68:71], a[4:7], v[16:19], v[68:71]\n\t"
      "ds_read_b128 v[40:43], %[ac] offset:22528\n\t"
      "s_waitcnt lgkmcnt(5)\n\t"
      "v_mfma_f32_16x16x32_bf16 v[80:83], a[0:3], v[20:23], v[80:83]\n\t"
      "v_mfma_f32_16x16x32_bf16 v[84:87], a[4:7], v[20:23], v[84:87]\n\t"
      "s_waitcnt vmcnt(13)\n\t"
      "v_mfma_f32_16x16x32_bf16 v[72:75], a[8:11], v[16:19], v[72:75]\n\t"
      "v_mfma_f32_16x16x32_bf16 v[88:91], a[8:11], v[20:23], v[88:91]\n\t"
      "ds_read_b128 v[44:47], %[ac] offset:23552\n\t"
      "s_waitcnt lgkmcnt(5)\n\t"
      "v_mfma_f32_16x16x32_bf16 v[96:99], a[0:3], v[24:27], v[96:99]\n\t"
      "v_mfma_f32_16x16x32_bf16 v[100:103], a[4:7], v[24:27], v[100:103]\n\t"
      "v_mfma_f32_16x16x32_bf16 v[104:107], a[8:11], v[24:27], v[104:107]\n\t"
      "s_waitcnt vmcnt(12)\n\t"
      "v_mfma_f32_16x16x32_bf16 v[76:79], a[12:15], v[16:19], v[76:79]\n\t"
      "v_mfma_f32_16x16x32_bf16 v[92:95], a[12:15], v[20:23], v[92:95]\n\t"
      "v_mfma_f32_16x16x32_bf16 v[108:111], a[12:15], v[24:27], v[108:111]\n\t"
      "s_waitcnt lgkmcnt(4)\n\t"
      "v_mfma_f32_16x16x32_bf16 v[112:115], a[0:3], v[28:31], v[112:115]\n\t"
      "v_mfma_f32_16x16x32_bf16 v[116:119], a[4:7], v[28:31], v[116:119]\n\t"
      "v_mfma_f32_16x16x32_bf16 v[120:123], a[8:11], v[28:31], v[120:123]\n\t"
      "v_mfma_f32_16x16x32_bf16 v[124:127], a[12:15], v[28:31], v[124:127]\n\t"
      "s_waitcnt vmcnt(11) lgkmcnt(3)\n\t"
      "v_mfma_f32_16x16x32_bf16 v[64:67], a[16:19], v[32:35], v[64:67]\n\t"
      "s_waitcnt vmcnt(10)\n\t"
      "v_mfma_f32_16x16x32_bf16 v[68:71], a[20:23], v[32:35], v[68:71]\n\t"
      "ds_read_b128 v[16:19], %[ac] offset:24576\n\t"
      "s_waitcnt lgkmcnt(3)\n\t"
      "v_mfma_f32_16x16x32_bf16 v[80:83], a[16:19], v[36:39], v[80:83]\n\t"
      "v_mfma_f32_16x16x32_bf16 v[84:87], a[20:23], v[36:39], v[84:87]\n\t"
      "s_waitcnt vmcnt(9)\n\t"
      "v_mfma_f32_16x16x32_bf16 v[72:75], a[24:27], v[32:35], v[72:75]\n\t"
      "v_mfma_f32_16x16x32_bf16 v[88:91], a[24:27], v[36:39], v[88:91]\n\t"
      "ds_read_b128 v[20:23], %[ac] offset:25600\n\t"
      "s_waitcnt lgkmcnt(3)\n\t"
      "v_mfma_f32_16x16x32_bf16 v[96:99], a[16:19], v[40:43], v[96:99]\n\t"
      "v_mfma_f32_16x16x32_bf16 v[100:103], a[20:23], v[40:43], v[100:103]\n\t"
      "v_mfma_f32_16x16x32_bf16 v[104:107], a[24:27], v[40:43], v[104:107]\n\t"
      "s_waitcnt vmcnt(8)\n\t"
      "v_mfma_f32_16x16x32_bf16 v[76:79], a[28:31], v[32:35], v[76:79]\n\t"
      "ds_read_b128 v[24:27], %[ac] offset:26624\n\t"
      "v_mfma_f32_16x16x32_bf16 v[92:95], a[28:31], v[36:39], v[92:95]\n\t"
      "v_mfma_f32_16x16x32_bf16 v[108:111], a[28:31], v[40:43], v[108:111]\n\t"
      "s_waitcnt lgkmcnt(3)\n\t"
      "v_mfma_f32_16x16x32_bf16 v[112:115], a[16:19], v[44:47], v[112:115]\n\t"
      "v_mfma_f32_16x16x32_bf16 v[116:119], a[20:23], v[44:47], v[116:119]\n\t"
      "ds_read_b128 v[28:31], %[ac] offset:27648\n\t"
      "v_mfma_f32_16x16x32_bf16 v[120:123], a[24:27], v[44:47], v[120:123]\n\t"
      "v_mfma_f32_16x16x32_bf16 v[124:127], a[28:31], v[44:47], v[124:127]\n\t"
      "s_waitcnt vmcnt(7) lgkmcnt(3)\n\t"
      "v_mfma_f32_16x16x32_bf16 v[64:67], a[32:35], v[16:19], v[64:67]\n\t"
      "s_waitcnt vmcnt(6)\n\t"
      "v_mfma_f32_16x16x32_bf16 v[68:71], a[36:39], v[16:19], v[68:71]\n\t"
      "ds_read_b128 v[32:35], %[ac] offset:28672\n\t"
      "s_waitcnt lgkmcnt(3)\n\t"
      "v_mfma_f32_16x16x32_bf16 v[80:83], a[32:35], v[20:23], v[80:83]\n\t"
      "v_mfma_f32_16x16x32_bf16 v[84:87], a[36:39], v[20:23], v[84:87]\n\t"
      "s_waitcnt vmcnt(5)\n\t"
      "v_mfma_f32_16x16x32_bf16 v[72:75], a[40:43], v[16:19], v[72:75]\n\t"
      "v_mfma_f32_16x16x32_bf16 v[88:91], a[40:43], v[20:23], v[88:91]\n\t"
      "ds_read_b128 v[36:39], %[ac] offset:29696\n\t"
      "s_waitcnt lgkmcnt(3)\n\t"
      "v_mfma_f32_16x16x32_bf16 v[96:99], a[32:35], v[24:27], v[96:99]\n\t"
      "v_mfma_f32_16x16x32_bf16 v[100:103], a[36:39], v[24:27], v[100:103]\n\t"
      "v_mfma_f32_16x16x32_bf16 v[104:107], a[40:43], v[24:27], v[104:107]\n\t"
      "s_waitcnt vmcnt(4)\n\t"
      "v_mfma_f32_16x16x32_bf16 v[76:79], a[44:47], v[16:19], v[76:79]\n\t"
      "ds_read_b128 v[40:43], %[ac] offset:30720\n\t"
      "v_mfma_f32_16x16x32_bf16 v[92:95], a[44:47], v[20:23], v[92:95]\n\t"
      "v_mfma_f32_16x16x32_bf16 v[108:111], a[44:47], v[24:27], v[108:111]\n\t"
      "s_waitcnt lgkmcnt(3)\n\t"
      "v_mfma_f32_16x16x32_bf16 v[112:115], a[32:35], v[28:31], v[112:115]\n\t"
      "v_mfma_f32_16x16x32_bf16 v[116:119], a[36:39], v[28:31], v[116:119]\n\t"
      "ds_read_b128 v[44:47], %[ac] offset:31744\n\t"
      "v_mfma_f32_16x16x32_bf16 v[120:123], a[40:43], v[28:31], v[120:123]\n\t"
      "v_mfma_f32_16x16x32_bf16 v[124:127], a[44:47], v[28:31], v[124:127]\n\t"
      "s_waitcnt vmcnt(3) lgkmcnt(3)\n\t"
      "v_mfma_f32_16x16x32_bf16 v[64:67], a[48:51], v[32:35], v[64:67]\n\t"
      "s_waitcnt vmcnt(2)\n\t"
      "v_mfma_f32_16x16x32_bf16 v[68:71], a[52:55], v[32:35], v[68:71]\n\t"
      "s_waitcnt lgkmcnt(2)\n\t"
      "v_mfma_f32_16x16x32_bf16 v[80:83], a[48:51], v[36:39], v[80:83]\n\t"
      "v_mfma_f32_16x16x32_bf16 v[84:87], a[52:55], v[36:39], v[84:87]\n\t"
      "s_waitcnt vmcnt(1)\n\t"
      "v_mfma_f32_16x16x32_bf16 v[72:75], a[56:59], v[32:35], v[72:75]\n\t"
      "v_mfma_f32_16x16x32_bf16 v[88:91], a[56:59], v[36:39], v[88:91]\n\t"
      "s_waitcnt lgkmcnt(1)\n\t"
      "v_mfma_f32_16x16x32_bf16 v[96:99], a[48:51], v[40:43], v[96:99]\n\t"
      "v_mfma_f32_16x16x32_bf16 v[100:103], a[52:55], v[40:43], v[100:103]\n\t"
      "v_mfma_f32_16x16x32_bf16 v[104:107], a[56:59], v[40:43], v[104:107]\n\t"
      "s_waitcnt vmcnt(0)\n\t"
      "v_mfma_f32_16x16x32_bf16 v[76:79], a[60:63], v[32:35], v[76:79]\n\t"
      "v_mfma_f32_16x16x32_bf16 v[92:95], a[60:63], v[36:39], v[92:95]\n\t"
      "v_mfma_f32_16x16x32_bf16 v[108:111], a[60:63], v[40:43], v[108:111]\n\t"
      "s_waitcnt lgkmcnt(0)\n\t"
      "v_mfma_f32_16x16x32_bf16 v[112:115], a[48:51], v[44:47], v[112:115]\n\t"
      "v_mfma_f32_16x16x32_bf16 v[116:119], a[52:55], v[44:47], v[116:119]\n\t"
      "v_mfma_f32_16x16x32_bf16 v[120:123], a[56:59], v[44:47], v[120:123]\n\t"
      "v_mfma_f32_16x16x32_bf16 v[124:127], a[60:63], v[44:47], v[124:127]\n\t"
      "s_nop 7\n\t"
      "v_cvt_pk_bf16_f32 v64, v64, v65\n\t"
      "v_cvt_pk_bf16_f32 v65, v66, v67\n\t"
      "v_cvt_pk_bf16_f32 v66, v68, v69\n\t"
      "v_cvt_pk_bf16_f32 v67, v70, v71\n\t"
      "s_add_u32 %[t0], %[so], 0\n\t"
      "buffer_store_dwordx4 v[64:67], %[vl], %[rs], %[t0] offen sc1\n\t"
      "v_cvt_pk_bf16_f32 v72, v72, v73\n\t"
      "v_cvt_pk_bf16_f32 v73, v74, v75\n\t"
      "v_cvt_pk_bf16_f32 v74, v76, v77\n\t"
      "v_cvt_pk_bf16_f32 v75, v78, v79\n\t"
      "s_add_u32 %[t0], %[so], 1024\n\t"
      "buffer_store_dwordx4 v[72:75], %[vl], %[rs], %[t0] offen sc1\n\t"
      "v_cvt_pk_bf16_f32 v80, v80, v81\n\t"
      "v_cvt_pk_bf16_f32 v81, v82, v83\n\t"
      "v_cvt_pk_bf16_f32 v82, v84, v85\n\t"
      "v_cvt_pk_bf16_f32 v83, v86, v87\n\t"
      "s_add_u32 %[t0], %[so], 16384\n\t"
      "buffer_store_dwordx4 v[80:83], %[vl], %[rs], %[t0] offen sc1\n\t"
      "v_cvt_pk_bf16_f32 v88, v88, v89\n\t"
      "v_cvt_pk_bf16_f32 v89, v90, v91\n\t"
      "v_cvt_pk_bf16_f32 v90, v92, v93\n\t"
      "v_cvt_pk_bf16_f32 v91, v94, v95\n\t"
      "s_add_u32 %[t0], %[so], 17408\n\t"
      "buffer_store_dwordx4 v[88:91], %[vl], %[rs], %[t0] offen sc1\n\t"
      "v_cvt_pk_bf16_f32 v96, v96, v97\n\t"
      "v_cvt_pk_bf16_f32 v97, v98, v99\n\t"
      "v_cvt_pk_bf16_f32 v98, v100, v101\n\t"
      "v_cvt_pk_bf16_f32 v99, v102, v103\n\t"
      "s_add_u32 %[t0], %[so], 32768\n\t"
      "buffer_store_dwordx4 v[96:99], %[vl], %[rs], %[t0] offen sc1\n\t"
      "v_cvt_pk_bf16_f32 v104, v104, v105\n\t"
      "v_cvt_pk_bf16_f32 v105, v106, v107\n\t"
      "v_cvt_pk_bf16_f32 v106, v108, v109\n\t"
      "v_cvt_pk_bf16_f32 v107, v110, v111\n\t"
      "s_add_u32 %[t0], %[so], 33792\n\t"
      "buffer_store_dwordx4 v[104:107], %[vl], %[rs], %[t0] offen sc1\n\t"
      "v_cvt_pk_bf16_f32 v112, v112, v113\n\t"
      "v_cvt_pk_bf16_f32 v113, v114, v115\n\t"
      "v_cvt_pk_bf16_f32 v114, v116, v117\n\t"
      "v_cvt_pk_bf16_f32 v115, v118, v119\n\t"
      "s_add_u32 %[t0], %[so], 49152\n\t"
      "buffer_store_dwordx4 v[112:115], %[vl], %[rs], %[t0] offen sc1\n\t"
      "v_cvt_pk_bf16_f32 v120, v120, v121\n\t"
      "v_cvt_pk_bf16_f32 v121, v122, v123\n\t"
      "v_cvt_pk_bf16_f32 v122, v124, v125\n\t"
      "v_cvt_pk_bf16_f32 v123, v126, v127\n\t"
      "s_add_u32 %[t0], %[so], 50176\n\t"
      "buffer_store_dwordx4 v[120:123], %[vl], %[rs], %[t0] offen sc1\n\t"
      "s_nop 1\n\t"
      : [t0] "=&s"(t0)
      : "{v[96:127]}"(WV), [vl] "v"(vL), [rs] "s"(srdS), [sa] "s"(sA), [da] "s"(dA), [so] "s"(sO), [sg] "s"(sG), [sd] "s"(sD), [ab] "v"(aB), [ac] "v"(aB + 65536u), [aw] "v"(aW), [c1] "s"(c1), [c2] "v"(c2)
      : "memory", "scc", "v16", "v17", "v18", "v19", "v20", "v21", "v22", "v23", "v24", "v25", "v26", "v27", "v28", "v29", "v30", "v31", "v32", "v33", "v34", "v35", "v36", "v37", "v38", "v39", "v40", "v41", "v42", "v43", "v44", "v45", "v46", "v47", "v48", "v49", "v50", "v51", "v52", "v53", "v54", "v55", "v56", "v57", "v58", "v59", "v60", "v61", "v62", "v63", "v64", "v65", "v66", "v67", "v68", "v69", "v70", "v71", "v72", "v73", "v74", "v75", "v76", "v77", "v78", "v79", "v80", "v81", "v82", "v83", "v84", "v85", "v86", "v87", "v88", "v89", "v90", "v91", "v92", "v93", "v94", "v95", "a0", "a1", "a2", "a3", "a4", "a5", "a6", "a7", "a8", "a9", "a10", "a11", "a12", "a13", "a14", "a15", "a16", "a17", "a18", "a19", "a20", "a21", "a22", "a23", "a24", "a25", "a26", "a27", "a28", "a29", "a30", "a31", "a32", "a33", "a34", "a35", "a36", "a37", "a38", "a39", "a40", "a41", "a42", "a43", "a44", "a45", "a46", "a47", "a48", "a49", "a50", "a51", "a52", "a53", "a54", "a55", "a56", "a57", "a58", "a59", "a60", "a61", "a62", "a63", "a64", "a65", "a66", "a67", "a68", "a69", "a70", "a71", "a72", "a73", "a74", "a75", "a76", "a77", "a78", "a79", "a80", "a81", "a82", "a83", "a84", "a85", "a86", "a87", "a88", "a89", "a90", "a91", "a92", "a93", "a94", "a95", "a96", "a97", "a98", "a99", "a100", "a101", "a102", "a103", "a104", "a105", "a106", "a107", "a108", "a109", "a110", "a111", "a112", "a113", "a114", "a115", "a116", "a117", "a118", "a119", "a120", "a121", "a122", "a123", "a124", "a125", "a126", "a127");
    TS(6);
    WAIT_V(0); BAR();
    count_up(srdS, F_S6 + tb, wid == 0u);
  }
  TS(7);
  {
    LANES;
    const unsigned rb = q8 >> 1, r = 8u * (q8 & 1u) + (lane & 7u), g4 = (lane >> 3) & 3u, s = 2u * wid + (lane >> 5);
    const unsigned fo = (rb * 16u + s) * 1024u + (r + 16u * g4) * 16u;
    const unsigned col = 32u * s + 4u * g4;
    f32x4 gb0, gb1;
    asm volatile("global_load_dwordx4 %0, %2, off sc1\n\tglobal_load_dwordx4 %1, %2, off offset:64 sc1" : "=&v"(gb0), "=&v"(gb1) : "v"(mod + 2560 + col) : "memory");
    await(srdS, F_S6 + (q8 >> 3), 7u, wid);
    TS(8);
    u32x4 p[7];
#pragma unroll
    for (int i = 0; i < 7; ++i) asm volatile("buffer_load_dwordx4 %0, %1, %2, %3 offen sc1" : "=v"(p[i]) : "v"(fo), "s"(srdS), "s"((unsigned)WS_PT2 + (unsigned)i * 2097152u) : "memory");
    asm volatile("s_waitcnt vmcnt(0)" : "+v"(p[0]), "+v"(p[1]), "+v"(p[2]), "+v"(p[3]), "+v"(p[4]), "+v"(p[5]), "+v"(p[6]), "+v"(gb0), "+v"(gb1) :: "memory");
    const f32x2 gb[4] = {{gb0[0], gb0[1]}, {gb0[2], gb0[3]}, {gb1[0], gb1[1]}, {gb1[2], gb1[3]}};
    unsigned o[4];
#pragma unroll
    for (int e = 0; e < 4; ++e) {
      f32x2 a = up2(p[0][e]);
#pragma unroll
      for (int i = 1; i < 7; ++i) a += up2(p[i][e]);
      const f32x2 y = up2(x1[e]) + rbf2(gb[e] * rbf2(a));
      o[e] = cvtpk(y[0], y[1]);
    }
    u16* op = out2 + (rb * 16u + r) * 512u + col;
    *(u32x2*)op = u32x2{o[0], o[1]};
    *(u32x2*)(op + 16) = u32x2{o[2], o[3]};
    TS(9);
  }
  }
}

#define GP(T) __attribute__((address_space(1))) T*
__global__ __launch_bounds__(512, 2) void k0_mod(const u16* __restrict__ cvec, const u16* __restrict__ wmod, const u16* __restrict__ ckv,
                                                 const u16* __restrict__ wuk, const u16* __restrict__ wuv, const u16* __restrict__ x, char* __restrict__ ws) {
  asm volatile(".Lk1e:");
  __shared__ __attribute__((aligned(16))) float red[512];
  const int b = blockIdx.x, xcd = b & 7, idx = b >> 3;
  const unsigned tid = threadIdx.x, lane = tid & 63u, wid = rfl(tid >> 6);
  unsigned ctd, pfd;
  {
    unsigned long long pc; unsigned rel, l1, l0;
    asm volatile("s_getpc_b64 %0\n.Lc0%=:\n\ts_mov_b32 %1, .Lc0%=-.Lk1s\n\ts_mov_b32 %2, .Lk1e-.Lk1s\n\ts_mov_b32 %3, .Lk0e-.Lk1s-4" : "=s"(pc), "=s"(rel), "=s"(l1), "=s"(l0));
    const unsigned cp = (tid >= 448u && tid < 464u ? (unsigned)idx * 2048u : l1) + (lane & 15u) * 128u;
    asm volatile("global_load_dword %0, %1, %2" : "=v"(ctd) : "v"(cp < l0 ? cp : l0), "s"(pc - rel) : "memory");
  }
  asm volatile("" : "+s"(cvec), "+s"(wmod), "+s"(ckv), "+s"(wuk), "+s"(wuv), "+s"(x), "+s"(ws));
  RT(6, 0);
#define NT 8192u
  constexpr unsigned NTW = (NT + 25u) / 26u;
  if (idx >= 6) {
    const unsigned n = (unsigned)idx - 6u, l0_ = n * NTW + tid, li = l0_ < NT - 1u ? l0_ : NT - 1u, xl = n * 15u + tid - NTW;
    const char* pf = tid < NTW ? (const char*)(li < 4096u ? wuk : wuv) + ((li & 4095u) >> 3) * 8192u + (unsigned)xcd * 1024u + (li & 7u) * 128u
                                : (const char*)x + (tid < NTW + 15u && xl < 384u ? ((xl >> 6) * 8u + (unsigned)xcd) * 8192u + (xl & 63u) * 128u : (unsigned)b * 8192u + lane * 128u);
    const unsigned e = (n * 8u + (unsigned)xcd) * 2u + wid, b2 = wid < 2u && e < 384u ? e >> 3 : (unsigned)b, w2 = wid < 2u && e < 384u ? e & 7u : wid;
    const unsigned lo = (lane & 7u) * 1024u + (lane >> 3) * 16u, so = (lane >> 3) * 256u + (lane & 7u) * 16u;
    if (b == 255 && tid < 296u) *(GP(unsigned))(ws + WS_FLG + tid * 128u) = 0u;
    u32x4 c0, c1;
    if (wid < 2u) {
      asm volatile("global_load_dwordx4 %0, %3, %5\n\tglobal_load_dwordx4 %1, %4, %5\n\tglobal_load_dword %2, %6, off\n\ts_waitcnt vmcnt(1)"
                   : "=&v"(c0), "=&v"(c1), "=&v"(pfd) : "v"((unsigned)b * 8192u + wid * 128u + lo), "v"(b2 * 8192u + w2 * 128u + lo), "s"(ckv), "v"(pf) : "memory");
      *(GP(u32x4))(ws + WS_CF + (b2 >> 1) * 16384u + w2 * 2048u + (b2 & 1u) * 128u + so) = c1;
    } else
      asm volatile("global_load_dwordx4 %0, %2, %3\n\tglobal_load_dword %1, %4, off\n\ts_waitcnt vmcnt(1)" : "=&v"(c0), "=&v"(pfd) : "v"((unsigned)b * 8192u + wid * 128u + lo), "s"(ckv), "v"(pf) : "memory");
    *(GP(u32x4))(ws + WS_CF + (unsigned)(b >> 1) * 16384u + wid * 2048u + (unsigned)(b & 1) * 128u + so) = c0;
  } else {
    const unsigned mcol = (unsigned)(xcd * 6 + idx) * 64u;
    unsigned cv16; u32x4 wm[8];
    const unsigned wo = mcol * 2u + (wid * 64u + (lane >> 3)) * 6144u + (lane & 7u) * 16u;
    asm volatile("global_load_ushort %0, %9, %10\n\tglobal_load_dwordx4 %1, %11, %19\n\tglobal_load_dwordx4 %2, %12, %19\n\tglobal_load_dwordx4 %3, %13, %19\n\tglobal_load_dwordx4 %4, %14, %19\n\tglobal_load_dwordx4 %5, %15, %19\n\tglobal_load_dwordx4 %6, %16, %19\n\tglobal_load_dwordx4 %7, %17, %19\n\tglobal_load_dwordx4 %8, %18, %19\n\ts_waitcnt vmcnt(4)"
                 : "=&v"(cv16), "=&v"(wm[0]), "=&v"(wm[1]), "=&v"(wm[2]), "=&v"(wm[3]), "=&v"(wm[4]), "=&v"(wm[5]), "=&v"(wm[6]), "=&v"(wm[7])
                 : "v"(tid * 2u), "s"(cvec), "v"(wo), "v"(wo + 49152u), "v"(wo + 98304u), "v"(wo + 147456u), "v"(wo + 196608u), "v"(wo + 245760u), "v"(wo + 294912u), "v"(wo + 344064u), "s"(wmod) : "memory");
    pfd = 0u;
    RT(6, 2);
    RT(6, 3);
    const float cv = bflo(cv16), sl = rbf(cv * __builtin_amdgcn_rcpf(1.f + __expf(-cv)));
    f32x2 a[4];
#pragma unroll
    for (int i = 0; i < 8; ++i) {
      if (i == 4) asm volatile("s_waitcnt vmcnt(0)" : "+v"(wm[4]), "+v"(wm[5]), "+v"(wm[6]), "+v"(wm[7]), "+v"(a[0]) :: "memory");
      const float s = __int_as_float(__builtin_amdgcn_ds_bpermute((int)((lane >> 3) * 4u + i * 32u), __float_as_int(sl)));
#pragma unroll
      for (int e = 0; e < 4; ++e) a[e] = i ? __builtin_elementwise_fma(f32x2{s, s}, up2(wm[i][e]), a[e]) : up2(wm[i][e]) * s;
    }
    float r[4];
#pragma unroll
    for (int e = 0; e < 4; ++e) { auto sw = __builtin_amdgcn_permlane32_swap(__float_as_uint(a[e >> 1][e & 1]), __float_as_uint(a[2 + (e >> 1)][e & 1]), false, false); r[e] = __uint_as_float(sw[0]) + __uint_as_float(sw[1]); }
#pragma unroll
    for (int e = 0; e < 2; ++e) { auto sw = __builtin_amdgcn_permlane16_swap(__float_as_uint(r[e]), __float_as_uint(r[e + 2]), false, false); r[e] = dpp_add<0x128>(__uint_as_float(sw[0]) + __uint_as_float(sw[1])); }
    if ((lane & 8u) == 0) *(f32x2*)(red + wid * 64u + (lane & 7u) * 8u + (lane >> 4) * 2u) = f32x2{r[0], r[1]};
    RT(6, 4);
    __syncthreads();
    RT(6, 5);
    if (tid < 64) { float s = 0.f;
#pragma unroll
      for (int w = 0; w < 8; ++w) s += red[w * 64 + tid];
      constexpr float QS = 0.08838834764831845f * 1.4426950408889634f;
      s = rbf(s);
      ((GP(float))(ws + WS_MOD))[mcol + tid] = mcol < 512u ? s * QS : mcol < 1024u ? rbf(1.f + s) * QS : s; }
  }
  asm volatile("s_waitcnt vmcnt(0)" : "+v"(pfd) :: "memory");
  asm volatile("" :: "v"(ctd));
  RT(6, 1);
  asm volatile(".Lk0e:");
}

extern "C" void kernel_launch(void* const* d_in, const int* in_sizes, int n_in, void* d_out, int out_size, void* d_ws, size_t ws_size, hipStream_t stream) {
  const u16* x = (const u16*)d_in[0]; const u16* ckv = (const u16*)d_in[1]; const u16* wq = (const u16*)d_in[2]; const u16* wuk = (const u16*)d_in[3];
  const u16* wuv = (const u16*)d_in[4]; const u16* wo = (const u16*)d_in[5]; const u16* wg = (const u16*)d_in[7]; const u16* wu = (const u16*)d_in[8];
  const u16* wd = (const u16*)d_in[9]; const u16* c = (const u16*)d_in[10]; const u16* wmod = (const u16*)d_in[11];
  char* ws = (char*)d_ws;
  (void)hipFuncSetAttribute((const void*)k1_attn, hipFuncAttributeMaxDynamicSharedMemorySize, K2_LDS);
  k0_mod<<<256, 512, 0, stream>>>(c, wmod, ckv, wuk, wuv, x, ws);
  k1_attn<<<256, 512, K2_LDS, stream>>>(x, wuk, wuv, wq, wo, wg, wu, wd, (u16*)d_out, ws);
}
```
